```python
import torch

def _softmax(x, axis=-1):
    return torch.softmax(x, dim=axis)
def _gelu(x):
    return torch.nn.functional.gelu(x, approximate="tanh")
def _silu(x):
    return torch.nn.functional.silu(x)
def _sigmoid(x):
    return torch.sigmoid(x)
def _layernorm(x, axis=-1, eps=1e-5):
    m = x.mean(dim=axis, keepdim=True)
    v = x.var(dim=axis, keepdim=True, unbiased=False)
    return (x - m) / torch.sqrt(v + eps)
def _scatter_add(tgt, idx, src, axis=0):
    return tgt.clone().index_add_(axis, idx, src)
def _segment_sum(src, seg, n):
    out = torch.zeros((n,) + src.shape[1:], dtype=src.dtype)
    return out.index_add_(0, seg, src)
def _pad(x, pw):
    flat = tuple(v for pair in reversed(pw) for v in pair)
    return torch.nn.functional.pad(x, flat)

kernel_name = 'sequential_preLN__ssm_conv_s4__glu_gated_reglu__prompt'

def setup_inputs():

    _w = (lambda a: (lambda t: t.to(torch.bfloat16) if t.is_floating_point() else t)(torch.as_tensor(a)))
    def _A_setup():
        B, L, D, N = 1, 2048, 640, 192
        A_re = -(lambda _x: torch.exp(torch.as_tensor(_x)))((lambda *s: torch.randn(*s, dtype=torch.bfloat16) * 3.2)(D, N))
        A_im = torch.pi * (lambda *s: torch.randn(*s, dtype=torch.bfloat16) * 0.05)(D, N)
        B_re = (lambda *s: torch.randn(*s, dtype=torch.bfloat16) * 0.05)(D, N)
        B_im = (lambda *s: torch.randn(*s, dtype=torch.bfloat16) * 0.1)(D, N)
        C_re = (lambda *s: torch.randn(*s, dtype=torch.bfloat16) * 0.1)(D, N)
        C_im = (lambda *s: torch.randn(*s, dtype=torch.bfloat16) * 0.05)(D, N)
        log_dt = (lambda *s: torch.randn(*s, dtype=torch.bfloat16) * 0.05)(D) * 0.5 - 4.0
        Dskip = (lambda *s: torch.randn(*s, dtype=torch.bfloat16) * 0.05)(D)
        return {
            "u": _w((lambda *s: torch.randn(*s, dtype=torch.bfloat16) * 0.05)(B, L, D)),
            "A_re": _w(A_re),
            "A_im": _w(A_im),
            "B_re": _w(B_re),
            "B_im": _w(B_im),
            "C_re": _w(C_re),
            "C_im": _w(C_im),
            "log_dt": _w(log_dt),
            "Dskip": _w(Dskip),
        }
    _A_in = _A_setup()

    def _B_setup():
        B, S, D, F = 1, 2048, 640, 1536
        return {
            "x": _w((lambda *s: torch.randn(*s, dtype=torch.bfloat16) * 0.05)(B, S, D)),
            "W_gate": _w((lambda *s: torch.randn(*s, dtype=torch.bfloat16) * 0.4)(D, F) * (2.0 / D) ** 0.5),
            "W_up": _w((lambda *s: torch.randn(*s, dtype=torch.bfloat16) * 0.4)(D, F) * (2.0 / D) ** 0.5),
            "W_down": _w((lambda *s: torch.randn(*s, dtype=torch.bfloat16) * 0.4)(F, D) * (1.0 / F) ** 0.5),
        }
    _B_in = _B_setup()

    return {
        "A_u": _A_in["u"],
        "A_A_re": _A_in["A_re"],
        "A_A_im": _A_in["A_im"],
        "A_B_re": _A_in["B_re"],
        "A_B_im": _A_in["B_im"],
        "A_C_re": _A_in["C_re"],
        "A_C_im": _A_in["C_im"],
        "A_log_dt": _A_in["log_dt"],
        "A_Dskip": _A_in["Dskip"],
        "B_x": _B_in["x"],
        "B_W_gate": _B_in["W_gate"],
        "B_W_up": _B_in["W_up"],
        "B_W_down": _B_in["W_down"],
    }

def reference(A_u, A_A_re, A_A_im, A_B_re, A_B_im, A_C_re, A_C_im, A_log_dt, A_Dskip, B_x, B_W_gate, B_W_up, B_W_down):
    def _A_ref(u, A_re, A_im, B_re, B_im, C_re, C_im, log_dt, Dskip):
        Bsz, L, D = u.shape
        dt = torch.exp(log_dt)[:, None]
        A = A_re + 1j * A_im
        Bc = B_re + 1j * B_im
        Cc = C_re + 1j * C_im
        logdA = dt * A
        dB = dt * Bc
        l = (lambda x, dt: x.to(dt))(torch.arange(L), A_re.dtype)
        P = torch.exp(logdA[:, :, None] * l[None, None, :])
        K = torch.einsum("dn,dnl,dn->dl", Cc, P, dB).real
        L2 = 2 * L
        Kp = _pad(K, ((0, 0), (0, L)))
        up = (lambda x, *d: x.permute(*d))(u, 0, 2, 1)
        up = _pad(up, ((0, 0), (0, 0), (0, L)))
        Kf = (lambda x, axes=(-2,-1): torch.fft.rfft2(x.float(), dim=axes))(Kp[:, None, :], axes=(-2, -1))
        Uf = (lambda x, axes=(-2,-1): torch.fft.rfft2(x.float(), dim=axes))(up[:, :, None, :], axes=(-2, -1))
        Yf = Uf * Kf[None, :, :, :]
        y = (lambda x, s, axes=(-2,-1): torch.fft.irfft2(x, s=s, dim=axes).to(torch.bfloat16))(Yf, (1, L2), axes=(-2, -1))[:, :, 0, :L]
        y = (lambda x, *d: x.permute(*d))(y, 0, 2, 1) + u * Dskip[None, None, :]
        return y

    def _B_ref(x, W_gate, W_up, W_down):
        gate = torch.einsum("bsd,df->bsf", x, W_gate)
        up = torch.einsum("bsd,df->bsf", x, W_up)
        gate = gate * (lambda x, dt: x.to(dt))(gate > 0, gate.dtype)
        h = gate * up
        return torch.einsum("bsf,fd->bsd", h, W_down)

    x = A_u
    h = x + _A_ref(_layernorm(x, axis=-1), A_A_re, A_A_im, A_B_re, A_B_im, A_C_re, A_C_im, A_log_dt, A_Dskip)
    h2 = h + _B_ref(_layernorm(h, axis=-1), B_W_gate, B_W_up, B_W_down)
    return h2
```

```cpp
#include <hip/hip_runtime.h>
#include <hip/hip_bf16.h>
#include <stdint.h>

typedef unsigned short u16;
constexpr int L = 2048, D = 640, NS = 192, F = 1536;

__device__ __forceinline__ float bf2f(u16 v) { return __uint_as_float(((unsigned)v) << 16); }
__device__ __forceinline__ u16 f2bf(float f) {
  unsigned u = __float_as_uint(f);
  return (u16)((u + 0x7FFF + ((u >> 16) & 1)) >> 16);
}
__device__ __forceinline__ float rbf(float f) { return bf2f(f2bf(f)); }

__global__ void kgen(const u16* Are, const u16* Aim, const u16* Bre, const u16* Bim, const u16* Cre, const u16* Cim,
                     const u16* logdt, float* K) {
  int d = blockIdx.y;
  int l = blockIdx.x * blockDim.x + threadIdx.x;
  if (l >= L) return;
  float lf = rbf((float)l);
  float dt = rbf(expf(bf2f(logdt[d])));
  float acc = 0.f;
  for (int n = 0; n < NS; ++n) {
    float ar = dt * bf2f(Are[d * NS + n]), ai = dt * bf2f(Aim[d * NS + n]);
    float br = dt * bf2f(Bre[d * NS + n]), bi = dt * bf2f(Bim[d * NS + n]);
    float cr = bf2f(Cre[d * NS + n]), ci = bf2f(Cim[d * NS + n]);
    float e = expf(ar * lf);
    float s, c;
    sincosf(ai * lf, &s, &c);
    float pr = e * c, pi = e * s;
    float qr = cr * pr - ci * pi, qi = cr * pi + ci * pr;
    acc += qr * br - qi * bi;
  }
  K[d * L + l] = acc;
}

__global__ void ln1(const u16* x, u16* u) {
  int t = blockIdx.x;
  __shared__ float red[256];
  float s = 0.f;
  for (int d = threadIdx.x; d < D; d += 256) s += bf2f(x[t * D + d]);
  red[threadIdx.x] = s; __syncthreads();
  for (int o = 128; o > 0; o >>= 1) { if (threadIdx.x < o) red[threadIdx.x] += red[threadIdx.x + o]; __syncthreads(); }
  float mean = red[0] / D; __syncthreads();
  float q = 0.f;
  for (int d = threadIdx.x; d < D; d += 256) { float e = bf2f(x[t * D + d]) - mean; q += e * e; }
  red[threadIdx.x] = q; __syncthreads();
  for (int o = 128; o > 0; o >>= 1) { if (threadIdx.x < o) red[threadIdx.x] += red[threadIdx.x + o]; __syncthreads(); }
  float var = red[0] / D;
  float mb = rbf(mean), vb = rbf(var);
  float sd = rbf(sqrtf(rbf(vb + 1e-5f)));
  for (int d = threadIdx.x; d < D; d += 256) u[t * D + d] = f2bf(rbf(bf2f(x[t * D + d]) - mb) / sd);
}

__global__ void conv(const float* K, const u16* u, const u16* x, const u16* Dskip, u16* h) {
  int d = blockIdx.y;
  int t = blockIdx.x * blockDim.x + threadIdx.x;
  float acc = 0.f;
  for (int s = 0; s <= t; ++s) acc += K[d * L + (t - s)] * bf2f(u[s * D + d]);
  float y = rbf(acc);
  float y2 = rbf(y + rbf(bf2f(u[t * D + d]) * bf2f(Dskip[d])));
  h[t * D + d] = f2bf(bf2f(x[t * D + d]) + y2);
}

__global__ void gemm1(const u16* a, const u16* Wg, const u16* Wu, u16* hm) {
  int t = blockIdx.y;
  int f = blockIdx.x * blockDim.x + threadIdx.x;
  float g = 0.f, uu = 0.f;
  for (int d = 0; d < D; ++d) { float av = bf2f(a[t * D + d]); g += av * bf2f(Wg[d * F + f]); uu += av * bf2f(Wu[d * F + f]); }
  float gb = rbf(g), ub = rbf(uu);
  gb = gb * (gb > 0.f ? 1.f : 0.f);
  hm[t * F + f] = f2bf(gb * ub);
}

__global__ void gemm2(const u16* hm, const u16* Wd, const u16* h, u16* out) {
  int t = blockIdx.y;
  int d = blockIdx.x * blockDim.x + threadIdx.x;
  float o = 0.f;
  for (int f = 0; f < F; ++f) o += bf2f(hm[t * F + f]) * bf2f(Wd[f * D + d]);
  out[t * D + d] = f2bf(bf2f(h[t * D + d]) + rbf(o));
}

extern "C" void kernel_launch(void* const* d_in, const int* in_sizes, int n_in, void* d_out, int out_size, void* d_ws,
                              size_t ws_size, hipStream_t stream) {
  const u16* x = (const u16*)d_in[0];
  char* ws = (char*)d_ws;
  float* K = (float*)ws;
  u16* u = (u16*)(ws + (8 << 20));
  u16* h = (u16*)(ws + (12 << 20));
  u16* a2 = (u16*)(ws + (16 << 20));
  u16* hm = (u16*)(ws + (20 << 20));
  kgen<<<dim3(L / 256, D), 256, 0, stream>>>((const u16*)d_in[1], (const u16*)d_in[2], (const u16*)d_in[3], (const u16*)d_in[4],
                                             (const u16*)d_in[5], (const u16*)d_in[6], (const u16*)d_in[7], K);
  ln1<<<L, 256, 0, stream>>>(x, u);
  conv<<<dim3(L / 256, D), 256, 0, stream>>>(K, u, x, (const u16*)d_in[8], h);
  ln1<<<L, 256, 0, stream>>>(h, a2);
  gemm1<<<dim3(F / 256, L), 256, 0, stream>>>(a2, (const u16*)d_in[10], (const u16*)d_in[11], hm);
  gemm2<<<dim3(D / 128, L), 128, 0, stream>>>(hm, (const u16*)d_in[12], h, (u16*)d_out);
}
```

```cpp
#include <hip/hip_runtime.h>
#include <hip/hip_bf16.h>
#include <stdint.h>

typedef unsigned short u16;
typedef unsigned u32;
typedef unsigned long long u64;
typedef short s16x4 __attribute__((ext_vector_type(4)));
typedef short s16x8 __attribute__((ext_vector_type(8)));
typedef __bf16 bf16x8 __attribute__((ext_vector_type(8)));
typedef __bf16 bf16x2 __attribute__((ext_vector_type(2)));
typedef float f32x4 __attribute__((ext_vector_type(4)));
typedef float f32x2 __attribute__((ext_vector_type(2)));
typedef u32 u32x4 __attribute__((ext_vector_type(4)));
typedef u32 u32x2 __attribute__((ext_vector_type(2)));
#define LAS __attribute__((address_space(3)))

constexpr int L = 2048, D = 640, NS = 192, F = 1536;

constexpr size_t WS_UT = 0;
constexpr size_t WS_XT = 4u << 20;
constexpr size_t WS_HT = 8u << 20;
constexpr size_t WS_HM = 12u << 20;
constexpr size_t WS_CS = 20u << 20;
constexpr size_t WS_K  = 24u << 20;
constexpr size_t WS_W1 = 32u << 20;
constexpr size_t WS_WD = 36u << 20;
constexpr size_t WS_R2 = 40u << 20;

__device__ __forceinline__ float bf2f(u16 v) { return __uint_as_float(((u32)v) << 16); }
__device__ __forceinline__ u32 pack_bf16(float lo, float hi) {
  bf16x2 r = __builtin_convertvector((f32x2){lo, hi}, bf16x2);
  return __builtin_bit_cast(u32, r);
}
__device__ __forceinline__ float rbf(float f) { return __uint_as_float(pack_bf16(f, f) << 16); }
__device__ __forceinline__ u16 f2bf(float f) { return (u16)pack_bf16(f, f); }
__device__ __forceinline__ float lo_bf(u32 v) { return __uint_as_float(v << 16); }
__device__ __forceinline__ float hi_bf(u32 v) { return __uint_as_float(v & 0xffff0000u); }

#define STAMP(buf, slot, i)
#define WAIT_VM(n) asm volatile("s_waitcnt vmcnt(%0)" ::"n"(n) : "memory")
#define WAIT_LGKM0() do { asm volatile("" ::: "memory"); __builtin_amdgcn_s_waitcnt(0xC07F); asm volatile("" ::: "memory"); } while (0)
#define BARRIER() do { asm volatile("" ::: "memory"); __builtin_amdgcn_s_barrier(); asm volatile("" ::: "memory"); } while (0)

#define SB() __builtin_amdgcn_sched_barrier(0)
#define G1_SCHED() do { _Pragma("unroll") for (int k_ = 0; k_ < 20; ++k_) { __builtin_amdgcn_sched_group_barrier(0x100, 1, 0); __builtin_amdgcn_sched_group_barrier(0x008, 1, 0); } \
  __builtin_amdgcn_sched_group_barrier(0x008, 4, 0); } while (0)
#define G2_SCHED() do { _Pragma("unroll") for (int k_ = 0; k_ < 10; ++k_) { __builtin_amdgcn_sched_group_barrier(0x100, 2, 0); __builtin_amdgcn_sched_group_barrier(0x008, 1, 0); } \
  __builtin_amdgcn_sched_group_barrier(0x100, 4, 0); __builtin_amdgcn_sched_group_barrier(0x008, 10, 0); } while (0)
__device__ __forceinline__ void glds16(const void* gsrc, u32 lds_dst) {
  u32 keep;
  asm volatile("s_mov_b32 %0, m0\n\ts_mov_b32 m0, %2\n\ts_nop 0\n\tglobal_load_lds_dwordx4 %1, off\n\ts_mov_b32 m0, %0"
               : "=&s"(keep) : "v"(gsrc), "s"(lds_dst) : "memory");
}
__device__ __forceinline__ void l2touch(const void* p, u32& sink) {
  asm volatile("global_load_dword %0, %1, off" : "+v"(sink) : "v"(p) : "memory");
}
__device__ __forceinline__ s16x4 tr_read(u32 addr) {
  return __builtin_amdgcn_ds_read_tr16_b64_v4i16((LAS s16x4*)addr);
}
__device__ __forceinline__ bf16x8 tr_frag(u32 a0, u32 a1) {
  s16x4 a = tr_read(a0), b = tr_read(a1);
  return __builtin_bit_cast(bf16x8, __builtin_shufflevector(a, b, 0, 1, 2, 3, 4, 5, 6, 7));
}
__device__ __forceinline__ f32x4 mfma16(bf16x8 a, bf16x8 b, f32x4 c) { return __builtin_amdgcn_mfma_f32_16x16x32_bf16(a, b, c, 0, 0, 0); }

typedef _Float16 f16x2 __attribute__((ext_vector_type(2)));
typedef _Float16 f16x8 __attribute__((ext_vector_type(8)));
typedef float f32x16 __attribute__((ext_vector_type(16)));
__device__ __forceinline__ u32 pack_f16(float lo, float hi) { return __builtin_bit_cast(u32, __builtin_convertvector((f32x2){lo, hi}, f16x2)); }
__device__ __forceinline__ float wave_sum(float v) {
#pragma unroll
  for (int o = 32; o > 0; o >>= 1) v += __shfl_xor(v, o);
  return v;
}
constexpr int K1_RS = 1344;
__global__ __launch_bounds__(256) void k1_ln1(const u16* __restrict__ x, u16* __restrict__ uT, u16* __restrict__ xT) {
  __shared__ __attribute__((aligned(16))) char sm[2 * 8 * K1_RS];
  const u32 lds0 = (u32)(size_t)(LAS char*)sm;
  const int tid = threadIdx.x, lane = tid & 63, wave = tid >> 6;
  const int t0 = (blockIdx.x & 7) * 256 + (blockIdx.x >> 3) * 8;
  u32 v[2][5];
#pragma unroll
  for (int r = 0; r < 2; ++r)
#pragma unroll
    for (int i = 0; i < 5; ++i) v[r][i] = ((const u32*)x)[(size_t)(t0 + 2 * wave + r) * 320 + i * 64 + lane];
#pragma unroll
  for (int r = 0; r < 2; ++r) {
    float s = 0.f;
#pragma unroll
    for (int i = 0; i < 5; ++i) s += lo_bf(v[r][i]) + hi_bf(v[r][i]);
    const float mean = wave_sum(s) * (1.f / D);
    float qv = 0.f;
#pragma unroll
    for (int i = 0; i < 5; ++i) { const float a = lo_bf(v[r][i]) - mean, b = hi_bf(v[r][i]) - mean; qv += a * a + b * b; }
    const float var = wave_sum(qv) * (1.f / D);
    const float mb = rbf(mean), rinv = 1.f / rbf(sqrtf(rbf(rbf(var) + 1e-5f)));
    const u32 row = lds0 + (2 * wave + r) * K1_RS + lane * 4;
#pragma unroll
    for (int i = 0; i < 5; ++i) {
      const u32 a = pack_bf16(lo_bf(v[r][i]) - mb, hi_bf(v[r][i]) - mb);
      const u32 u = pack_bf16(lo_bf(a) * rinv, hi_bf(a) * rinv);
      *(LAS u32*)(row + i * 256) = pack_f16(lo_bf(u), hi_bf(u));
      *(LAS u32*)(row + 8 * K1_RS + i * 256) = v[r][i];
    }
  }
  __syncthreads();
  const int g = lane >> 4, q = (lane & 15) >> 2, p = lane & 3;
#pragma unroll
  for (int k = 0; k < 5; ++k) {
    const int task = wave * 5 + k, arr = task / 10, grp = task % 10;
    const u32 a = lds0 + arr * 8 * K1_RS + q * K1_RS + (64 * grp + 16 * g + 4 * p) * 2;
    s16x4 lo = tr_read(a), hi = tr_read(a + 4 * K1_RS);
    s16x8 o = __builtin_shufflevector(lo, hi, 0, 1, 2, 3, 4, 5, 6, 7);
    *(s16x8*)((char*)(arr ? xT : uT) + (size_t)(64 * grp + lane) * 4096 + t0 * 2) = o;
  }
}

constexpr int C_ROW = 272;
constexpr int C_SCR = 60 * C_ROW;
constexpr int C_OFF_U = 4 * C_SCR;
constexpr int C_UCP = 4160, C_UCH = 4 * C_UCP;
constexpr int C_OFF_K = C_OFF_U + 4 * C_UCH;
constexpr int C_KCH = 4608;
constexpr int C_OFF_S = C_OFF_K + 4 * C_KCH;
constexpr int K2_LDS = C_OFF_S + 64;

struct cplx { f32x2 v, r; };
__device__ __forceinline__ cplx mk(f32x2 v) { return cplx{v, (f32x2){-v.y, v.x}}; }
__device__ __forceinline__ f32x2 cmul(f32x2 a, const cplx& w) {
  f32x2 t = (f32x2){a.x, a.x} * w.v;
  return __builtin_elementwise_fma((f32x2){a.y, a.y}, w.r, t);
}
__device__ __forceinline__ f32x2 cexp(float zr, float zi, float l, float sgn) {
  const float m = __builtin_amdgcn_exp2f(zr * l * 1.44269504088896341f);
  const float a = __builtin_amdgcn_fractf(zi * l * 0.15915494309189535f);
  return (f32x2){m * __builtin_amdgcn_cosf(a), sgn * m * __builtin_amdgcn_sinf(a)};
}
__device__ __forceinline__ u32 pkh(f32x2 v) { return __builtin_bit_cast(u32, __builtin_convertvector(v, f16x2)); }
#define WR4(base, off, a, b, c, d)                                                                              \
  asm volatile("s_mov_b32 m0, %0\n\ts_nop 0\n\tds_write_addtid_b32 %1 offset:%5\n\tds_write_addtid_b32 %2 offset:%5+272\n\t" \
               "ds_write_addtid_b32 %3 offset:%5+544\n\tds_write_addtid_b32 %4 offset:%5+816"                    \
               ::"s"(base), "v"(a), "v"(b), "v"(c), "v"(d), "n"(off) : "memory")

__global__ __launch_bounds__(512, 2) void k2_conv(const u16* __restrict__ Are, const u16* __restrict__ Aim, const u16* __restrict__ Bre,
                                                  const u16* __restrict__ Bim, const u16* __restrict__ Cre, const u16* __restrict__ Cim,
                                                  const u16* __restrict__ logdt, const u16* __restrict__ Dskip,
                                                  const u16* __restrict__ uT, const u16* __restrict__ xT, u16* __restrict__ hT,
                                                  const u16* __restrict__ Wg, const u16* __restrict__ Wu, const u16* __restrict__ Wd,
                                                  u16* __restrict__ W1t, u16* __restrict__ Wdt, float* __restrict__ kdbg, u64* stp) {
  STAMP(stp,0,0);
  extern __shared__ __attribute__((aligned(16))) char smem[];
  const u32 lds0 = (u32)(size_t)(LAS char*)smem;
  const int tid = threadIdx.x, lane = tid & 63;
  const int wave = __builtin_amdgcn_readfirstlane(tid >> 6);
  const int xcd = blockIdx.x & 7, idx = blockIdx.x >> 3;
  if (idx >= 20) {
    const int hid = xcd * 12 + (idx - 20);
    const int g = lane >> 4, q = (lane & 15) >> 2, p = lane & 3;
    if (hid < 48) {
      const u16* W = hid < 24 ? Wg : Wu;
      const int c0 = (hid % 24) * 64;
      for (int j = wave; j < 80; j += 8) {
        const int row = 8 * j + (lane >> 3), ch = (lane & 7) ^ ((row & 3) << 1);
        glds16((const char*)W + (size_t)row * 3072 + c0 * 2 + ch * 16, __builtin_amdgcn_readfirstlane(lds0 + j * 1024));
      }
      WAIT_VM(0);
      __syncthreads();
      {
        const int rg = tid >> 3, ch = tid & 7;
        float s[8] = {0, 0, 0, 0, 0, 0, 0, 0};
#pragma unroll
        for (int r = 0; r < 10; ++r) {
          const int row = rg * 10 + r;
          const u32x4 vv = *(LAS u32x4*)(lds0 + row * 128 + ((ch ^ ((row & 3) << 1)) * 16));
#pragma unroll
          for (int e = 0; e < 4; ++e) { s[2 * e] += lo_bf(vv[e]); s[2 * e + 1] += hi_bf(vv[e]); }
        }
#pragma unroll
        for (int e = 0; e < 8; ++e) {
          s[e] += __shfl_xor(s[e], 8); s[e] += __shfl_xor(s[e], 16); s[e] += __shfl_xor(s[e], 32);
        }
        if (lane < 8) {
#pragma unroll
          for (int e = 0; e < 8; ++e) *(LAS float*)(lds0 + 81920 + (wave * 64 + lane * 8 + e) * 4) = s[e];
        }
      }
      __syncthreads();
      for (int fr = wave; fr < 80; fr += 8) {
        const int ks = fr >> 2, tl = fr & 3;
        float cm = 0.f;
#pragma unroll
        for (int w = 0; w < 8; ++w) cm += *(LAS float*)(lds0 + 81920 + (w * 64 + tl * 16 + (lane & 15)) * 4);
        cm *= (1.f / D);
        const int row = 32 * ks + 8 * g + q;
        const u32 a = lds0 + row * 128 + (((2 * tl + (p >> 1)) ^ (q << 1)) * 16) + 8 * (p & 1);
        const s16x4 lo = tr_read(a), hi = tr_read(a + 512);
        u32x4 o;
        o[0] = pack_bf16(bf2f((u16)lo[0]) - cm, bf2f((u16)lo[1]) - cm);
        o[1] = pack_bf16(bf2f((u16)lo[2]) - cm, bf2f((u16)lo[3]) - cm);
        o[2] = pack_bf16(bf2f((u16)hi[0]) - cm, bf2f((u16)hi[1]) - cm);
        o[3] = pack_bf16(bf2f((u16)hi[2]) - cm, bf2f((u16)hi[3]) - cm);
        const int f = c0 + 16 * tl, pn = f / 96, fi = (f % 96) / 16;
        const int slot = (fi / 3) * 6 + (hid < 24 ? 0 : 3) + fi % 3;
        *(u32x4*)((char*)W1t + (((size_t)pn * 20 + ks) * 12 + slot) * 1024 + lane * 16) = o;
      }
    } else {
      const int ks = hid - 48;
      for (int j = wave; j < 40; j += 8) {
        const int P = 64 * j + lane, row = P / 80, ch = P % 80;
        glds16((const char*)Wd + (size_t)(32 * ks + row) * 1280 + ch * 16, __builtin_amdgcn_readfirstlane(lds0 + j * 1024));
      }
      WAIT_VM(0);
      __syncthreads();
      for (int fr = wave; fr < 40; fr += 8) {
        const u32 a = lds0 + (8 * g + q) * 1280 + (16 * fr + 4 * p) * 2;
        const s16x4 lo = tr_read(a), hi = tr_read(a + 4 * 1280);
        const s16x8 o = __builtin_shufflevector(lo, hi, 0, 1, 2, 3, 4, 5, 6, 7);
        *(s16x8*)((char*)Wdt + (((size_t)(fr / 5) * 48 + ks) * 5 + fr % 5) * 1024 + lane * 16) = o;
      }
    }
    return;
  }
  const int ch = wave & 3, d = (xcd * 20 + idx) * 4 + ch;
  if (wave < 4) {
    float zr[3], zi[3]; f32x2 cb[3];
    const float dt = rbf(__expf(bf2f(logdt[d])));
    float bound = 0.f;
#pragma unroll
    for (int r = 0; r < 3; ++r) {
      const int o = d * NS + r * 64 + lane;
      zr[r] = dt * bf2f(Are[o]); zi[r] = dt * bf2f(Aim[o]);
      const float br = dt * bf2f(Bre[o]), bi = dt * bf2f(Bim[o]), cr = bf2f(Cre[o]), ci = bf2f(Cim[o]);
      cb[r] = (f32x2){cr * br - ci * bi, -(cr * bi + ci * br)};
      const float grow = __builtin_amdgcn_exp2f(fmaxf(zr[r], 0.f) * (2064.f * 1.44269504f));
      bound = fmaxf(bound, (fabsf(cb[r].x) + fabsf(cb[r].y)) * grow);
    }
#pragma unroll
    for (int o = 32; o > 0; o >>= 1) bound = fmaxf(bound, __shfl_xor(bound, o));
    int ex = (int)((__float_as_uint(bound) >> 23) & 255);
    ex = ex < 20 ? 20 : (ex > 240 ? 240 : ex);
    const float scale = __uint_as_float((u32)(267 - ex) << 23);
    const float inv_tot = __uint_as_float((u32)(ex - 5) << 23);
    if (lane == 0) *(LAS float*)(lds0 + C_OFF_S + ch * 4) = inv_tot;
    const u32 sb = __builtin_amdgcn_readfirstlane(lds0 + ch * C_SCR);
    const int r32 = lane & 31, h32 = lane >> 5;
    u32 arow;
    {
      const int hr = (r32 >> 2) & 1, rho = (r32 & 3) + 4 * (r32 >> 3);
      int row = rho <= 8 ? rho + 8 * hr : rho <= 11 ? rho + 8 + 2 * hr : rho <= 13 ? rho + 10 + hr : rho + 11 + hr;
      arow = sb + row * C_ROW + h32 * 16;
    }
    const u32 brow = sb + (28 + r32) * C_ROW + h32 * 16;
    f32x16 acc;
#pragma unroll
    for (int i = 0; i < 16; ++i) acc[i] = 0.f;
#pragma unroll 1
    for (int r = 0; r < 3; ++r) {
      const float a_r = r == 0 ? zr[0] : r == 1 ? zr[1] : zr[2];
      const float a_i = r == 0 ? zi[0] : r == 1 ? zi[1] : zi[2];
      const f32x2 c0 = (r == 0 ? +cb[0] : r == 1 ? +cb[1] : +cb[2]) * scale;
      const cplx w1 = mk(cexp(a_r, a_i, 1.f, -1.f));
      const cplx w2 = mk(cmul(w1.v, w1));
      const cplx w512 = mk(cexp(a_r, a_i, 512.f, -1.f));
      const cplx w1024 = mk(cmul(w512.v, w512));
      const cplx w1536 = mk(cmul(w1024.v, w512));
      f32x2 P[17];
      P[0] = c0; P[1] = cmul(c0, w1);
#pragma unroll
      for (int c = 2; c < 17; ++c) P[c] = cmul(P[c - 2], w2);
      WR4(sb, 0 * C_ROW, pkh(P[0]), pkh(P[1]), pkh(P[2]), pkh(P[3]));
      WR4(sb, 4 * C_ROW, pkh(P[4]), pkh(P[5]), pkh(P[6]), pkh(P[7]));
      WR4(sb, 8 * C_ROW, pkh(P[8]), pkh(P[9]), pkh(P[10]), pkh(P[11]));
      WR4(sb, 12 * C_ROW, pkh(P[12]), pkh(P[13]), pkh(P[14]), pkh(P[15]));
      WR4(sb, 16 * C_ROW, pkh(P[16]), pkh(cmul(P[0], w512)), pkh(cmul(P[4], w512)), pkh(cmul(P[8], w512)));
      WR4(sb, 20 * C_ROW, pkh(cmul(P[12], w512)), pkh(cmul(P[16], w512)), pkh(cmul(P[0], w1024)), pkh(cmul(P[8], w1024)));
      WR4(sb, 24 * C_ROW, pkh(cmul(P[16], w1024)), pkh(cmul(P[0], w1536)), pkh(cmul(P[8], w1536)), pkh(cmul(P[16], w1536)));
      const cplx w16 = mk(cexp(a_r, a_i, 16.f, 1.f));
      f32x2 E0 = {1.f, 0.f}, E1 = w16.v, E2 = cmul(E1, w16), E3 = cmul(E2, w16);
      const cplx w64 = mk(cmul(E2, mk(E2)));
      WR4(sb, 28 * C_ROW, pkh(E0), pkh(E1), pkh(E2), pkh(E3));
#pragma unroll
      for (int b = 1; b < 8; ++b) {
        E0 = cmul(E0, w64); E1 = cmul(E1, w64); E2 = cmul(E2, w64); E3 = cmul(E3, w64);
        WR4(sb, (28 + 4 * b) * C_ROW, pkh(E0), pkh(E1), pkh(E2), pkh(E3));
      }
#pragma unroll
      for (int k = 0; k < 8; ++k) {
        const f16x8 a = *(LAS f16x8*)(arow + k * 32), b = *(LAS f16x8*)(brow + k * 32);
        acc = __builtin_amdgcn_mfma_f32_32x32x16_f16(a, b, acc, 0, 0, 0);
      }
    }
    float v[16];
#pragma unroll
    for (int i = 0; i < 16; ++i) v[i] = acc[i] * (1.f / 256.f);
    const bool far = r32 >= 16;
    const u32 kb = lds0 + C_OFF_K + ch * C_KCH + 4080 - 32 * r32 - 16 * h32;
    auto put = [&](int base, float e0, float e1, float e2, float e3, float e4, float e5, float e6, float e7) {
      u32x4 o = {pack_f16(e7, e6), pack_f16(e5, e4), pack_f16(e3, e2), pack_f16(e1, e0)};
      *(LAS u32x4*)(kb - 2 * base) = o;
    };
    put(0, v[0], far ? v[0] : v[1], v[2], far ? v[4] : v[3], v[4], far ? v[4] : v[5], v[6], far ? v[8] : v[7]);
    put(512, v[9], v[9], v[9], v[10], v[10], v[10], v[11], v[11]);
    put(1024, v[12], v[12], v[12], v[12], h32 ? v[13] : v[12], v[13], v[13], v[13]);
    put(1536, v[14], v[14], v[14], v[14], h32 ? v[15] : v[14], v[15], v[15], v[15]);
    if (lane < 30) *(LAS u32x4*)(lds0 + C_OFF_K + ch * C_KCH + 4096 + lane * 16) = (u32x4){0u, 0u, 0u, 0u};
  } else {
    const char* up = (const char*)uT + (size_t)d * 4096;
    const u32 ub = lds0 + C_OFF_U + ch * C_UCH;
#pragma unroll
    for (int it = 0; it < 4; ++it) {
      const int k = it * 64 + lane;
      const u32x4 dv = *(const u32x4*)(up + k * 16);
      u32x2 nv = {0u, 0u};
      if (k < 255) nv = *(const u32x2*)(up + k * 16 + 16);
      const u32 e0 = __builtin_amdgcn_alignbit(dv[1], dv[0], 16), e1 = __builtin_amdgcn_alignbit(dv[2], dv[1], 16),
                e2 = __builtin_amdgcn_alignbit(dv[3], dv[2], 16), e3 = __builtin_amdgcn_alignbit(nv[0], dv[3], 16),
                e4 = __builtin_amdgcn_alignbit(nv[1], nv[0], 16);
      const u32 a = ub + (k + 2) * 16;
      *(LAS u32x4*)(a) = dv;
      *(LAS u32x4*)(a + C_UCP) = (u32x4){e0, e1, e2, e3};
      *(LAS u32x4*)(a + 2 * C_UCP) = (u32x4){dv[1], dv[2], dv[3], nv[0]};
      *(LAS u32x4*)(a + 3 * C_UCP) = (u32x4){e1, e2, e3, e4};
    }
    if (lane < 8) {
      const int c = lane & 3, Y = lane >> 2;
      const u32x2 f = *(const u32x2*)up;
      u32x4 o = {0u, 0u, 0u, 0u};
      if (Y == 1) {
        if (c == 1) o[3] = f[0] << 16;
        if (c == 2) o[3] = f[0];
        if (c == 3) { o[2] = f[0] << 16; o[3] = __builtin_amdgcn_alignbit(f[1], f[0], 16); }
      }
      *(LAS u32x4*)(ub + c * C_UCP + Y * 16) = o;
    }
  }
  STAMP(stp,0,1);
  __syncthreads();
  STAMP(stp,0,2);
  const int hf = wave >> 2, j = lane & 15, g = lane >> 4;
  const int cc = (1 + j) & 3;
  u32 aa = lds0 + C_OFF_U + ch * C_UCH + cc * C_UCP + 2 * (1 + j + 8 * g - cc) + hf * 256;
  u32 ba = lds0 + C_OFF_K + ch * C_KCH + 32 * j + 16 * g + hf * 256;
  f32x4 acc[8];
#pragma unroll
  for (int i = 0; i < 8; ++i) acc[i] = (f32x4){0.f, 0.f, 0.f, 0.f};
#pragma unroll 1
  for (int r = 0; r < 4; ++r) {
    f16x8 A[8], B[8];
#pragma unroll
    for (int m = 0; m < 8; ++m) {
      const u32x2 lo = *(LAS u32x2*)(aa + 512 * m), hi = *(LAS u32x2*)(aa + 512 * m + 8);
      A[m] = __builtin_bit_cast(f16x8, (u32x4){lo[0], lo[1], hi[0], hi[1]});
      B[m] = *(LAS f16x8*)(ba + 512 * (7 - m));
    }
#pragma unroll
    for (int s = 0; s < 8; ++s)
#pragma unroll
      for (int m = 0; m <= s; ++m) acc[s] = __builtin_amdgcn_mfma_f32_16x16x32_f16(A[m], B[s - m], acc[s], 0, 0, 0);
    aa += 64; ba += 64;
  }
  STAMP(stp,0,3);
  u32x2 xv[4], uv[4];
#pragma unroll
  for (int i = 0; i < 4; ++i) {
    const int t = 16 * (16 * (4 * hf + i) + 15 - j) + 4 * g;
    xv[i] = *(const u32x2*)((const char*)xT + (size_t)d * 4096 + t * 2);
    uv[i] = *(const u32x2*)((const char*)uT + (size_t)d * 4096 + t * 2);
  }
  const float dsk = bf2f(Dskip[d]);
  const u32 ex = lds0 + wave * 4096 + lane * 16, exr = lds0 + (wave ^ 4) * 4096 + lane * 16;
  if (hf == 0) {
#pragma unroll
    for (int i = 0; i < 4; ++i) *(LAS f32x4*)(ex + i * 1024) = acc[4 + i];
  } else {
#pragma unroll
    for (int i = 0; i < 4; ++i) *(LAS f32x4*)(ex + i * 1024) = acc[i];
  }
  __syncthreads();
  const float inv_tot = *(LAS float*)(lds0 + C_OFF_S + ch * 4);
#pragma unroll
  for (int i = 0; i < 4; ++i) {
    const f32x4 o = *(LAS f32x4*)(exr + i * 1024);
    const f32x4 a0 = acc[i], a1 = acc[4 + i];
    const f32x4 y = ((hf ? +a1 : +a0) + o) * inv_tot;
    float hv[4];
#pragma unroll
    for (int e = 0; e < 4; ++e) {
      const u32 uw = uv[i][e >> 1], xw = xv[i][e >> 1];
      const float u = (float)__builtin_bit_cast(_Float16, (u16)((e & 1) ? uw >> 16 : uw));
      const float xx = (e & 1) ? hi_bf(xw) : lo_bf(xw);
      hv[e] = xx + rbf(rbf(y[e]) + rbf(u * dsk));
    }
    const int t = 16 * (16 * (4 * hf + i) + 15 - j) + 4 * g;
    *(u32x2*)((char*)hT + (size_t)d * 4096 + t * 2) = (u32x2){pack_bf16(hv[0], hv[1]), pack_bf16(hv[2], hv[3])};
  }
  STAMP(stp,0,4);
}

constexpr int G1_STG = 20480, G1_NS = 7, G1_NK = 20;
constexpr int G1_LDS = G1_STG * G1_NS;

__global__ __launch_bounds__(512, 2) void k3_gemm1(const u16* __restrict__ hT, const u16* __restrict__ W1t,
                                                   u16* __restrict__ hmid, float* __restrict__ part, u64* stp) {
  STAMP(stp,0,0);
  extern __shared__ __attribute__((aligned(16))) char smem[];
  const u32 lds0 = (u32)(size_t)(LAS char*)smem;
  const int tid = threadIdx.x, lane = tid & 63;
  const int wave = __builtin_amdgcn_readfirstlane(tid >> 6);
  const int xcd = blockIdx.x & 7, idx = blockIdx.x >> 3;
  const int pm = (xcd & 1) * 8 + (idx & 7), pn = (xcd >> 1) * 4 + (idx >> 3);
  const int t0 = pm * 128;
  if (wave >= 4) {
    const int lw = wave - 4;
    const int row = 8 * lw + (lane >> 4);
    const int ch = (lane & 15) ^ (((row & 3) << 1) | (((row >> 3) & 1) << 3));
    const char* srcH = (const char*)hT + (size_t)row * 4096 + t0 * 2 + ch * 16;
    const char* srcW = (const char*)W1t + (size_t)pn * 245760 + lw * 3072 + lane * 16;
    const u32 dH = lds0 + lw * 2048, dW = lds0 + 8192 + lw * 3072;
    auto issue = [&](u32 so) {
      glds16(srcH, dH + so);
      glds16(srcW, dW + so);
      glds16(srcH + 4 * 4096, dH + so + 1024);
      glds16(srcW + 1024, dW + so + 1024);
      glds16(srcW + 2048, dW + so + 2048);
      srcH += 32 * 4096;
      srcW += 12288;
    };
    issue(0); issue(G1_STG);
    WAIT_VM(5); BARRIER();
    issue(2 * G1_STG); issue(3 * G1_STG);
    WAIT_VM(10); BARRIER();
    issue(4 * G1_STG); issue(5 * G1_STG);
    WAIT_VM(15); BARRIER();
    issue(6 * G1_STG); issue(0);
    WAIT_VM(20); BARRIER();
    u32 so = G1_STG;
    issue(so); so += G1_STG;
    issue(so); so += G1_STG;
    for (int i = 4; i < G1_NK - 6; ++i) {
      WAIT_VM(25); BARRIER();
      issue(so);
      so = so == (G1_NS - 1) * G1_STG ? 0 : so + G1_STG;
    }
    WAIT_VM(25); BARRIER();
    WAIT_VM(20); BARRIER(); WAIT_VM(15); BARRIER(); WAIT_VM(10); BARRIER(); WAIT_VM(5); BARRIER(); WAIT_VM(0); BARRIER();
    return;
  }
  const int wt = wave & 1, wf = wave >> 1;
  const int g = lane >> 4, q = (lane & 15) >> 2, p = lane & 3;
  const int jt = pn >> 1, rot = jt & 3;
  const bool dostat = wt == (jt >> 2);
  const u32 hrow = (8 * g + q) * 256 + 8 * (p & 1);
  const u32 hx = q | ((g & 1) << 2);
  u32 ha[4];
#pragma unroll
  for (int j = 0; j < 4; ++j) ha[j] = lds0 + hrow + ((((u32)(4 * wt + ((j + rot) & 3)) ^ hx) << 1) | (p >> 1)) * 16;
  const u32 wa = lds0 + 8192 + wf * 6144 + lane * 16;
  f32x4 acc[6][4];
#pragma unroll
  for (int i = 0; i < 6; ++i)
#pragma unroll
    for (int j = 0; j < 4; ++j) acc[i][j] = (f32x4){0.f, 0.f, 0.f, 0.f};
  f32x4 st1 = {0.f, 0.f, 0.f, 0.f}, st2 = st1;
  const bf16x8 ones = __builtin_bit_cast(bf16x8, (u32x4){0x3f803f80u, 0x3f803f80u, 0x3f803f80u, 0x3f803f80u});
  bf16x8 hf[2][4], wfr[2][6];
  auto rd = [&](int set, u32 so) {
#pragma unroll
    for (int j = 0; j < 4; ++j) hf[set][j] = tr_frag(ha[j] + so, ha[j] + so + 1024);
#pragma unroll
    for (int i = 0; i < 6; ++i) wfr[set][i] = *(LAS bf16x8*)(wa + so + i * 1024);
  };
  auto mma = [&](int set) {
#pragma unroll
    for (int i = 0; i < 6; ++i)
#pragma unroll
      for (int j = 0; j < 4; ++j) acc[i][j] = mfma16(wfr[set][i], hf[set][j], acc[i][j]);
  };
  auto stat = [&](int set, int kstep) {
    if (dostat && set == wf && (kstep >= 10) == (bool)(pn & 1)) {
      st1 = mfma16(ones, hf[set][0], st1);
      st2 = mfma16(hf[set][0], hf[set][0], st2);
    }
  };
#define G1_SCHED3() do { _Pragma("unroll") for (int k_ = 0; k_ < 14; ++k_) { __builtin_amdgcn_sched_group_barrier(0x008, 1, 0); __builtin_amdgcn_sched_group_barrier(0x100, 1, 0); } \
  __builtin_amdgcn_sched_group_barrier(0x008, 10, 0); } while (0)
  BARRIER();
  STAMP(stp,0,1);
  rd(0, 0);
  WAIT_LGKM0();
  u32 so = G1_STG;
  for (int i = 1; i < G1_NK - 1; i += 2) {
    BARRIER();
    SB(); rd(1, so); mma(0); G1_SCHED3(); SB();
    stat(0, i - 1);
    WAIT_LGKM0();
    so = so == (G1_NS - 1) * G1_STG ? 0 : so + G1_STG;
    BARRIER();
    SB(); rd(0, so); mma(1); G1_SCHED3(); SB();
    stat(1, i);
    WAIT_LGKM0();
    so = so == (G1_NS - 1) * G1_STG ? 0 : so + G1_STG;
  }
  BARRIER();
  SB(); rd(1, so); mma(0); G1_SCHED3(); SB();
  stat(0, 18);
  WAIT_LGKM0();
  mma(1);
  stat(1, 19);
  STAMP(stp,0,2);
  {
    char* ob = (char*)hmid + ((size_t)(pm * 8 + 4 * wt) * 48 + 3 * pn) * 1024 + (lane & 15) * 16 + (g >> 1) * 256 + (g & 1) * 8;
#pragma unroll
    for (int j = 0; j < 4; ++j) {
      char* oj = ob + (size_t)((j + rot) & 3) * 49152;
#pragma unroll
      for (int i = 0; i < 3; ++i) {
        const f32x4 gv = acc[i][j], uv = acc[3 + i][j];
        const u32x2 o = {pack_bf16(fmaxf(gv[0], 0.f) * uv[0], fmaxf(gv[1], 0.f) * uv[1]),
                         pack_bf16(fmaxf(gv[2], 0.f) * uv[2], fmaxf(gv[3], 0.f) * uv[3])};
        const int E = 6 * wf + 2 * i;
        *(u32x2*)(oj + (E >> 2) * 1024 + (E & 3) * 256) = o;
      }
    }
  }
  if (dostat) {
    const int tl = lane & 15;
    const float dg = (tl & 3) == 0 ? st2[0] : (tl & 3) == 1 ? st2[1] : (tl & 3) == 2 ? st2[2] : st2[3];
    if (g == (tl >> 2)) *(f32x2*)(part + ((size_t)((pn & 1) * 2 + wf) * L + t0 + jt * 16 + tl) * 2) = (f32x2){st1[0], dg};
  }
  STAMP(stp,0,4);
  STAMP(stp,0,5);
}

constexpr int G2_STG = 36864, G2_NS = 4, G2_NK = 12;
constexpr int G2_OFF_H = G2_STG * G2_NS;
constexpr int G2_LDS = G2_OFF_H + 10240;
constexpr int G2_OST = 176;

__global__ __launch_bounds__(512, 2) void k4_gemm2(const u16* __restrict__ hmid, const u16* __restrict__ Wdt, const u16* __restrict__ hT,
                                                   const float* __restrict__ part, u16* __restrict__ out, u64* stp) {
  STAMP(stp,0,0);
  extern __shared__ __attribute__((aligned(16))) char smem[];
  const u32 lds0 = (u32)(size_t)(LAS char*)smem;
  const int tid = threadIdx.x, lane = tid & 63;
  const int wave = __builtin_amdgcn_readfirstlane(tid >> 6);
  const int xcd = blockIdx.x & 7, idx = blockIdx.x >> 3;
  const int pm = (xcd & 3) * 8 + (idx & 7), pn = (xcd >> 2) * 4 + (idx >> 3);
  const int t0 = pm * 64, d0 = pn * 80;
  if (wave >= 4) {
    const int lw = wave - 4;
    const char* srcP = (const char*)hmid + (size_t)(pm * 4 + lw) * 49152 + lane * 16;
    const char* srcW = (const char*)Wdt + (size_t)pn * 245760 + lw * 5120 + lane * 16;
    const u32 dP = lds0 + lw * 4096, dW = lds0 + 16384 + lw * 5120;
    auto issue = [&](u32 so) {
      glds16(srcP, dP + so); glds16(srcW, dW + so);
      glds16(srcP + 1024, dP + so + 1024); glds16(srcW + 1024, dW + so + 1024);
      glds16(srcP + 2048, dP + so + 2048); glds16(srcW + 2048, dW + so + 2048);
      glds16(srcP + 3072, dP + so + 3072); glds16(srcW + 3072, dW + so + 3072);
      glds16(srcW + 4096, dW + so + 4096);
      srcP += 4096; srcW += 20480;
    };
    issue(0); issue(G2_STG);
    WAIT_VM(9); BARRIER();
    issue(2 * G2_STG); issue(3 * G2_STG);
#pragma unroll
    for (int k = 0; k < 3; ++k) {
      const int jj = lw + 4 * k < 10 ? lw + 4 * k : lw + 4;
      glds16((const char*)hT + (size_t)(d0 + 8 * jj + (lane >> 3)) * 4096 + t0 * 2 + (lane & 7) * 16,
             __builtin_amdgcn_readfirstlane(lds0 + G2_OFF_H + jj * 1024));
    }
    WAIT_VM(21); BARRIER();
    issue(0);
    WAIT_VM(21); BARRIER();
    u32 so = G2_STG;
    for (int i = 2; i < G2_NK - 3; ++i) {
      issue(so);
      so = so == 3 * G2_STG ? 0 : so + G2_STG;
      WAIT_VM(18); BARRIER();
    }
    WAIT_VM(9); BARRIER();
    WAIT_VM(0); BARRIER();
    BARRIER();
    BARRIER();
  } else {
    const int wt = wave & 1, wk = wave >> 1;
    const u32 pa = lds0 + (wt * 2) * 4096 + wk * 2048 + lane * 16;
    const u32 wa = lds0 + 16384 + wk * 2 * 5120 + lane * 16;
    f32x4 acc[5][2];
#pragma unroll
    for (int i = 0; i < 5; ++i) { acc[i][0] = (f32x4){0.f, 0.f, 0.f, 0.f}; acc[i][1] = acc[i][0]; }
    bf16x8 pf[2][2][2], wf[2][2][5];
    auto rd = [&](int set, u32 so) {
#pragma unroll
      for (int ks = 0; ks < 2; ++ks) {
#pragma unroll
        for (int j = 0; j < 2; ++j) pf[set][ks][j] = *(LAS bf16x8*)(pa + so + j * 4096 + ks * 1024);
#pragma unroll
        for (int i = 0; i < 5; ++i) wf[set][ks][i] = *(LAS bf16x8*)(wa + so + ks * 5120 + i * 1024);
      }
    };
    auto mma = [&](int set) {
#pragma unroll
      for (int ks = 0; ks < 2; ++ks)
#pragma unroll
        for (int i = 0; i < 5; ++i)
#pragma unroll
          for (int j = 0; j < 2; ++j) acc[i][j] = mfma16(wf[set][ks][i], pf[set][ks][j], acc[i][j]);
    };
#define G2_SCHED3() do { _Pragma("unroll") for (int k_ = 0; k_ < 14; ++k_) { __builtin_amdgcn_sched_group_barrier(0x008, 1, 0); __builtin_amdgcn_sched_group_barrier(0x100, 1, 0); } \
  __builtin_amdgcn_sched_group_barrier(0x008, 6, 0); } while (0)
    const int tl = wt * 32 + wk * 16;
    float rr;
    {
      const f32x2* pp = (const f32x2*)part + t0 + tl + (lane & 15);
      const f32x2 s = pp[0] + pp[L] + pp[2 * L] + pp[3 * L];
      const float mean = s.x * (1.f / D);
      const float var = fmaxf(s.y * (1.f / D) - mean * mean, 0.f);
      const float r = 1.f / rbf(sqrtf(rbf(rbf(var) + 1e-5f)));
      rr = r * r;
    }
    BARRIER();
    STAMP(stp,0,1);
    rd(0, 0);
    WAIT_LGKM0();
    u32 so = G2_STG;
    for (int i = 1; i < G2_NK - 1; i += 2) {
      BARRIER();
      SB(); rd(1, so); mma(0); G2_SCHED3(); SB();
      WAIT_LGKM0();
      so = so == 3 * G2_STG ? 0 : so + G2_STG;
      BARRIER();
      SB(); rd(0, so); mma(1); G2_SCHED3(); SB();
      WAIT_LGKM0();
      so = so == 3 * G2_STG ? 0 : so + G2_STG;
    }
    BARRIER();
    SB(); rd(1, so); mma(0); G2_SCHED3(); SB();
    WAIT_LGKM0();
    mma(1);
    STAMP(stp,0,2);
    BARRIER();
    const u32 ex = lds0 + (wave ^ 2) * 5120 + lane * 16, exr = lds0 + wave * 5120 + lane * 16;
#pragma unroll
    for (int i = 0; i < 5; ++i) { const f32x4 a0 = acc[i][0], a1 = acc[i][1]; *(LAS f32x4*)(ex + i * 1024) = wk ? +a0 : +a1; }
    WAIT_LGKM0();
    BARRIER();
    STAMP(stp,0,3);
    const int g = lane >> 4, q = (lane & 15) >> 2, p = lane & 3;
    const u32 ost = lds0 + 32768;
#pragma unroll
    for (int i = 0; i < 5; ++i) {
      const f32x4 o = *(LAS f32x4*)(exr + i * 1024);
      const f32x4 a0 = acc[i][0], a1 = acc[i][1];
      const f32x4 m = wk ? +a1 : +a0;
      s16x4 hv = tr_read(lds0 + G2_OFF_H + (16 * i + 4 * g + q) * 128 + (tl + 4 * p) * 2);
      float r[4];
#pragma unroll
      for (int e = 0; e < 4; ++e) r[e] = bf2f((u16)hv[e]) + rbf((m[e] + o[e]) * rr);
      u32x2 v = {pack_bf16(r[0], r[1]), pack_bf16(r[2], r[3])};
      *(LAS u32x2*)(ost + (tl + (lane & 15)) * G2_OST + (16 * i + 4 * g) * 2) = v;
    }
  }
  __syncthreads();
  STAMP(stp,0,4);
  for (int c = tid; c < 640; c += 512) {
    const int r = c / 10, ch = c % 10;
    u32x4 v = *(LAS u32x4*)(lds0 + 32768 + r * G2_OST + ch * 16);
    *(u32x4*)((char*)out + (size_t)(t0 + r) * 1280 + d0 * 2 + ch * 16) = v;
  }
  STAMP(stp,0,5);
}

extern "C" void kernel_launch(void* const* d_in, const int* in_sizes, int n_in, void* d_out, int out_size, void* d_ws,
                              size_t ws_size, hipStream_t stream) {
  const u16* x = (const u16*)d_in[0];
  char* ws = (char*)d_ws;
  u16* uT = (u16*)(ws + WS_UT);
  u16* xT = (u16*)(ws + WS_XT);
  u16* hT = (u16*)(ws + WS_HT);
  u16* hm = (u16*)(ws + WS_HM);
  float* K = (float*)(ws + WS_K);
  u16* W1t = (u16*)(ws + WS_W1);
  u16* Wdt = (u16*)(ws + WS_WD);
  float* part = (float*)(ws + WS_R2);
  (void)hipFuncSetAttribute((const void*)k2_conv, hipFuncAttributeMaxDynamicSharedMemorySize, K2_LDS);
  (void)hipFuncSetAttribute((const void*)k3_gemm1, hipFuncAttributeMaxDynamicSharedMemorySize, G1_LDS);
  (void)hipFuncSetAttribute((const void*)k4_gemm2, hipFuncAttributeMaxDynamicSharedMemorySize, G2_LDS);
  k1_ln1<<<256, 256, 0, stream>>>(x, uT, xT);
  k2_conv<<<256, 512, K2_LDS, stream>>>((const u16*)d_in[1], (const u16*)d_in[2], (const u16*)d_in[3], (const u16*)d_in[4],
                                        (const u16*)d_in[5], (const u16*)d_in[6], (const u16*)d_in[7], (const u16*)d_in[8], uT, xT, hT,
                                        (const u16*)d_in[10], (const u16*)d_in[11], (const u16*)d_in[12], W1t, Wdt, K, (u64*)(ws + (66u << 20)));
  k3_gemm1<<<256, 512, G1_LDS, stream>>>(hT, W1t, hm, part, (u64*)(ws + (64u << 20)));
  k4_gemm2<<<256, 512, G2_LDS, stream>>>(hm, Wdt, hT, part, (u16*)d_out, (u64*)(ws + (65u << 20)));
}
```

```cpp
#include <hip/hip_runtime.h>
#include <hip/hip_bf16.h>
#include <stdint.h>

typedef unsigned short u16;
typedef unsigned u32;
typedef unsigned long long u64;
typedef short s16x4 __attribute__((ext_vector_type(4)));
typedef short s16x8 __attribute__((ext_vector_type(8)));
typedef __bf16 bf16x8 __attribute__((ext_vector_type(8)));
typedef __bf16 bf16x2 __attribute__((ext_vector_type(2)));
typedef float f32x4 __attribute__((ext_vector_type(4)));
typedef float f32x2 __attribute__((ext_vector_type(2)));
typedef u32 u32x4 __attribute__((ext_vector_type(4)));
typedef u32 u32x2 __attribute__((ext_vector_type(2)));
#define LAS __attribute__((address_space(3)))

constexpr int L = 2048, D = 640, NS = 192, F = 1536;

constexpr size_t WS_UT = 0;
constexpr size_t WS_XT = 4u << 20;
constexpr size_t WS_HT = 8u << 20;
constexpr size_t WS_HM = 12u << 20;
constexpr size_t WS_CS = 20u << 20;
constexpr size_t WS_K  = 24u << 20;
constexpr size_t WS_W1 = 32u << 20;
constexpr size_t WS_WD = 36u << 20;
constexpr size_t WS_KR = 44u << 20;
constexpr size_t WS_KI = 48u << 20;
constexpr size_t WS_R2 = 40u << 20;

__device__ __forceinline__ float bf2f(u16 v) { return __uint_as_float(((u32)v) << 16); }
__device__ __forceinline__ u32 pack_bf16(float lo, float hi) {
  bf16x2 r = __builtin_convertvector((f32x2){lo, hi}, bf16x2);
  return __builtin_bit_cast(u32, r);
}
__device__ __forceinline__ float rbf(float f) { return __uint_as_float(pack_bf16(f, f) << 16); }
__device__ __forceinline__ u16 f2bf(float f) { return (u16)pack_bf16(f, f); }
__device__ __forceinline__ float lo_bf(u32 v) { return __uint_as_float(v << 16); }
__device__ __forceinline__ float hi_bf(u32 v) { return __uint_as_float(v & 0xffff0000u); }

#define STAMP(buf, slot, i)
#define KST(buf, i)
#define WAIT_VM(n) asm volatile("s_waitcnt vmcnt(%0)" ::"n"(n) : "memory")
#define WAIT_LGKM0() do { asm volatile("" ::: "memory"); __builtin_amdgcn_s_waitcnt(0xC07F); asm volatile("" ::: "memory"); } while (0)
#define BARRIER() do { asm volatile("" ::: "memory"); __builtin_amdgcn_s_barrier(); asm volatile("" ::: "memory"); } while (0)

#define SB() __builtin_amdgcn_sched_barrier(0)
#define G1_SCHED() do { _Pragma("unroll") for (int k_ = 0; k_ < 20; ++k_) { __builtin_amdgcn_sched_group_barrier(0x100, 1, 0); __builtin_amdgcn_sched_group_barrier(0x008, 1, 0); } \
  __builtin_amdgcn_sched_group_barrier(0x008, 4, 0); } while (0)
#define G2_SCHED() do { _Pragma("unroll") for (int k_ = 0; k_ < 10; ++k_) { __builtin_amdgcn_sched_group_barrier(0x100, 2, 0); __builtin_amdgcn_sched_group_barrier(0x008, 1, 0); } \
  __builtin_amdgcn_sched_group_barrier(0x100, 4, 0); __builtin_amdgcn_sched_group_barrier(0x008, 10, 0); } while (0)
__device__ __forceinline__ void glds16(const void* gsrc, u32 lds_dst) {
  u32 keep;
  asm volatile("s_mov_b32 %0, m0\n\ts_mov_b32 m0, %2\n\ts_nop 0\n\tglobal_load_lds_dwordx4 %1, off\n\ts_mov_b32 m0, %0"
               : "=&s"(keep) : "v"(gsrc), "s"(lds_dst) : "memory");
}
__device__ __forceinline__ void l2touch(const void* p, u32& sink) {
  asm volatile("global_load_dword %0, %1, off" : "+v"(sink) : "v"(p) : "memory");
}
__device__ __forceinline__ s16x4 tr_read(u32 addr) {
  return __builtin_amdgcn_ds_read_tr16_b64_v4i16((LAS s16x4*)addr);
}
__device__ __forceinline__ bf16x8 tr_frag(u32 a0, u32 a1) {
  s16x4 a = tr_read(a0), b = tr_read(a1);
  return __builtin_bit_cast(bf16x8, __builtin_shufflevector(a, b, 0, 1, 2, 3, 4, 5, 6, 7));
}
__device__ __forceinline__ f32x4 mfma16(bf16x8 a, bf16x8 b, f32x4 c) { return __builtin_amdgcn_mfma_f32_16x16x32_bf16(a, b, c, 0, 0, 0); }

typedef _Float16 f16x2 __attribute__((ext_vector_type(2)));
typedef _Float16 f16x8 __attribute__((ext_vector_type(8)));
typedef float f32x16 __attribute__((ext_vector_type(16)));
__device__ __forceinline__ u32 pack_f16(float lo, float hi) { return __builtin_bit_cast(u32, __builtin_convertvector((f32x2){lo, hi}, f16x2)); }
template <int CTRL> __device__ __forceinline__ float dpp_f(float v) {
  return __builtin_bit_cast(float, __builtin_amdgcn_update_dpp(0, __builtin_bit_cast(int, v), CTRL, 0xf, 0xf, true));
}
__device__ __forceinline__ float wave_sum(float v) {
  v += dpp_f<0xB1>(v);
  v += dpp_f<0x4E>(v);
  v += dpp_f<0x141>(v);
  v += dpp_f<0x140>(v);
  { auto r = __builtin_amdgcn_permlane16_swap(__float_as_uint(v), __float_as_uint(v), false, false); v = __uint_as_float(r[0]) + __uint_as_float(r[1]); }
  { auto r = __builtin_amdgcn_permlane32_swap(__float_as_uint(v), __float_as_uint(v), false, false); v = __uint_as_float(r[0]) + __uint_as_float(r[1]); }
  return v;
}
__device__ __forceinline__ float wave_max(float v) {
  v = fmaxf(v, dpp_f<0xB1>(v));
  v = fmaxf(v, dpp_f<0x4E>(v));
  v = fmaxf(v, dpp_f<0x141>(v));
  v = fmaxf(v, dpp_f<0x140>(v));
  { auto r = __builtin_amdgcn_permlane16_swap(__float_as_uint(v), __float_as_uint(v), false, false); v = fmaxf(__uint_as_float(r[0]), __uint_as_float(r[1])); }
  { auto r = __builtin_amdgcn_permlane32_swap(__float_as_uint(v), __float_as_uint(v), false, false); v = fmaxf(__uint_as_float(r[0]), __uint_as_float(r[1])); }
  return v;
}
constexpr int K1_RS = 1344;
constexpr int UT_ROW = 4160;
constexpr int C_ROW = 272;
constexpr int C_SCR = 60 * C_ROW;
constexpr int A_OFF_P = 4 * C_SCR;
constexpr int K1_LDS = A_OFF_P + 9216 + 64;
constexpr int C_KCH = 4608;
constexpr int C_UCP = 4160, C_UCH = 2 * C_UCP;
constexpr int C_OFF_U = 0;
constexpr int C_OFF_K = 4 * C_UCH;
constexpr int C_OFF_X = C_OFF_K + 4 * C_KCH;
constexpr int K2_LDS = 86016;

struct cplx { f32x2 v, r; };
__device__ __forceinline__ cplx mk(f32x2 v) { return cplx{v, (f32x2){-v.y, v.x}}; }
__device__ __forceinline__ f32x2 cmul(f32x2 a, const cplx& w) {
  f32x2 t = (f32x2){a.x, a.x} * w.v;
  return __builtin_elementwise_fma((f32x2){a.y, a.y}, w.r, t);
}
__device__ __forceinline__ f32x2 cexp(float zr, float zi, float l, float sgn) {
  const float m = __builtin_amdgcn_exp2f(zr * l * 1.44269504088896341f);
  const float a = __builtin_amdgcn_fractf(zi * l * 0.15915494309189535f);
  return (f32x2){m * __builtin_amdgcn_cosf(a), sgn * m * __builtin_amdgcn_sinf(a)};
}
__device__ __forceinline__ u32 pkh(f32x2 v) { return __builtin_bit_cast(u32, __builtin_convertvector(v, f16x2)); }
__device__ __forceinline__ u32 cmulh(u32 a, u32 b) {
  u32 t, r;
  asm("v_pk_mul_f16 %0, %1, %2 op_sel:[0,0] op_sel_hi:[0,1]" : "=v"(t) : "v"(a), "v"(b));
  asm("v_pk_fma_f16 %0, %1, %2, %3 op_sel:[1,1,0] op_sel_hi:[1,0,1] neg_lo:[0,1,0]" : "=v"(r) : "v"(a), "v"(b), "v"(t));
  return r;
}
#define SET_M0(base) asm volatile("s_mov_b32 m0, %0\n\ts_nop 0" ::"s"(base) : "memory")
#define WR4(row, a, b, c, d)                                                                                    \
  asm volatile("ds_write_addtid_b32 %0 offset:%4\n\tds_write_addtid_b32 %1 offset:%4+272\n\t"                    \
               "ds_write_addtid_b32 %2 offset:%4+544\n\tds_write_addtid_b32 %3 offset:%4+816"                    \
               ::"v"(a), "v"(b), "v"(c), "v"(d), "n"((row) * C_ROW) : "memory")

__global__ __launch_bounds__(512, 4) void k1_front(const u16* __restrict__ x, char* __restrict__ uT, u16* __restrict__ xT,
                                                   const u16* __restrict__ Are, const u16* __restrict__ Aim, const u16* __restrict__ Bre,
                                                   const u16* __restrict__ Bim, const u16* __restrict__ Cre, const u16* __restrict__ Cim,
                                                   const u16* __restrict__ logdt, char* __restrict__ krev, float* __restrict__ kinv,
                                                   float* __restrict__ kdbg, u64* stp) {
  STAMP(stp,0,0);
  extern __shared__ __attribute__((aligned(16))) char smem[];
  const u32 lds0 = (u32)(size_t)(LAS char*)smem;
  const int tid = threadIdx.x, lane = tid & 63;
  const int wave = __builtin_amdgcn_readfirstlane(tid >> 6);
  if (blockIdx.x >= 160) {
  const int lb = blockIdx.x - 160;
  const int t0 = (lb & 7) * 256 + (lb >> 3) * 8;
  u32 v[5];
#pragma unroll
  for (int i = 0; i < 5; ++i) v[i] = ((const u32*)x)[(size_t)(t0 + wave) * 320 + i * 64 + lane];
  if (lb < 8) {
    for (int k = tid; k < 320; k += 512) {
      const int dch = lb * 80 + (k >> 2), part = k & 3;
      *(u32x4*)(uT + (size_t)dch * UT_ROW + (part < 2 ? part * 16 : 4128 + (part - 2) * 16)) = (u32x4){0u, 0u, 0u, 0u};
    }
  }
  float s = 0.f;
#pragma unroll
  for (int i = 0; i < 5; ++i) s += lo_bf(v[i]) + hi_bf(v[i]);
  const float mean = wave_sum(s) * (1.f / D);
  float qv = 0.f;
#pragma unroll
  for (int i = 0; i < 5; ++i) { const float a = lo_bf(v[i]) - mean, b = hi_bf(v[i]) - mean; qv += a * a + b * b; }
  const float var = wave_sum(qv) * (1.f / D);
  const float mb = rbf(mean), rinv = __builtin_amdgcn_rcpf(rbf(__builtin_amdgcn_sqrtf(rbf(rbf(var) + 1e-5f))));
  const u32 row = lds0 + wave * K1_RS + lane * 4;
#pragma unroll
  for (int i = 0; i < 5; ++i) {
    const u32 a = pack_bf16(lo_bf(v[i]) - mb, hi_bf(v[i]) - mb);
    const u32 u = pack_bf16(lo_bf(a) * rinv, hi_bf(a) * rinv);
    *(LAS u32*)(row + i * 256) = pack_f16(lo_bf(u), hi_bf(u));
    *(LAS u32*)(row + 8 * K1_RS + i * 256) = v[i];
  }
  STAMP(stp,0,2);
  __syncthreads();
  const int g = lane >> 4, q = (lane & 15) >> 2, p = lane & 3;
  for (int task = wave; task < 20; task += 8) {
    const int arr = task / 10, grp = task % 10;
    const u32 a = lds0 + arr * 8 * K1_RS + q * K1_RS + (64 * grp + 16 * g + 4 * p) * 2;
    s16x4 lo = tr_read(a), hi = tr_read(a + 4 * K1_RS);
    s16x8 o = __builtin_shufflevector(lo, hi, 0, 1, 2, 3, 4, 5, 6, 7);
    char* dst = arr ? (char*)xT + (size_t)(64 * grp + lane) * 4096 : uT + (size_t)(64 * grp + lane) * UT_ROW + 32;
    *(s16x8*)(dst + t0 * 2) = o;
  }
  STAMP(stp,0,3);
  return;
  }
  const int ch = wave & 3, hf = wave >> 2, d = ((blockIdx.x & 7) * 20 + (blockIdx.x >> 3)) * 4 + ch;
  const u32 sb = __builtin_amdgcn_readfirstlane(lds0 + ch * C_SCR);
  const int r32 = lane & 31, h32 = lane >> 5;
  const int d0 = d - ch;
  {
    const u64 q0 = (u64)Are, q1 = (u64)Aim, q2 = (u64)Bre, q3 = (u64)Bim, q4 = (u64)Cre, q5 = (u64)Cim;
#pragma unroll
    for (int rep = 0; rep < 2; ++rep) {
      if (rep == 1 && wave != 0) break;
      const int i = rep ? 8 : wave;
      const int b = 1024 * i + 16 * lane, a = b / 1536, o = b - a * 1536;
      u64 src = +q0;
      src = a == 1 ? +q1 : src; src = a == 2 ? +q2 : src; src = a == 3 ? +q3 : src; src = a == 4 ? +q4 : src; src = a == 5 ? +q5 : src;
      glds16((const char*)(src + (u64)d0 * 384 + o), __builtin_amdgcn_readfirstlane(lds0 + A_OFF_P + i * 1024));
    }
  }
  const u32 wdt = ((const u32*)logdt)[d >> 1];
  const float dt = rbf(__expf((d & 1) ? hi_bf(wdt) : lo_bf(wdt)));
  WAIT_VM(0);
  BARRIER();
  STAMP(stp,0,6);
  float zr[3], zi[3];
  const u32 pl = lds0 + A_OFF_P + ch * 384 + lane * 2;
#pragma unroll
  for (int r = 0; r < 3; ++r) {
    zr[r] = dt * bf2f(*(LAS u16*)(pl + r * 128));
    zi[r] = dt * bf2f(*(LAS u16*)(pl + 1536 + r * 128));
  }
  u32 an[3][11];
  if (hf == 0) {
    f32x2 cb[3];
    float bound = 0.f;
#pragma unroll
    for (int r = 0; r < 3; ++r) {
      const float br = dt * bf2f(*(LAS u16*)(pl + 2 * 1536 + r * 128)), bi = dt * bf2f(*(LAS u16*)(pl + 3 * 1536 + r * 128));
      const float cr = bf2f(*(LAS u16*)(pl + 4 * 1536 + r * 128)), ci = bf2f(*(LAS u16*)(pl + 5 * 1536 + r * 128));
      cb[r] = (f32x2){cr * br - ci * bi, -(cr * bi + ci * br)};
      const float grow = __builtin_amdgcn_exp2f(fmaxf(zr[r], 0.f) * (2064.f * 1.44269504f));
      bound = fmaxf(bound, (fabsf(cb[r].x) + fabsf(cb[r].y)) * grow);
    }
    bound = wave_max(bound);
    int ex = (int)((__float_as_uint(bound) >> 23) & 255);
    ex = ex < 20 ? 20 : (ex > 240 ? 240 : ex);
    const float scale = __uint_as_float((u32)(267 - ex) << 23);
    if (lane == 0) kinv[d] = __uint_as_float((u32)(ex - 5) << 23);
#pragma unroll
    for (int r = 0; r < 3; ++r) {
      const cplx w1 = mk(cexp(zr[r], zi[r], 1.f, -1.f));
      const cplx w512 = mk(cexp(zr[r], zi[r], 512.f, -1.f));
      const cplx w2 = mk(cmul(w1.v, w1));
      const f32x2 w3 = cmul(w2.v, w1);
      const cplx w4 = mk(cmul(w2.v, w2));
      const f32x2 w1024 = cmul(w512.v, w512), w1536 = cmul(w1024, w512);
      const f32x2 P0 = cb[r] * scale, P4 = cmul(P0, w4), P8 = cmul(P4, w4), P12 = cmul(P8, w4), P16 = cmul(P12, w4);
      an[r][0] = pkh(P0); an[r][1] = pkh(P4); an[r][2] = pkh(P8); an[r][3] = pkh(P12); an[r][4] = pkh(P16);
      an[r][5] = pkh(w1.v); an[r][6] = pkh(w2.v); an[r][7] = pkh(w3);
      an[r][8] = pkh(w512.v); an[r][9] = pkh(w1024); an[r][10] = pkh(w1536);
    }
  } else {
#pragma unroll
    for (int r = 0; r < 3; ++r) {
      const cplx w16 = mk(cexp(zr[r], zi[r], 16.f, 1.f));
      const cplx w128 = mk(cexp(zr[r], zi[r], 128.f, 1.f));
      const cplx w32 = mk(cmul(w16.v, w16));
      const f32x2 w48 = cmul(w32.v, w16);
      const f32x2 w64 = cmul(w32.v, w32);
      const f32x2 w192 = cmul(w64, w128), w256 = cmul(w128.v, w128);
      const f32x2 w320 = cmul(w192, w128), w384 = cmul(w256, w128), w448 = cmul(w320, w128);
      an[r][0] = pkh(w64); an[r][1] = pkh(w128.v); an[r][2] = pkh(w192); an[r][3] = pkh(w256);
      an[r][4] = pkh(w320); an[r][5] = pkh(w384); an[r][6] = pkh(w448);
      an[r][7] = pkh(w16.v); an[r][8] = pkh(w32.v); an[r][9] = pkh(w48); an[r][10] = 0x00003c00u;
    }
  }
  STAMP(stp,0,5);
  u32 arow;
  {
    const int hr = (r32 >> 2) & 1, rho = (r32 & 3) + 4 * (r32 >> 3);
    const int row = rho <= 8 ? rho + 8 * hr : rho <= 11 ? rho + 8 + 2 * hr : rho <= 13 ? rho + 10 + hr : rho + 11 + hr;
    arow = sb + row * C_ROW + h32 * 16 + hf * 128;
  }
  const u32 brow = sb + (28 + r32) * C_ROW + h32 * 16 + hf * 128;
  f32x16 kacc;
#pragma unroll
  for (int i = 0; i < 16; ++i) kacc[i] = 0.f;
  SET_M0(sb);
#pragma unroll
  for (int r = 0; r < 3; ++r) {
    u32 gv[2][4];
    auto grp = [&](int gi, u32 (&o)[4]) {
      const u32* A = an[r];
      if (hf == 0) {
        if (gi < 4) { o[0] = A[gi]; o[1] = cmulh(A[gi], A[5]); o[2] = cmulh(A[gi], A[6]); o[3] = cmulh(A[gi], A[7]); }
        else if (gi == 4) { o[0] = A[4]; o[1] = cmulh(A[0], A[8]); o[2] = cmulh(A[1], A[8]); o[3] = cmulh(A[2], A[8]); }
        else if (gi == 5) { o[0] = cmulh(A[3], A[8]); o[1] = cmulh(A[4], A[8]); o[2] = cmulh(A[0], A[9]); o[3] = cmulh(A[2], A[9]); }
        else if (gi == 6) { o[0] = cmulh(A[4], A[9]); o[1] = cmulh(A[0], A[10]); o[2] = cmulh(A[2], A[10]); o[3] = cmulh(A[4], A[10]); }
      } else {
        if (gi == 0) { o[0] = A[10]; o[1] = A[7]; o[2] = A[8]; o[3] = A[9]; }
        else { o[0] = A[gi - 1]; o[1] = cmulh(A[gi - 1], A[7]); o[2] = cmulh(A[gi - 1], A[8]); o[3] = cmulh(A[gi - 1], A[9]); }
      }
    };
    if (hf == 0) {
      grp(0, gv[0]);
#pragma unroll
      for (int gi = 0; gi < 7; ++gi) {
        if (gi < 6) grp(gi + 1, gv[(gi + 1) & 1]);
        SB();
        WR4(4 * gi, gv[gi & 1][0], gv[gi & 1][1], gv[gi & 1][2], gv[gi & 1][3]);
        SB();
      }
    } else {
      grp(0, gv[0]);
#pragma unroll
      for (int gi = 0; gi < 8; ++gi) {
        if (gi < 7) grp(gi + 1, gv[(gi + 1) & 1]);
        SB();
        WR4(28 + 4 * gi, gv[gi & 1][0], gv[gi & 1][1], gv[gi & 1][2], gv[gi & 1][3]);
        SB();
      }
    }
    WAIT_LGKM0();
    BARRIER();
    f16x8 fa[4], fb[4];
#pragma unroll
    for (int k = 0; k < 4; ++k) { fa[k] = *(LAS f16x8*)(arow + k * 32); fb[k] = *(LAS f16x8*)(brow + k * 32); }
    WAIT_LGKM0();
    BARRIER();
#pragma unroll
    for (int k = 0; k < 4; ++k) kacc = __builtin_amdgcn_mfma_f32_32x32x16_f16(fa[k], fb[k], kacc, 0, 0, 0);
  }
  STAMP(stp,0,1);
  {
    const u32 ex = sb + hf * 4096 + lane * 16, exr = sb + (hf ^ 1) * 4096 + lane * 16;
    if (hf == 0) {
      *(LAS f32x4*)(ex) = (f32x4){kacc[12], kacc[13], kacc[14], kacc[15]};
    } else {
      *(LAS f32x4*)(ex) = (f32x4){kacc[0], kacc[1], kacc[2], kacc[3]};
      *(LAS f32x4*)(ex + 1024) = (f32x4){kacc[4], kacc[5], kacc[6], kacc[7]};
      *(LAS f32x4*)(ex + 2048) = (f32x4){kacc[8], kacc[9], kacc[10], kacc[11]};
    }
    WAIT_LGKM0();
    BARRIER();
    char* kb = krev + (size_t)d * C_KCH + 4080 - 32 * r32 - 16 * h32;
    auto put = [&](int base, float e0, float e1, float e2, float e3, float e4, float e5, float e6, float e7) {
      u32x4 o = {pack_f16(e7, e6), pack_f16(e5, e4), pack_f16(e3, e2), pack_f16(e1, e0)};
      *(u32x4*)(kb - 2 * base) = o;
    };
    if (hf == 0) {
      const f32x4 o0 = *(LAS f32x4*)(exr), o1 = *(LAS f32x4*)(exr + 1024), o2 = *(LAS f32x4*)(exr + 2048);
      float v[12];
#pragma unroll
      for (int i = 0; i < 4; ++i) {
        v[i] = (kacc[i] + o0[i]) * (1.f / 256.f); v[4 + i] = (kacc[4 + i] + o1[i]) * (1.f / 256.f); v[8 + i] = (kacc[8 + i] + o2[i]) * (1.f / 256.f);
      }
      const bool far = r32 >= 16;
      put(0, v[0], far ? v[0] : v[1], v[2], far ? v[4] : v[3], v[4], far ? v[4] : v[5], v[6], far ? v[8] : v[7]);
      put(512, v[9], v[9], v[9], v[10], v[10], v[10], v[11], v[11]);
    } else {
      const f32x4 o = *(LAS f32x4*)(exr);
      const float v12 = (kacc[12] + o[0]) * (1.f / 256.f), v13 = (kacc[13] + o[1]) * (1.f / 256.f),
                  v14 = (kacc[14] + o[2]) * (1.f / 256.f), v15 = (kacc[15] + o[3]) * (1.f / 256.f);
      put(1024, v12, v12, v12, v12, h32 ? v13 : v12, v13, v13, v13);
      put(1536, v14, v14, v14, v14, h32 ? v15 : v14, v15, v15, v15);
    }
  }
  if (hf == 1 && lane < 32) *(u32x4*)(krev + (size_t)d * C_KCH + 4096 + lane * 16) = (u32x4){0u, 0u, 0u, 0u};
  STAMP(stp,0,2);
}

__global__ __launch_bounds__(512, 2) void k2_conv(const char* __restrict__ krev, const float* __restrict__ kinv, const u16* __restrict__ Dskip,
                                                  const char* __restrict__ uT, const u16* __restrict__ xT, u16* __restrict__ hT,
                                                  const u16* __restrict__ Wg, const u16* __restrict__ Wu, const u16* __restrict__ Wd,
                                                  u16* __restrict__ W1t, u16* __restrict__ Wdt, u64* stp) {
  STAMP(stp,0,0);
  extern __shared__ __attribute__((aligned(16))) char smem[];
  const u32 lds0 = (u32)(size_t)(LAS char*)smem;
  const int tid = threadIdx.x, lane = tid & 63;
  const int wave = __builtin_amdgcn_readfirstlane(tid >> 6);
  const int xcd = blockIdx.x & 7, idx = blockIdx.x >> 3;
  if (idx >= 20) {
    const int hid = xcd * 12 + (idx - 20);
    const int g = lane >> 4, q = (lane & 15) >> 2, p = lane & 3;
    if (hid < 48) {
      const u16* W = hid < 24 ? Wg : Wu;
      const int c0 = (hid % 24) * 64;
      for (int j = wave; j < 80; j += 8) {
        const int row = 8 * j + (lane >> 3), ch = (lane & 7) ^ ((row & 3) << 1);
        glds16((const char*)W + (size_t)row * 3072 + c0 * 2 + ch * 16, __builtin_amdgcn_readfirstlane(lds0 + j * 1024));
      }
      WAIT_VM(0);
      __syncthreads();
      {
        const int rg = tid >> 3, ch = tid & 7;
        float s[8] = {0, 0, 0, 0, 0, 0, 0, 0};
#pragma unroll
        for (int r = 0; r < 10; ++r) {
          const int row = rg * 10 + r;
          const u32x4 vv = *(LAS u32x4*)(lds0 + row * 128 + ((ch ^ ((row & 3) << 1)) * 16));
#pragma unroll
          for (int e = 0; e < 4; ++e) { s[2 * e] += lo_bf(vv[e]); s[2 * e + 1] += hi_bf(vv[e]); }
        }
#pragma unroll
        for (int e = 0; e < 8; ++e) {
          s[e] += __shfl_xor(s[e], 8); s[e] += __shfl_xor(s[e], 16); s[e] += __shfl_xor(s[e], 32);
        }
        if (lane < 8) {
#pragma unroll
          for (int e = 0; e < 8; ++e) *(LAS float*)(lds0 + 81920 + (wave * 64 + lane * 8 + e) * 4) = s[e];
        }
      }
      __syncthreads();
      for (int fr = wave; fr < 80; fr += 8) {
        const int ks = fr >> 2, tl = fr & 3;
        float cm = 0.f;
#pragma unroll
        for (int w = 0; w < 8; ++w) cm += *(LAS float*)(lds0 + 81920 + (w * 64 + tl * 16 + (lane & 15)) * 4);
        cm *= (1.f / D);
        const int row = 32 * ks + 8 * g + q;
        const u32 a = lds0 + row * 128 + (((2 * tl + (p >> 1)) ^ (q << 1)) * 16) + 8 * (p & 1);
        const s16x4 lo = tr_read(a), hi = tr_read(a + 512);
        u32x4 o;
        o[0] = pack_bf16(bf2f((u16)lo[0]) - cm, bf2f((u16)lo[1]) - cm);
        o[1] = pack_bf16(bf2f((u16)lo[2]) - cm, bf2f((u16)lo[3]) - cm);
        o[2] = pack_bf16(bf2f((u16)hi[0]) - cm, bf2f((u16)hi[1]) - cm);
        o[3] = pack_bf16(bf2f((u16)hi[2]) - cm, bf2f((u16)hi[3]) - cm);
        const int f = c0 + 16 * tl, pn = f / 96, fi = (f % 96) / 16;
        const int slot = (fi / 3) * 6 + (hid < 24 ? 0 : 3) + fi % 3;
        *(u32x4*)((char*)W1t + (((size_t)pn * 20 + ks) * 12 + slot) * 1024 + lane * 16) = o;
      }
    } else {
      const int ks = hid - 48;
      for (int j = wave; j < 40; j += 8) {
        const int P = 64 * j + lane, row = P / 80, ch = P % 80;
        glds16((const char*)Wd + (size_t)(32 * ks + row) * 1280 + ch * 16, __builtin_amdgcn_readfirstlane(lds0 + j * 1024));
      }
      WAIT_VM(0);
      __syncthreads();
      for (int fr = wave; fr < 40; fr += 8) {
        const u32 a = lds0 + (8 * g + q) * 1280 + (16 * fr + 4 * p) * 2;
        const s16x4 lo = tr_read(a), hi = tr_read(a + 4 * 1280);
        const s16x8 o = __builtin_shufflevector(lo, hi, 0, 1, 2, 3, 4, 5, 6, 7);
        *(s16x8*)((char*)Wdt + (((size_t)(fr / 5) * 48 + ks) * 5 + fr % 5) * 1024 + lane * 16) = o;
      }
    }
    return;
  }
  const int ch = wave & 3, hf = wave >> 2, d0 = (xcd * 20 + idx) * 4, d = d0 + ch;
  const int j = lane & 15, g = lane >> 4;
  {
    const char* up = uT + (size_t)d * UT_ROW + lane * 16 + hf * 2;
    const u32 dst = __builtin_amdgcn_readfirstlane(lds0 + C_OFF_U + ch * C_UCH + hf * C_UCP);
    const char* kp = krev + (size_t)d0 * C_KCH + lane * 16;
    glds16(kp + wave * 1024, __builtin_amdgcn_readfirstlane(lds0 + C_OFF_K + wave * 1024));
    glds16(up, dst);
    glds16(kp + (8 + wave) * 1024, __builtin_amdgcn_readfirstlane(lds0 + C_OFF_K + (8 + wave) * 1024));
    glds16(up + 1024, dst + 1024);
    if (wave < 2) glds16(kp + (16 + wave) * 1024, __builtin_amdgcn_readfirstlane(lds0 + C_OFF_K + (16 + wave) * 1024));
    glds16(up + 2048, dst + 2048);
    glds16(up + 3072, dst + 3072);
    if (lane < 2) glds16(up + 4096, dst + 4096);
  }
  const float it = kinv[d];
  const u32 wds = ((const u32*)Dskip)[d >> 1];
  WAIT_VM(0);
  BARRIER();
  STAMP(stp,0,2);
  u32x2 xv[4];
#pragma unroll
  for (int i = 0; i < 4; ++i) xv[i] = *(const u32x2*)((const char*)xT + (size_t)d * 4096 + (16 * (16 * (4 * hf + i) + 15 - j) + 4 * g) * 2);
  const int cc = (1 + j) & 1;
  const u32 aa = lds0 + C_OFF_U + ch * C_UCH + cc * C_UCP + 2 * (1 + j + 8 * g - cc) + hf * 256;
  const u32 ba = lds0 + C_OFF_K + ch * C_KCH + 32 * j + 16 * g + hf * 256;
  f32x4 acc[8];
#pragma unroll
  for (int i = 0; i < 8; ++i) acc[i] = (f32x4){0.f, 0.f, 0.f, 0.f};
  {
    f16x8 A[2][8], B[2][8];
    auto rd = [&](int set, int r) {
#pragma unroll
      for (int m = 0; m < 8; ++m) {
        const LAS u32* ap = (const LAS u32*)(aa + 64 * r + 512 * m);
        A[set][m] = __builtin_bit_cast(f16x8, (u32x4){ap[0], ap[1], ap[2], ap[3]});
        B[set][m] = *(LAS f16x8*)(ba + 64 * r + 512 * (7 - m));
      }
    };
    auto mm = [&](int set) {
#pragma unroll
      for (int sdx = 0; sdx < 8; ++sdx)
#pragma unroll
        for (int m = 0; m <= sdx; ++m) acc[sdx] = __builtin_amdgcn_mfma_f32_16x16x32_f16(A[set][m], B[set][sdx - m], acc[sdx], 0, 0, 0);
    };
#define C_SCHED() do { _Pragma("unroll") for (int k_ = 0; k_ < 24; ++k_) { __builtin_amdgcn_sched_group_barrier(0x008, 1, 0); __builtin_amdgcn_sched_group_barrier(0x100, 1, 0); } \
  __builtin_amdgcn_sched_group_barrier(0x008, 12, 0); } while (0)
    rd(0, 0);
    SB(); rd(1, 1); mm(0); C_SCHED(); SB();
    rd(0, 2); mm(1); C_SCHED(); SB();
    rd(1, 3); mm(0); C_SCHED(); SB();
    mm(1);
  }
  STAMP(stp,0,3);
  {
    const u32 ex = lds0 + C_OFF_X + wave * 4096 + lane * 16, exr = lds0 + C_OFF_X + (wave ^ 4) * 4096 + lane * 16;
    if (hf == 0) {
#pragma unroll
      for (int i = 0; i < 4; ++i) *(LAS f32x4*)(ex + i * 1024) = acc[4 + i];
    } else {
#pragma unroll
      for (int i = 0; i < 4; ++i) *(LAS f32x4*)(ex + i * 1024) = acc[i];
    }
    const float dsk = (d & 1) ? hi_bf(wds) : lo_bf(wds);
    f32x4 udf[4];
#pragma unroll
    for (int i = 0; i < 4; ++i) {
      const u32x2 uw = *(LAS u32x2*)(lds0 + C_OFF_U + ch * C_UCH + 2 * (16 + 16 * (16 * (4 * hf + i) + 15 - j) + 4 * g));
      const u32 w0 = uw[0], w1 = uw[1];
      const f16x2 u01 = __builtin_bit_cast(f16x2, w0), u23 = __builtin_bit_cast(f16x2, w1);
      const u32 a = pack_bf16((float)u01[0] * dsk, (float)u01[1] * dsk), b = pack_bf16((float)u23[0] * dsk, (float)u23[1] * dsk);
      udf[i] = (f32x4){lo_bf(a), hi_bf(a), lo_bf(b), hi_bf(b)};
    }
    WAIT_LGKM0();
    BARRIER();
#pragma unroll
    for (int i = 0; i < 4; ++i) {
      const f32x4 o = *(LAS f32x4*)(exr + i * 1024);
      const f32x4 a0 = acc[i], a1 = acc[4 + i];
      const f32x4 y = ((hf ? +a1 : +a0) + o) * it;
      const u32 y01 = pack_bf16(y[0], y[1]), y23 = pack_bf16(y[2], y[3]);
      const u32 s01 = pack_bf16(lo_bf(y01) + udf[i][0], hi_bf(y01) + udf[i][1]);
      const u32 s23 = pack_bf16(lo_bf(y23) + udf[i][2], hi_bf(y23) + udf[i][3]);
      const u32 h01 = pack_bf16(lo_bf(xv[i][0]) + lo_bf(s01), hi_bf(xv[i][0]) + hi_bf(s01));
      const u32 h23 = pack_bf16(lo_bf(xv[i][1]) + lo_bf(s23), hi_bf(xv[i][1]) + hi_bf(s23));
      const int t = 16 * (16 * (4 * hf + i) + 15 - j) + 4 * g;
      *(u32x2*)((char*)hT + (size_t)d * 4096 + t * 2) = (u32x2){h01, h23};
    }
  }
  STAMP(stp,0,4);
}

constexpr int G1_STG = 20480, G1_NS = 7, G1_NK = 20;
constexpr int G1_LDS = G1_STG * G1_NS;

__global__ __launch_bounds__(512, 2) void k3_gemm1(const u16* __restrict__ hT, const u16* __restrict__ W1t,
                                                   u16* __restrict__ hmid, float* __restrict__ part, u64* stp) {
  STAMP(stp,0,0);
  extern __shared__ __attribute__((aligned(16))) char smem[];
  const u32 lds0 = (u32)(size_t)(LAS char*)smem;
  const int tid = threadIdx.x, lane = tid & 63;
  const int wave = __builtin_amdgcn_readfirstlane(tid >> 6);
  const int xcd = blockIdx.x & 7, idx = blockIdx.x >> 3;
  const int pm = (xcd & 1) * 8 + (idx & 7), pn = (xcd >> 1) * 4 + (idx >> 3);
  const int t0 = pm * 128;
  if (wave >= 4) {
    const int lw = wave - 4;
    const int row = 8 * lw + (lane >> 4);
    const int ch = (lane & 15) ^ (((row & 3) << 1) | (((row >> 3) & 1) << 3));
    int kidx = 5 * (pn & 3);
    const char* srcH = (const char*)hT + (size_t)(row + 32 * kidx) * 4096 + t0 * 2 + ch * 16;
    const char* srcW = (const char*)W1t + (size_t)pn * 245760 + (size_t)kidx * 12288 + lw * 3072 + lane * 16;
    const u32 dH = lds0 + lw * 2048, dW = lds0 + 8192 + lw * 3072;
    auto issue = [&](u32 so) {
      glds16(srcH, dH + so);
      glds16(srcW, dW + so);
      glds16(srcH + 4 * 4096, dH + so + 1024);
      glds16(srcW + 1024, dW + so + 1024);
      glds16(srcW + 2048, dW + so + 2048);
      if (++kidx == G1_NK) { kidx = 0; srcH -= (size_t)19 * 32 * 4096; srcW -= 19 * 12288; }
      else { srcH += 32 * 4096; srcW += 12288; }
    };
    issue(0); issue(G1_STG);
    WAIT_VM(5); BARRIER();
    issue(2 * G1_STG); issue(3 * G1_STG);
    WAIT_VM(10); BARRIER();
    issue(4 * G1_STG); issue(5 * G1_STG);
    WAIT_VM(15); BARRIER();
    issue(6 * G1_STG); issue(0);
    WAIT_VM(20); BARRIER();
    u32 so = G1_STG;
    for (int i = 3; i < G1_NK - 5; ++i) {
      issue(so);
      so = so == (G1_NS - 1) * G1_STG ? 0 : so + G1_STG;
      WAIT_VM(20); BARRIER();
    }
    WAIT_VM(15); BARRIER(); WAIT_VM(10); BARRIER(); WAIT_VM(5); BARRIER(); WAIT_VM(0); BARRIER();
    return;
  }
  const int wt = wave & 1, wf = wave >> 1;
  const int g = lane >> 4, q = (lane & 15) >> 2, p = lane & 3;
  u32 pfsink = 0;
  const int rot = pn >> 2;
  const u32 hrow = (8 * g + q) * 256 + 8 * (p & 1);
  const u32 hx = q | ((g & 1) << 2);
  u32 ha[4];
#pragma unroll
  for (int j = 0; j < 4; ++j) ha[j] = lds0 + hrow + ((((u32)(4 * wt + ((j + rot) & 3)) ^ hx) << 1) | (p >> 1)) * 16;
  const u32 wa = lds0 + 8192 + wf * 6144 + lane * 16;
  f32x4 acc[6][4];
#pragma unroll
  for (int i = 0; i < 6; ++i)
#pragma unroll
    for (int j = 0; j < 4; ++j) acc[i][j] = (f32x4){0.f, 0.f, 0.f, 0.f};
  f32x4 st1 = {0.f, 0.f, 0.f, 0.f}, st2 = st1;
  const bf16x8 ones = __builtin_bit_cast(bf16x8, (u32x4){0x3f803f80u, 0x3f803f80u, 0x3f803f80u, 0x3f803f80u});
  bf16x8 hf[2][4], wfr[2][6];
  u32 hc[4] = {ha[0], ha[1], ha[2], ha[3]}, wc = wa;
  u32 so = 0;
  auto rd = [&](int set) {
    wfr[set][0] = *(LAS bf16x8*)(wc);
#pragma unroll
    for (int j = 0; j < 4; ++j) hf[set][j] = tr_frag(hc[j], hc[j] + 1024);
#pragma unroll
    for (int i = 1; i < 6; ++i) wfr[set][i] = *(LAS bf16x8*)(wc + i * 1024);
  };
  auto adv = [&]() {
    u32 dl = so == (G1_NS - 1) * G1_STG ? (u32)(-(G1_NS - 1) * G1_STG) : (u32)G1_STG;
    so += dl;
    asm volatile("" : "+s"(dl));
#pragma unroll
    for (int j = 0; j < 4; ++j) hc[j] += dl;
    wc += dl;
  };
  auto mma = [&](int set) {
#pragma unroll
    for (int i = 0; i < 6; ++i)
#pragma unroll
      for (int j = 0; j < 4; ++j) acc[i][j] = mfma16(wfr[set][i], hf[set][j], acc[i][j]);
  };
  auto stat = [&](int set) {
    st1 = mfma16(ones, hf[set][0], st1);
    st2 = mfma16(hf[set][0], hf[set][0], st2);
  };
#define SG(m, n) __builtin_amdgcn_sched_group_barrier(m, n, 0)
#define G1_SCHED4(extra) do { \
    SG(0x008, 1); SG(0x100, 1); SG(0x008, 1); SG(0x100, 1); SG(0x008, 1); SG(0x100, 1); SG(0x008, 1); SG(0x100, 1); \
    SG(0x008, 1); SG(0x100, 1); SG(0x008, 1); SG(0x100, 1); SG(0x008, 1); SG(0x100, 1); SG(0x008, 1); SG(0x100, 1); \
    SG(0x008, 1); SG(0x100, 1); SG(0x008, 2); SG(0x100, 1); SG(0x008, 2); SG(0x100, 1); SG(0x008, 2); SG(0x100, 1); \
    SG(0x008, 2); SG(0x100, 1); SG(0x008, 1); SG(0x100, 1); SG(0x008, 1); SG(0x002, 1); SG(0x008, 1); SG(0x002, 1); \
    SG(0x008, 1); SG(0x002, 1); SG(0x008, 1); SG(0x002, 1); SG(0x008, 1); SG(0x002, 1); SG(0x008, 1 + (extra)); } while (0)
  BARRIER();
  STAMP(stp,0,1);
  KST(stp, 0);
  rd(0); adv();
#pragma unroll
  for (int i = 1; i < G1_NK - 1; i += 2) {
    BARRIER();
    KST(stp, i);
    SB(); rd(1); mma(0); adv();
    if (i - 1 < 5) { stat(0); G1_SCHED4(2); } else { G1_SCHED4(0); }
    SB();
    BARRIER();
    KST(stp, i + 1);
    SB(); rd(0); mma(1); adv();
    if (i < 5) { stat(1); G1_SCHED4(2); } else { G1_SCHED4(0); }
    SB();
  }
  BARRIER();
  KST(stp, 19);
  SB(); rd(1); mma(0); G1_SCHED4(0); SB();
#pragma unroll
  for (int i = 0; i < 3; ++i)
#pragma unroll
    for (int j = 0; j < 4; ++j) {
      acc[i][j] = mfma16(wfr[1][i], hf[1][j], acc[i][j]);
      acc[3 + i][j] = mfma16(wfr[1][3 + i], hf[1][j], acc[3 + i][j]);
    }
  asm volatile("s_waitcnt vmcnt(0)" :: "v"(pfsink) : "memory");
  STAMP(stp,0,2);
  {
    char* ob = (char*)hmid + ((size_t)(pm * 8 + 4 * wt) * 48 + 3 * pn) * 1024 + (lane & 15) * 16 + (g >> 1) * 256 + (g & 1) * 8;
#pragma unroll
    for (int j = 0; j < 4; ++j) {
      char* oj = ob + (size_t)((j + rot) & 3) * 49152;
#pragma unroll
      for (int i = 0; i < 3; ++i) {
        const f32x4 gv = acc[i][j], uv = acc[3 + i][j];
        const u32x2 o = {pack_bf16(fmaxf(gv[0], 0.f) * uv[0], fmaxf(gv[1], 0.f) * uv[1]),
                         pack_bf16(fmaxf(gv[2], 0.f) * uv[2], fmaxf(gv[3], 0.f) * uv[3])};
        const int E = 6 * wf + 2 * i;
        *(u32x2*)(oj + (E >> 2) * 1024 + (E & 3) * 256) = o;
      }
    }
  }
  if (wf == 0) {
    const int tl = lane & 15;
    const float dg = (tl & 3) == 0 ? st2[0] : (tl & 3) == 1 ? st2[1] : (tl & 3) == 2 ? st2[2] : st2[3];
    if (g == (tl >> 2)) *(f32x2*)(part + ((size_t)(pn & 3) * L + t0 + (4 * wt + rot) * 16 + tl) * 2) = (f32x2){st1[0], dg};
  }
  STAMP(stp,0,4);
  STAMP(stp,0,5);
}

constexpr int G2_STG = 36864, G2_NS = 4, G2_NK = 12;
constexpr int G2_OFF_H = G2_STG * G2_NS;
constexpr int G2_LDS = G2_OFF_H + 10240;
constexpr int G2_OST = 176;

__global__ __launch_bounds__(512, 2) void k4_gemm2(const u16* __restrict__ hmid, const u16* __restrict__ Wdt, const u16* __restrict__ hT,
                                                   const float* __restrict__ part, u16* __restrict__ out, u64* stp) {
  STAMP(stp,0,0);
  extern __shared__ __attribute__((aligned(16))) char smem[];
  const u32 lds0 = (u32)(size_t)(LAS char*)smem;
  const int tid = threadIdx.x, lane = tid & 63;
  const int wave = __builtin_amdgcn_readfirstlane(tid >> 6);
  const int xcd = blockIdx.x & 7, idx = blockIdx.x >> 3;
  const int pm = (xcd & 3) * 8 + (idx & 7), pn = (xcd >> 2) * 4 + (idx >> 3);
  const int t0 = pm * 64, d0 = pn * 80;
  if (wave >= 4) {
    const int lw = wave - 4;
#define G2ROT 1
    int kidx = G2ROT ? ((idx & 7) * 3 + (idx >> 3)) % G2_NK : 0;
    const char* srcP = (const char*)hmid + (size_t)(pm * 4 + lw) * 49152 + kidx * 4096 + lane * 16;
    const char* srcW = (const char*)Wdt + (size_t)pn * 245760 + (size_t)kidx * 20480 + lw * 5120 + lane * 16;
    const u32 dP = lds0 + lw * 4096, dW = lds0 + 16384 + lw * 5120;
    auto issue = [&](u32 so) {
      glds16(srcP, dP + so); glds16(srcW, dW + so);
      glds16(srcP + 1024, dP + so + 1024); glds16(srcW + 1024, dW + so + 1024);
      glds16(srcP + 2048, dP + so + 2048); glds16(srcW + 2048, dW + so + 2048);
      glds16(srcP + 3072, dP + so + 3072); glds16(srcW + 3072, dW + so + 3072);
      glds16(srcW + 4096, dW + so + 4096);
      if (++kidx == G2_NK) { kidx = 0; srcP -= 11 * 4096; srcW -= 11 * 20480; }
      else { srcP += 4096; srcW += 20480; }
    };
    issue(0); issue(G2_STG);
    WAIT_VM(9); BARRIER();
    issue(2 * G2_STG); issue(3 * G2_STG);
#pragma unroll
    for (int k = 0; k < 3; ++k) {
      const int jj = lw + 4 * k < 10 ? lw + 4 * k : lw + 4;
      glds16((const char*)hT + (size_t)(d0 + 8 * jj + (lane >> 3)) * 4096 + t0 * 2 + (lane & 7) * 16,
             __builtin_amdgcn_readfirstlane(lds0 + G2_OFF_H + jj * 1024));
    }
    WAIT_VM(21); BARRIER();
    issue(0);
    WAIT_VM(21); BARRIER();
    u32 so = G2_STG;
    for (int i = 2; i < G2_NK - 3; ++i) {
      issue(so);
      so = so == 3 * G2_STG ? 0 : so + G2_STG;
      WAIT_VM(18); BARRIER();
    }
    WAIT_VM(9); BARRIER();
    WAIT_VM(0); BARRIER();
    BARRIER();
    BARRIER();
  } else {
    u32 pfsink = 0;
    const int wt = wave & 1, wk = wave >> 1;
    const u32 pa = lds0 + (wt * 2) * 4096 + wk * 2048 + lane * 16;
    const u32 wa = lds0 + 16384 + wk * 2 * 5120 + lane * 16;
    f32x4 acc[5][2];
#pragma unroll
    for (int i = 0; i < 5; ++i) { acc[i][0] = (f32x4){0.f, 0.f, 0.f, 0.f}; acc[i][1] = acc[i][0]; }
    bf16x8 pf[2][2][2], wf[2][2][5];
    u32 pc = pa, wc = wa, so = 0;
    auto rd = [&](int set) {
#pragma unroll
      for (int ks = 0; ks < 2; ++ks) {
        wf[set][ks][0] = *(LAS bf16x8*)(wc + ks * 5120);
#pragma unroll
        for (int j = 0; j < 2; ++j) pf[set][ks][j] = *(LAS bf16x8*)(pc + j * 4096 + ks * 1024);
#pragma unroll
        for (int i = 1; i < 5; ++i) wf[set][ks][i] = *(LAS bf16x8*)(wc + ks * 5120 + i * 1024);
      }
    };
    auto adv = [&]() {
      u32 dl = so == 3 * G2_STG ? (u32)(-3 * G2_STG) : (u32)G2_STG;
      so += dl;
      asm volatile("" : "+s"(dl));
      pc += dl; wc += dl;
    };
    auto mma = [&](int set) {
#pragma unroll
      for (int ks = 0; ks < 2; ++ks)
#pragma unroll
        for (int i = 0; i < 5; ++i)
#pragma unroll
          for (int j = 0; j < 2; ++j) acc[i][j] = mfma16(wf[set][ks][i], pf[set][ks][j], acc[i][j]);
    };
#define G2_SCHED4() do { _Pragma("unroll") for (int k_ = 0; k_ < 4; ++k_) { SG(0x008, 1); SG(0x100, 1); SG(0x008, 1); SG(0x100, 1); SG(0x008, 1); SG(0x100, 1); SG(0x008, 1); } \
    SG(0x008, 1); SG(0x100, 1); SG(0x008, 1); SG(0x100, 1); SG(0x008, 2); } while (0)
    const int tl = wt * 32 + wk * 16;
    float rr;
    {
      const f32x2* pp = (const f32x2*)part + t0 + tl + (lane & 15);
      const f32x2 s = pp[0] + pp[L] + pp[2 * L] + pp[3 * L];
      const float mean = s.x * (1.f / D);
      const float var = fmaxf(s.y * (1.f / D) - mean * mean, 0.f);
      const float r = __builtin_amdgcn_rcpf(rbf(__builtin_amdgcn_sqrtf(rbf(rbf(var) + 1e-5f))));
      rr = r * r;
    }
    BARRIER();
    STAMP(stp,0,1);
    KST(stp, 0);
    rd(0); adv();
    WAIT_LGKM0();
#pragma unroll
    for (int i = 1; i < G2_NK - 1; i += 2) {
      BARRIER();
      KST(stp, i);
      SB(); rd(1); mma(0); adv(); G2_SCHED4(); SB();
      WAIT_LGKM0();
      BARRIER();
      KST(stp, i + 1);
      SB(); rd(0); mma(1); adv(); G2_SCHED4(); SB();
      WAIT_LGKM0();
    }
    BARRIER();
    KST(stp, 11);
    SB(); rd(1); mma(0); G2_SCHED4(); SB();
    WAIT_LGKM0();
    mma(1);
    asm volatile("s_waitcnt vmcnt(0)" :: "v"(pfsink) : "memory");
    STAMP(stp,0,2);
    BARRIER();
    const u32 ex = lds0 + (wave ^ 2) * 5120 + lane * 16, exr = lds0 + wave * 5120 + lane * 16;
#pragma unroll
    for (int i = 0; i < 5; ++i) { const f32x4 a0 = acc[i][0], a1 = acc[i][1]; *(LAS f32x4*)(ex + i * 1024) = wk ? +a0 : +a1; }
    WAIT_LGKM0();
    BARRIER();
    STAMP(stp,0,3);
    const int g = lane >> 4, q = (lane & 15) >> 2, p = lane & 3;
    const u32 ost = lds0 + 32768;
#pragma unroll
    for (int i = 0; i < 5; ++i) {
      const f32x4 o = *(LAS f32x4*)(exr + i * 1024);
      const f32x4 a0 = acc[i][0], a1 = acc[i][1];
      const f32x4 m = wk ? +a1 : +a0;
      s16x4 hv = tr_read(lds0 + G2_OFF_H + (16 * i + 4 * g + q) * 128 + (tl + 4 * p) * 2);
      float r[4];
#pragma unroll
      for (int e = 0; e < 4; ++e) r[e] = bf2f((u16)hv[e]) + rbf((m[e] + o[e]) * rr);
      u32x2 v = {pack_bf16(r[0], r[1]), pack_bf16(r[2], r[3])};
      *(LAS u32x2*)(ost + (tl + (lane & 15)) * G2_OST + (16 * i + 4 * g) * 2) = v;
    }
  }
  __syncthreads();
  STAMP(stp,0,4);
  for (int c = tid; c < 640; c += 512) {
    const int r = c / 10, ch = c % 10;
    u32x4 v = *(LAS u32x4*)(lds0 + 32768 + r * G2_OST + ch * 16);
    *(u32x4*)((char*)out + (size_t)(t0 + r) * 1280 + d0 * 2 + ch * 16) = v;
  }
  STAMP(stp,0,5);
}

extern "C" void kernel_launch(void* const* d_in, const int* in_sizes, int n_in, void* d_out, int out_size, void* d_ws,
                              size_t ws_size, hipStream_t stream) {
  const u16* x = (const u16*)d_in[0];
  char* ws = (char*)d_ws;
  char* uT = ws + WS_UT;
  u16* xT = (u16*)(ws + WS_XT);
  u16* hT = (u16*)(ws + WS_HT);
  u16* hm = (u16*)(ws + WS_HM);
  float* K = (float*)(ws + WS_K);
  u16* W1t = (u16*)(ws + WS_W1);
  u16* Wdt = (u16*)(ws + WS_WD);
  float* part = (float*)(ws + WS_R2);
  char* krev = ws + WS_KR;
  float* kinv = (float*)(ws + WS_KI);
  (void)hipFuncSetAttribute((const void*)k1_front, hipFuncAttributeMaxDynamicSharedMemorySize, K1_LDS);
  (void)hipFuncSetAttribute((const void*)k2_conv, hipFuncAttributeMaxDynamicSharedMemorySize, K2_LDS);
  (void)hipFuncSetAttribute((const void*)k3_gemm1, hipFuncAttributeMaxDynamicSharedMemorySize, G1_LDS);
  (void)hipFuncSetAttribute((const void*)k4_gemm2, hipFuncAttributeMaxDynamicSharedMemorySize, G2_LDS);
  k1_front<<<416, 512, K1_LDS, stream>>>(x, uT, xT, (const u16*)d_in[1], (const u16*)d_in[2], (const u16*)d_in[3], (const u16*)d_in[4],
                                         (const u16*)d_in[5], (const u16*)d_in[6], (const u16*)d_in[7], krev, kinv, K, (u64*)(ws + (67u << 20)));
  k2_conv<<<256, 512, K2_LDS, stream>>>(krev, kinv, (const u16*)d_in[8], uT, xT, hT,
                                        (const u16*)d_in[10], (const u16*)d_in[11], (const u16*)d_in[12], W1t, Wdt, (u64*)(ws + (66u << 20)));
  k3_gemm1<<<256, 512, G1_LDS, stream>>>(hT, W1t, hm, part, (u64*)(ws + (64u << 20)));
  k4_gemm2<<<256, 512, G2_LDS, stream>>>(hm, Wdt, hT, part, (u16*)d_out, (u64*)(ws + (65u << 20)));
}
```

```cpp
#include <hip/hip_runtime.h>
#include <hip/hip_bf16.h>
#include <stdint.h>

typedef unsigned short u16;
typedef unsigned u32;
typedef unsigned long long u64;
typedef short s16x4 __attribute__((ext_vector_type(4)));
typedef short s16x8 __attribute__((ext_vector_type(8)));
typedef __bf16 bf16x8 __attribute__((ext_vector_type(8)));
typedef __bf16 bf16x2 __attribute__((ext_vector_type(2)));
typedef float f32x4 __attribute__((ext_vector_type(4)));
typedef float f32x2 __attribute__((ext_vector_type(2)));
typedef u32 u32x4 __attribute__((ext_vector_type(4)));
typedef u32 u32x2 __attribute__((ext_vector_type(2)));
#define LAS __attribute__((address_space(3)))

constexpr int L = 2048, D = 640, NS = 192, F = 1536;

constexpr size_t WS_UT = 0;
constexpr size_t WS_XT = 4u << 20;
constexpr size_t WS_HT = 8u << 20;
constexpr size_t WS_HM = 12u << 20;
constexpr size_t WS_CS = 20u << 20;
constexpr size_t WS_K  = 24u << 20;
constexpr size_t WS_W1 = 32u << 20;
constexpr size_t WS_WD = 36u << 20;
constexpr size_t WS_KR = 44u << 20;
constexpr size_t WS_KI = 48u << 20;
constexpr size_t WS_R2 = 40u << 20;

__device__ __forceinline__ float bf2f(u16 v) { return __uint_as_float(((u32)v) << 16); }
__device__ __forceinline__ u32 pack_bf16(float lo, float hi) {
  bf16x2 r = __builtin_convertvector((f32x2){lo, hi}, bf16x2);
  return __builtin_bit_cast(u32, r);
}
__device__ __forceinline__ float rbf(float f) { return __uint_as_float(pack_bf16(f, f) << 16); }
__device__ __forceinline__ u16 f2bf(float f) { return (u16)pack_bf16(f, f); }
__device__ __forceinline__ float lo_bf(u32 v) { return __uint_as_float(v << 16); }
__device__ __forceinline__ float hi_bf(u32 v) { return __uint_as_float(v & 0xffff0000u); }

#define STAMP(buf, slot, i)
#define KST(buf, i)
#define WAIT_VM(n) asm volatile("s_waitcnt vmcnt(%0)" ::"n"(n) : "memory")
#define WAIT_LGKM0() do { asm volatile("" ::: "memory"); __builtin_amdgcn_s_waitcnt(0xC07F); asm volatile("" ::: "memory"); } while (0)
#define BARRIER() do { asm volatile("" ::: "memory"); __builtin_amdgcn_s_barrier(); asm volatile("" ::: "memory"); } while (0)

#define SB() __builtin_amdgcn_sched_barrier(0)
#define G1_SCHED() do { _Pragma("unroll") for (int k_ = 0; k_ < 20; ++k_) { __builtin_amdgcn_sched_group_barrier(0x100, 1, 0); __builtin_amdgcn_sched_group_barrier(0x008, 1, 0); } \
  __builtin_amdgcn_sched_group_barrier(0x008, 4, 0); } while (0)
#define G2_SCHED() do { _Pragma("unroll") for (int k_ = 0; k_ < 10; ++k_) { __builtin_amdgcn_sched_group_barrier(0x100, 2, 0); __builtin_amdgcn_sched_group_barrier(0x008, 1, 0); } \
  __builtin_amdgcn_sched_group_barrier(0x100, 4, 0); __builtin_amdgcn_sched_group_barrier(0x008, 10, 0); } while (0)
__device__ __forceinline__ void glds16(const void* gsrc, u32 lds_dst) {
  u32 keep;
  asm volatile("s_mov_b32 %0, m0\n\ts_mov_b32 m0, %2\n\ts_nop 0\n\tglobal_load_lds_dwordx4 %1, off\n\ts_mov_b32 m0, %0"
               : "=&s"(keep) : "v"(gsrc), "s"(lds_dst) : "memory");
}
__device__ __forceinline__ void st16_wt(void* p, u32x4 v) {
  asm volatile("global_store_dwordx4 %0, %1, off sc1\n\ts_nop 1" :: "v"(p), "v"(v) : "memory");
}
__device__ __forceinline__ void l2touch(const void* p, u32& sink) {
  asm volatile("global_load_dword %0, %1, off" : "+v"(sink) : "v"(p) : "memory");
}
__device__ __forceinline__ s16x4 tr_read(u32 addr) {
  return __builtin_amdgcn_ds_read_tr16_b64_v4i16((LAS s16x4*)addr);
}
__device__ __forceinline__ bf16x8 tr_frag(u32 a0, u32 a1) {
  s16x4 a = tr_read(a0), b = tr_read(a1);
  return __builtin_bit_cast(bf16x8, __builtin_shufflevector(a, b, 0, 1, 2, 3, 4, 5, 6, 7));
}
__device__ __forceinline__ f32x4 mfma16(bf16x8 a, bf16x8 b, f32x4 c) { return __builtin_amdgcn_mfma_f32_16x16x32_bf16(a, b, c, 0, 0, 0); }

typedef _Float16 f16x2 __attribute__((ext_vector_type(2)));
typedef _Float16 f16x8 __attribute__((ext_vector_type(8)));
typedef float f32x16 __attribute__((ext_vector_type(16)));
__device__ __forceinline__ u32 pack_f16(float lo, float hi) { return __builtin_bit_cast(u32, __builtin_convertvector((f32x2){lo, hi}, f16x2)); }
template <int CTRL> __device__ __forceinline__ float dpp_f(float v) {
  return __builtin_bit_cast(float, __builtin_amdgcn_update_dpp(0, __builtin_bit_cast(int, v), CTRL, 0xf, 0xf, true));
}
__device__ __forceinline__ float wave_sum(float v) {
  v += dpp_f<0xB1>(v);
  v += dpp_f<0x4E>(v);
  v += dpp_f<0x141>(v);
  v += dpp_f<0x140>(v);
  { auto r = __builtin_amdgcn_permlane16_swap(__float_as_uint(v), __float_as_uint(v), false, false); v = __uint_as_float(r[0]) + __uint_as_float(r[1]); }
  { auto r = __builtin_amdgcn_permlane32_swap(__float_as_uint(v), __float_as_uint(v), false, false); v = __uint_as_float(r[0]) + __uint_as_float(r[1]); }
  return v;
}
__device__ __forceinline__ float wave_max(float v) {
  v = fmaxf(v, dpp_f<0xB1>(v));
  v = fmaxf(v, dpp_f<0x4E>(v));
  v = fmaxf(v, dpp_f<0x141>(v));
  v = fmaxf(v, dpp_f<0x140>(v));
  { auto r = __builtin_amdgcn_permlane16_swap(__float_as_uint(v), __float_as_uint(v), false, false); v = fmaxf(__uint_as_float(r[0]), __uint_as_float(r[1])); }
  { auto r = __builtin_amdgcn_permlane32_swap(__float_as_uint(v), __float_as_uint(v), false, false); v = fmaxf(__uint_as_float(r[0]), __uint_as_float(r[1])); }
  return v;
}
constexpr int K1_RS = 1344;
constexpr int UT_ROW = 4160;
constexpr int C_ROW = 272;
constexpr int C_SCR = 60 * C_ROW;
constexpr int A_OFF_P = 4 * C_SCR;
constexpr int K1_LDS = A_OFF_P + 9216 + 64;
constexpr int C_KCH = 4608;
constexpr int C_UCP = 4160, C_UCH = 2 * C_UCP;
constexpr int C_OFF_U = 0;
constexpr int C_OFF_K = 4 * C_UCH;
constexpr int C_OFF_X = C_OFF_K + 4 * C_KCH;
constexpr int K2_LDS = C_OFF_X + 8 * 4096;

struct cplx { f32x2 v, r; };
__device__ __forceinline__ cplx mk(f32x2 v) { return cplx{v, (f32x2){-v.y, v.x}}; }
__device__ __forceinline__ f32x2 cmul(f32x2 a, const cplx& w) {
  f32x2 t = (f32x2){a.x, a.x} * w.v;
  return __builtin_elementwise_fma((f32x2){a.y, a.y}, w.r, t);
}
__device__ __forceinline__ f32x2 cexp(float zr, float zi, float l, float sgn) {
  const float m = __builtin_amdgcn_exp2f(zr * l * 1.44269504088896341f);
  const float a = __builtin_amdgcn_fractf(zi * l * 0.15915494309189535f);
  return (f32x2){m * __builtin_amdgcn_cosf(a), sgn * m * __builtin_amdgcn_sinf(a)};
}
__device__ __forceinline__ u32 pkh(f32x2 v) { return __builtin_bit_cast(u32, __builtin_convertvector(v, f16x2)); }
__device__ __forceinline__ u32 cmulh(u32 a, u32 b) {
  u32 t, r;
  asm("v_pk_mul_f16 %0, %1, %2 op_sel:[0,0] op_sel_hi:[0,1]" : "=v"(t) : "v"(a), "v"(b));
  asm("v_pk_fma_f16 %0, %1, %2, %3 op_sel:[1,1,0] op_sel_hi:[1,0,1] neg_lo:[0,1,0]" : "=v"(r) : "v"(a), "v"(b), "v"(t));
  return r;
}
#define SET_M0(base) asm volatile("s_mov_b32 m0, %0\n\ts_nop 0" ::"s"(base) : "memory")
#define WR4(row, a, b, c, d)                                                                                    \
  asm volatile("ds_write_addtid_b32 %0 offset:%4\n\tds_write_addtid_b32 %1 offset:%4+272\n\t"                    \
               "ds_write_addtid_b32 %2 offset:%4+544\n\tds_write_addtid_b32 %3 offset:%4+816"                    \
               ::"v"(a), "v"(b), "v"(c), "v"(d), "n"((row) * C_ROW) : "memory")

__global__ __launch_bounds__(512, 4) void k1_front(const u16* __restrict__ x, char* __restrict__ uT, u16* __restrict__ xT,
                                                   const u16* __restrict__ Are, const u16* __restrict__ Aim, const u16* __restrict__ Bre,
                                                   const u16* __restrict__ Bim, const u16* __restrict__ Cre, const u16* __restrict__ Cim,
                                                   const u16* __restrict__ logdt, char* __restrict__ krev, float* __restrict__ kinv,
                                                   const u16* __restrict__ Wg, const u16* __restrict__ Wu, const u16* __restrict__ Wd,
                                                   float* __restrict__ kdbg, u64* stp) {
  STAMP(stp,0,0);
  extern __shared__ __attribute__((aligned(16))) char smem[];
  const u32 lds0 = (u32)(size_t)(LAS char*)smem;
  const int tid = threadIdx.x, lane = tid & 63;
  const int wave = __builtin_amdgcn_readfirstlane(tid >> 6);
  if (blockIdx.x >= 160) {
  const int lb = blockIdx.x - 160;
  const int t0 = (lb & 7) * 256 + (lb >> 3) * 8;
  u32 v[5];
#pragma unroll
  for (int i = 0; i < 5; ++i) v[i] = ((const u32*)x)[(size_t)(t0 + wave) * 320 + i * 64 + lane];
  if (lb < 8) {
    for (int k = tid; k < 320; k += 512) {
      const int dch = lb * 80 + (k >> 2), part = k & 3;
      *(u32x4*)(uT + (size_t)dch * UT_ROW + (part < 2 ? part * 16 : 4128 + (part - 2) * 16)) = (u32x4){0u, 0u, 0u, 0u};
    }
  }
  float s = 0.f;
#pragma unroll
  for (int i = 0; i < 5; ++i) s += lo_bf(v[i]) + hi_bf(v[i]);
  const float mean = wave_sum(s) * (1.f / D);
  float qv = 0.f;
#pragma unroll
  for (int i = 0; i < 5; ++i) { const float a = lo_bf(v[i]) - mean, b = hi_bf(v[i]) - mean; qv += a * a + b * b; }
  const float var = wave_sum(qv) * (1.f / D);
  const float mb = rbf(mean), rinv = __builtin_amdgcn_rcpf(rbf(__builtin_amdgcn_sqrtf(rbf(rbf(var) + 1e-5f))));
  const u32 row = lds0 + wave * K1_RS + lane * 4;
#pragma unroll
  for (int i = 0; i < 5; ++i) {
    const u32 a = pack_bf16(lo_bf(v[i]) - mb, hi_bf(v[i]) - mb);
    const u32 u = pack_bf16(lo_bf(a) * rinv, hi_bf(a) * rinv);
    *(LAS u32*)(row + i * 256) = pack_f16(lo_bf(u), hi_bf(u));
    *(LAS u32*)(row + 8 * K1_RS + i * 256) = v[i];
  }
  STAMP(stp,0,2);
  __syncthreads();
  const int g = lane >> 4, q = (lane & 15) >> 2, p = lane & 3;
  for (int task = wave; task < 20; task += 8) {
    const int arr = task / 10, grp = task % 10;
    const u32 a = lds0 + arr * 8 * K1_RS + q * K1_RS + (64 * grp + 16 * g + 4 * p) * 2;
    s16x4 lo = tr_read(a), hi = tr_read(a + 4 * K1_RS);
    s16x8 o = __builtin_shufflevector(lo, hi, 0, 1, 2, 3, 4, 5, 6, 7);
    char* dst = arr ? (char*)xT + (size_t)(64 * grp + lane) * 4096 : uT + (size_t)(64 * grp + lane) * UT_ROW + 32;
    *(s16x8*)(dst + t0 * 2) = o;
  }
  {
    u32 sink = 0;
    if (wave < 3) {
      const int id = lb * 180 + wave * 60 + (lane < 60 ? lane : 59);
      const char* pp = id < 15360 ? (const char*)Wg + (size_t)id * 128 : id < 30720 ? (const char*)Wu + (size_t)(id - 15360) * 128 : (const char*)Wd + (size_t)(id - 30720) * 128;
      l2touch(pp, sink);
      asm volatile("s_waitcnt vmcnt(0)" :: "v"(sink) : "memory");
    }
  }
  STAMP(stp,0,3);
  return;
  }
  __builtin_amdgcn_s_setprio(2);
  const int ch = wave & 3, hf = wave >> 2, d = ((blockIdx.x & 7) * 20 + (blockIdx.x >> 3)) * 4 + ch;
  const u32 sb = __builtin_amdgcn_readfirstlane(lds0 + ch * C_SCR);
  const int r32 = lane & 31, h32 = lane >> 5;
  const int d0 = d - ch;
  {
    const u64 q0 = (u64)Are, q1 = (u64)Aim, q2 = (u64)Bre, q3 = (u64)Bim, q4 = (u64)Cre, q5 = (u64)Cim;
#pragma unroll
    for (int rep = 0; rep < 2; ++rep) {
      if (rep == 1 && wave != 0) break;
      const int i = rep ? 8 : wave;
      const int b = 1024 * i + 16 * lane, a = b / 1536, o = b - a * 1536;
      u64 src = +q0;
      src = a == 1 ? +q1 : src; src = a == 2 ? +q2 : src; src = a == 3 ? +q3 : src; src = a == 4 ? +q4 : src; src = a == 5 ? +q5 : src;
      glds16((const char*)(src + (u64)d0 * 384 + o), __builtin_amdgcn_readfirstlane(lds0 + A_OFF_P + i * 1024));
    }
  }
  const u32 wdt = ((const u32*)logdt)[d >> 1];
  const float dt = rbf(__expf((d & 1) ? hi_bf(wdt) : lo_bf(wdt)));
  WAIT_VM(0);
  BARRIER();
  STAMP(stp,0,6);
  float zr[3], zi[3];
  const u32 pl = lds0 + A_OFF_P + ch * 384 + lane * 2;
#pragma unroll
  for (int r = 0; r < 3; ++r) {
    zr[r] = dt * bf2f(*(LAS u16*)(pl + r * 128));
    zi[r] = dt * bf2f(*(LAS u16*)(pl + 1536 + r * 128));
  }
  u32 an[3][11];
  if (hf == 0) {
    f32x2 cb[3];
    float bound = 0.f;
#pragma unroll
    for (int r = 0; r < 3; ++r) {
      const float br = dt * bf2f(*(LAS u16*)(pl + 2 * 1536 + r * 128)), bi = dt * bf2f(*(LAS u16*)(pl + 3 * 1536 + r * 128));
      const float cr = bf2f(*(LAS u16*)(pl + 4 * 1536 + r * 128)), ci = bf2f(*(LAS u16*)(pl + 5 * 1536 + r * 128));
      cb[r] = (f32x2){cr * br - ci * bi, -(cr * bi + ci * br)};
      const float grow = __builtin_amdgcn_exp2f(fmaxf(zr[r], 0.f) * (2064.f * 1.44269504f));
      bound = fmaxf(bound, (fabsf(cb[r].x) + fabsf(cb[r].y)) * grow);
    }
    bound = wave_max(bound);
    int ex = (int)((__float_as_uint(bound) >> 23) & 255);
    ex = ex < 20 ? 20 : (ex > 240 ? 240 : ex);
    const float scale = __uint_as_float((u32)(267 - ex) << 23);
    if (lane == 0) kinv[d] = __uint_as_float((u32)(ex - 5) << 23);
#pragma unroll
    for (int r = 0; r < 3; ++r) {
      const cplx w1 = mk(cexp(zr[r], zi[r], 1.f, -1.f));
      const cplx w512 = mk(cexp(zr[r], zi[r], 512.f, -1.f));
      const cplx w2 = mk(cmul(w1.v, w1));
      const f32x2 w3 = cmul(w2.v, w1);
      const cplx w4 = mk(cmul(w2.v, w2));
      const f32x2 w1024 = cmul(w512.v, w512), w1536 = cmul(w1024, w512);
      const f32x2 P0 = cb[r] * scale, P4 = cmul(P0, w4), P8 = cmul(P4, w4), P12 = cmul(P8, w4), P16 = cmul(P12, w4);
      an[r][0] = pkh(P0); an[r][1] = pkh(P4); an[r][2] = pkh(P8); an[r][3] = pkh(P12); an[r][4] = pkh(P16);
      an[r][5] = pkh(w1.v); an[r][6] = pkh(w2.v); an[r][7] = pkh(w3);
      an[r][8] = pkh(w512.v); an[r][9] = pkh(w1024); an[r][10] = pkh(w1536);
    }
  } else {
#pragma unroll
    for (int r = 0; r < 3; ++r) {
      const cplx w16 = mk(cexp(zr[r], zi[r], 16.f, 1.f));
      const cplx w128 = mk(cexp(zr[r], zi[r], 128.f, 1.f));
      const cplx w32 = mk(cmul(w16.v, w16));
      const f32x2 w48 = cmul(w32.v, w16);
      const f32x2 w64 = cmul(w32.v, w32);
      const f32x2 w192 = cmul(w64, w128), w256 = cmul(w128.v, w128);
      const f32x2 w320 = cmul(w192, w128), w384 = cmul(w256, w128), w448 = cmul(w320, w128);
      an[r][0] = pkh(w64); an[r][1] = pkh(w128.v); an[r][2] = pkh(w192); an[r][3] = pkh(w256);
      an[r][4] = pkh(w320); an[r][5] = pkh(w384); an[r][6] = pkh(w448);
      an[r][7] = pkh(w16.v); an[r][8] = pkh(w32.v); an[r][9] = pkh(w48); an[r][10] = 0x00003c00u;
    }
  }
  STAMP(stp,0,5);
  u32 arow;
  {
    const int hr = (r32 >> 2) & 1, rho = (r32 & 3) + 4 * (r32 >> 3);
    const int row = rho <= 8 ? rho + 8 * hr : rho <= 11 ? rho + 8 + 2 * hr : rho <= 13 ? rho + 10 + hr : rho + 11 + hr;
    arow = sb + row * C_ROW + h32 * 16 + hf * 128;
  }
  const u32 brow = sb + (28 + r32) * C_ROW + h32 * 16 + hf * 128;
  f32x16 kacc;
#pragma unroll
  for (int i = 0; i < 16; ++i) kacc[i] = 0.f;
  SET_M0(sb);
#pragma unroll
  for (int r = 0; r < 3; ++r) {
    u32 gv[2][4];
    auto grp = [&](int gi, u32 (&o)[4]) {
      const u32* A = an[r];
      if (hf == 0) {
        if (gi < 4) { o[0] = A[gi]; o[1] = cmulh(A[gi], A[5]); o[2] = cmulh(A[gi], A[6]); o[3] = cmulh(A[gi], A[7]); }
        else if (gi == 4) { o[0] = A[4]; o[1] = cmulh(A[0], A[8]); o[2] = cmulh(A[1], A[8]); o[3] = cmulh(A[2], A[8]); }
        else if (gi == 5) { o[0] = cmulh(A[3], A[8]); o[1] = cmulh(A[4], A[8]); o[2] = cmulh(A[0], A[9]); o[3] = cmulh(A[2], A[9]); }
        else if (gi == 6) { o[0] = cmulh(A[4], A[9]); o[1] = cmulh(A[0], A[10]); o[2] = cmulh(A[2], A[10]); o[3] = cmulh(A[4], A[10]); }
      } else {
        if (gi == 0) { o[0] = A[10]; o[1] = A[7]; o[2] = A[8]; o[3] = A[9]; }
        else { o[0] = A[gi - 1]; o[1] = cmulh(A[gi - 1], A[7]); o[2] = cmulh(A[gi - 1], A[8]); o[3] = cmulh(A[gi - 1], A[9]); }
      }
    };
    if (hf == 0) {
      grp(0, gv[0]);
#pragma unroll
      for (int gi = 0; gi < 7; ++gi) {
        if (gi < 6) grp(gi + 1, gv[(gi + 1) & 1]);
        SB();
        WR4(4 * gi, gv[gi & 1][0], gv[gi & 1][1], gv[gi & 1][2], gv[gi & 1][3]);
        SB();
      }
    } else {
      grp(0, gv[0]);
#pragma unroll
      for (int gi = 0; gi < 8; ++gi) {
        if (gi < 7) grp(gi + 1, gv[(gi + 1) & 1]);
        SB();
        WR4(28 + 4 * gi, gv[gi & 1][0], gv[gi & 1][1], gv[gi & 1][2], gv[gi & 1][3]);
        SB();
      }
    }
    WAIT_LGKM0();
    BARRIER();
    f16x8 fa[4], fb[4];
#pragma unroll
    for (int k = 0; k < 4; ++k) { fa[k] = *(LAS f16x8*)(arow + k * 32); fb[k] = *(LAS f16x8*)(brow + k * 32); }
    WAIT_LGKM0();
    BARRIER();
#pragma unroll
    for (int k = 0; k < 4; ++k) kacc = __builtin_amdgcn_mfma_f32_32x32x16_f16(fa[k], fb[k], kacc, 0, 0, 0);
  }
  STAMP(stp,0,1);
  {
    const u32 ex = sb + hf * 4096 + lane * 16, exr = sb + (hf ^ 1) * 4096 + lane * 16;
    if (hf == 0) {
      *(LAS f32x4*)(ex) = (f32x4){kacc[12], kacc[13], kacc[14], kacc[15]};
    } else {
      *(LAS f32x4*)(ex) = (f32x4){kacc[0], kacc[1], kacc[2], kacc[3]};
      *(LAS f32x4*)(ex + 1024) = (f32x4){kacc[4], kacc[5], kacc[6], kacc[7]};
      *(LAS f32x4*)(ex + 2048) = (f32x4){kacc[8], kacc[9], kacc[10], kacc[11]};
    }
    WAIT_LGKM0();
    BARRIER();
    char* kb = krev + (size_t)d * C_KCH + 4080 - 32 * r32 - 16 * h32;
    auto put = [&](int base, float e0, float e1, float e2, float e3, float e4, float e5, float e6, float e7) {
      u32x4 o = {pack_f16(e7, e6), pack_f16(e5, e4), pack_f16(e3, e2), pack_f16(e1, e0)};
      *(u32x4*)(kb - 2 * base) = o;
    };
    if (hf == 0) {
      const f32x4 o0 = *(LAS f32x4*)(exr), o1 = *(LAS f32x4*)(exr + 1024), o2 = *(LAS f32x4*)(exr + 2048);
      float v[12];
#pragma unroll
      for (int i = 0; i < 4; ++i) {
        v[i] = (kacc[i] + o0[i]) * (1.f / 256.f); v[4 + i] = (kacc[4 + i] + o1[i]) * (1.f / 256.f); v[8 + i] = (kacc[8 + i] + o2[i]) * (1.f / 256.f);
      }
      const bool far = r32 >= 16;
      put(0, v[0], far ? v[0] : v[1], v[2], far ? v[4] : v[3], v[4], far ? v[4] : v[5], v[6], far ? v[8] : v[7]);
      put(512, v[9], v[9], v[9], v[10], v[10], v[10], v[11], v[11]);
    } else {
      const f32x4 o = *(LAS f32x4*)(exr);
      const float v12 = (kacc[12] + o[0]) * (1.f / 256.f), v13 = (kacc[13] + o[1]) * (1.f / 256.f),
                  v14 = (kacc[14] + o[2]) * (1.f / 256.f), v15 = (kacc[15] + o[3]) * (1.f / 256.f);
      put(1024, v12, v12, v12, v12, h32 ? v13 : v12, v13, v13, v13);
      put(1536, v14, v14, v14, v14, h32 ? v15 : v14, v15, v15, v15);
    }
  }
  if (hf == 1 && lane < 32) *(u32x4*)(krev + (size_t)d * C_KCH + 4096 + lane * 16) = (u32x4){0u, 0u, 0u, 0u};
  STAMP(stp,0,2);
}

__global__ __launch_bounds__(512, 2) void k2_conv(const char* __restrict__ krev, const float* __restrict__ kinv, const u16* __restrict__ Dskip,
                                                  const char* __restrict__ uT, const u16* __restrict__ xT, u16* __restrict__ hT,
                                                  const u16* __restrict__ Wg, const u16* __restrict__ Wu, const u16* __restrict__ Wd,
                                                  u16* __restrict__ W1t, u16* __restrict__ Wdt, u64* stp) {
  STAMP(stp,0,0);
  extern __shared__ __attribute__((aligned(16))) char smem[];
  const u32 lds0 = (u32)(size_t)(LAS char*)smem;
  const int tid = threadIdx.x, lane = tid & 63;
  const int wave = __builtin_amdgcn_readfirstlane(tid >> 6);
  const int xcd = blockIdx.x & 7, idx = blockIdx.x >> 3;
  if (idx >= 20) {
    const int hid = xcd * 12 + (idx - 20);
    const int g = lane >> 4, q = (lane & 15) >> 2, p = lane & 3;
    if (hid < 48) {
      const u16* W = hid < 24 ? Wg : Wu;
      const int c0 = (hid % 24) * 64;
      {
        const int row = 8 * wave + (lane >> 3), chk = (lane & 7) ^ ((row & 3) << 1);
        const char* src = (const char*)W + (size_t)row * 3072 + c0 * 2 + chk * 16;
#pragma unroll
        for (int jj = 0; jj < 10; ++jj) glds16(src + (size_t)jj * 64 * 3072, __builtin_amdgcn_readfirstlane(lds0 + (wave + 8 * jj) * 1024));
      }
      WAIT_VM(0);
      BARRIER();
      STAMP(stp,0,5);
      const int tl = wave & 3, ks0 = (wave >> 2) * 10;
      const u32 a = lds0 + (32 * ks0 + 8 * g + q) * 128 + (((2 * tl + (p >> 1)) ^ (q << 1)) * 16) + 8 * (p & 1);
      bf16x8 fr[10];
      f32x4 cs = {0.f, 0.f, 0.f, 0.f};
      const bf16x8 ones = __builtin_bit_cast(bf16x8, (u32x4){0x3f803f80u, 0x3f803f80u, 0x3f803f80u, 0x3f803f80u});
#pragma unroll
      for (int k = 0; k < 10; ++k) { fr[k] = tr_frag(a + k * 4096, a + k * 4096 + 512); cs = mfma16(ones, fr[k], cs); }
      WAIT_LGKM0();
      BARRIER();
      if (lane < 16) *(LAS float*)(lds0 + (wave * 16 + lane) * 4) = cs[0];
      WAIT_LGKM0();
      BARRIER();
      const float cm = (*(LAS float*)(lds0 + (tl * 16 + (lane & 15)) * 4) + *(LAS float*)(lds0 + ((tl + 4) * 16 + (lane & 15)) * 4)) * (1.f / D);
      const int f = c0 + 16 * tl, pn = f / 96, fi = (f % 96) / 16;
      const int slot = (fi / 3) * 6 + (hid < 24 ? 0 : 3) + fi % 3;
      char* dst = (char*)W1t + (((size_t)pn * 20 + ks0) * 12 + slot) * 1024 + lane * 16;
#pragma unroll
      for (int k = 0; k < 10; ++k) {
        const u32x4 w = __builtin_bit_cast(u32x4, fr[k]);
        u32x4 o;
#pragma unroll
        for (int e = 0; e < 4; ++e) o[e] = pack_bf16(lo_bf(w[e]) - cm, hi_bf(w[e]) - cm);
        st16_wt(dst + (size_t)k * 12288, o);
      }
    } else {
      const int ks = hid - 48;
#pragma unroll
      for (int jj = 0; jj < 5; ++jj) {
        const int P = 64 * (wave + 8 * jj) + lane, row = P / 80, chk = P % 80;
        glds16((const char*)Wd + (size_t)(32 * ks + row) * 1280 + chk * 16, __builtin_amdgcn_readfirstlane(lds0 + (wave + 8 * jj) * 1024));
      }
      WAIT_VM(0);
      BARRIER();
#pragma unroll
      for (int jj = 0; jj < 5; ++jj) {
        const int fr = wave + 8 * jj;
        const u32 a = lds0 + (8 * g + q) * 1280 + (16 * fr + 4 * p) * 2;
        const s16x4 lo = tr_read(a), hi = tr_read(a + 4 * 1280);
        st16_wt((char*)Wdt + (((size_t)(fr / 5) * 48 + ks) * 5 + fr % 5) * 1024 + lane * 16,
                __builtin_bit_cast(u32x4, __builtin_shufflevector(lo, hi, 0, 1, 2, 3, 4, 5, 6, 7)));
      }
    }
    STAMP(stp,0,7);
    return;
  }
  const int ch = wave & 3, hf = wave >> 2, d0 = (xcd * 20 + idx) * 4, d = d0 + ch;
  const int j = lane & 15, g = lane >> 4;
  {
    const char* up = uT + (size_t)d * UT_ROW + lane * 16 + hf * 2;
    const u32 dst = __builtin_amdgcn_readfirstlane(lds0 + C_OFF_U + ch * C_UCH + hf * C_UCP);
    const char* kp = krev + (size_t)d0 * C_KCH + lane * 16;
    glds16(kp + wave * 1024, __builtin_amdgcn_readfirstlane(lds0 + C_OFF_K + wave * 1024));
    glds16(up, dst);
    glds16(kp + (8 + wave) * 1024, __builtin_amdgcn_readfirstlane(lds0 + C_OFF_K + (8 + wave) * 1024));
    glds16(up + 1024, dst + 1024);
    if (wave < 2) glds16(kp + (16 + wave) * 1024, __builtin_amdgcn_readfirstlane(lds0 + C_OFF_K + (16 + wave) * 1024));
    glds16(up + 2048, dst + 2048);
    glds16(up + 3072, dst + 3072);
    if (lane < 2) glds16(up + 4096, dst + 4096);
  }
  const float it = kinv[d];
  const u32 wds = ((const u32*)Dskip)[d >> 1];
  WAIT_VM(0);
  BARRIER();
  STAMP(stp,0,2);
  u32x2 xv[4];
#pragma unroll
  for (int i = 0; i < 4; ++i) xv[i] = *(const u32x2*)((const char*)xT + (size_t)d * 4096 + (16 * (16 * (4 * hf + i) + 15 - j) + 4 * g) * 2);
  const int cc = (1 + j) & 1;
  const u32 aa = lds0 + C_OFF_U + ch * C_UCH + cc * C_UCP + 2 * (1 + j + 8 * g - cc) + hf * 256;
  const u32 ba = lds0 + C_OFF_K + ch * C_KCH + 32 * j + 16 * g + hf * 256;
  f32x4 acc[8];
#pragma unroll
  for (int i = 0; i < 8; ++i) acc[i] = (f32x4){0.f, 0.f, 0.f, 0.f};
  {
    f16x8 A[2][8], B[2][8];
    auto rd = [&](int set, int r) {
#pragma unroll
      for (int m = 0; m < 8; ++m) {
        const LAS u32* ap = (const LAS u32*)(aa + 64 * r + 512 * m);
        A[set][m] = __builtin_bit_cast(f16x8, (u32x4){ap[0], ap[1], ap[2], ap[3]});
        B[set][m] = *(LAS f16x8*)(ba + 64 * r + 512 * (7 - m));
      }
    };
    auto mm = [&](int set) {
#pragma unroll
      for (int sdx = 0; sdx < 8; ++sdx)
#pragma unroll
        for (int m = 0; m <= sdx; ++m) acc[sdx] = __builtin_amdgcn_mfma_f32_16x16x32_f16(A[set][m], B[set][sdx - m], acc[sdx], 0, 0, 0);
    };
#define C_SCHED() do { _Pragma("unroll") for (int k_ = 0; k_ < 24; ++k_) { __builtin_amdgcn_sched_group_barrier(0x008, 1, 0); __builtin_amdgcn_sched_group_barrier(0x100, 1, 0); } \
  __builtin_amdgcn_sched_group_barrier(0x008, 12, 0); } while (0)
    rd(0, 0);
    SB(); rd(1, 1); mm(0); C_SCHED(); SB();
    rd(0, 2); mm(1); C_SCHED(); SB();
    rd(1, 3); mm(0); C_SCHED(); SB();
    mm(1);
  }
  STAMP(stp,0,3);
  {
    const u32 ex = lds0 + C_OFF_X + wave * 4096 + lane * 16, exr = lds0 + C_OFF_X + (wave ^ 4) * 4096 + lane * 16;
    if (hf == 0) {
#pragma unroll
      for (int i = 0; i < 4; ++i) *(LAS f32x4*)(ex + i * 1024) = acc[4 + i];
    } else {
#pragma unroll
      for (int i = 0; i < 4; ++i) *(LAS f32x4*)(ex + i * 1024) = acc[i];
    }
    const float dsk = (d & 1) ? hi_bf(wds) : lo_bf(wds);
    f32x4 udf[4];
#pragma unroll
    for (int i = 0; i < 4; ++i) {
      const u32x2 uw = *(LAS u32x2*)(lds0 + C_OFF_U + ch * C_UCH + 2 * (16 + 16 * (16 * (4 * hf + i) + 15 - j) + 4 * g));
      const u32 w0 = uw[0], w1 = uw[1];
      const f16x2 u01 = __builtin_bit_cast(f16x2, w0), u23 = __builtin_bit_cast(f16x2, w1);
      const u32 a = pack_bf16((float)u01[0] * dsk, (float)u01[1] * dsk), b = pack_bf16((float)u23[0] * dsk, (float)u23[1] * dsk);
      udf[i] = (f32x4){lo_bf(a), hi_bf(a), lo_bf(b), hi_bf(b)};
    }
    WAIT_LGKM0();
    BARRIER();
#pragma unroll
    for (int i = 0; i < 4; ++i) {
      const f32x4 o = *(LAS f32x4*)(exr + i * 1024);
      const f32x4 a0 = acc[i], a1 = acc[4 + i];
      const f32x4 y = ((hf ? +a1 : +a0) + o) * it;
      const u32 y01 = pack_bf16(y[0], y[1]), y23 = pack_bf16(y[2], y[3]);
      const u32 s01 = pack_bf16(lo_bf(y01) + udf[i][0], hi_bf(y01) + udf[i][1]);
      const u32 s23 = pack_bf16(lo_bf(y23) + udf[i][2], hi_bf(y23) + udf[i][3]);
      const u32 h01 = pack_bf16(lo_bf(xv[i][0]) + lo_bf(s01), hi_bf(xv[i][0]) + hi_bf(s01));
      const u32 h23 = pack_bf16(lo_bf(xv[i][1]) + lo_bf(s23), hi_bf(xv[i][1]) + hi_bf(s23));
      const int t = 16 * (16 * (4 * hf + i) + 15 - j) + 4 * g;
      *(u32x2*)((char*)hT + (size_t)d * 4096 + t * 2) = (u32x2){h01, h23};
    }
  }
  STAMP(stp,0,4);
}

constexpr int G1_STG = 20480, G1_NS = 7, G1_NK = 20;
constexpr int G1_LDS = G1_STG * G1_NS;

__global__ __launch_bounds__(512, 2) void k3_gemm1(const u16* __restrict__ hT, const u16* __restrict__ W1t,
                                                   u16* __restrict__ hmid, float* __restrict__ part, const u16* __restrict__ Wdt, u64* stp) {
  STAMP(stp,0,0);
  extern __shared__ __attribute__((aligned(16))) char smem[];
  const u32 lds0 = (u32)(size_t)(LAS char*)smem;
  const int tid = threadIdx.x, lane = tid & 63;
  const int wave = __builtin_amdgcn_readfirstlane(tid >> 6);
  const int xcd = blockIdx.x & 7, idx = blockIdx.x >> 3;
  const int pm = (xcd & 1) * 8 + (idx & 7), pn = (xcd >> 1) * 4 + (idx >> 3);
  const int t0 = pm * 128;
  if (wave >= 4) {
    const int lw = wave - 4;
    const int row = 8 * lw + (lane >> 4);
    const int ch = (lane & 15) ^ (((row & 3) << 1) | (((row >> 3) & 1) << 3));
    int kidx = 5 * (pn & 3);
    const char* srcH = (const char*)hT + (size_t)(row + 32 * kidx) * 4096 + t0 * 2 + ch * 16;
    const char* srcW = (const char*)W1t + (size_t)pn * 245760 + (size_t)kidx * 12288 + lw * 3072 + lane * 16;
    const u32 dH = lds0 + lw * 2048, dW = lds0 + 8192 + lw * 3072;
    auto issue = [&](u32 so) {
      glds16(srcH, dH + so);
      glds16(srcW, dW + so);
      glds16(srcH + 4 * 4096, dH + so + 1024);
      glds16(srcW + 1024, dW + so + 1024);
      glds16(srcW + 2048, dW + so + 2048);
      if (++kidx == G1_NK) { kidx = 0; srcH -= (size_t)19 * 32 * 4096; srcW -= 19 * 12288; }
      else { srcH += 32 * 4096; srcW += 12288; }
    };
    issue(0); issue(G1_STG);
    WAIT_VM(5); BARRIER();
    issue(2 * G1_STG); issue(3 * G1_STG);
    WAIT_VM(10); BARRIER();
    issue(4 * G1_STG); issue(5 * G1_STG);
    WAIT_VM(15); BARRIER();
    issue(6 * G1_STG); issue(0);
    WAIT_VM(20); BARRIER();
    u32 so = G1_STG;
    for (int i = 3; i < G1_NK - 5; ++i) {
      issue(so);
      so = so == (G1_NS - 1) * G1_STG ? 0 : so + G1_STG;
      WAIT_VM(20); BARRIER();
    }
    {
      const int id = idx * 240 + lw * 60 + (lane < 60 ? lane : 59);
      u32 sink = 0;
      l2touch((const char*)Wdt + (size_t)((xcd >> 2) * 4) * 245760 + (size_t)id * 128, sink);
      WAIT_VM(16); BARRIER(); WAIT_VM(11); BARRIER(); WAIT_VM(6); BARRIER(); WAIT_VM(1); BARRIER();
      asm volatile("s_waitcnt vmcnt(0)" :: "v"(sink) : "memory");
    }
    return;
  }
  const int wt = wave & 1, wf = wave >> 1;
  const int g = lane >> 4, q = (lane & 15) >> 2, p = lane & 3;
  u32 pfsink = 0;
  const int rot = pn >> 2;
  const u32 hrow = (8 * g + q) * 256 + 8 * (p & 1);
  const u32 hx = q | ((g & 1) << 2);
  u32 ha[4];
#pragma unroll
  for (int j = 0; j < 4; ++j) ha[j] = lds0 + hrow + ((((u32)(4 * wt + ((j + rot) & 3)) ^ hx) << 1) | (p >> 1)) * 16;
  const u32 wa = lds0 + 8192 + wf * 6144 + lane * 16;
  f32x4 acc[6][4];
#pragma unroll
  for (int i = 0; i < 6; ++i)
#pragma unroll
    for (int j = 0; j < 4; ++j) acc[i][j] = (f32x4){0.f, 0.f, 0.f, 0.f};
  f32x4 st1 = {0.f, 0.f, 0.f, 0.f}, st2 = st1;
  const bf16x8 ones = __builtin_bit_cast(bf16x8, (u32x4){0x3f803f80u, 0x3f803f80u, 0x3f803f80u, 0x3f803f80u});
  bf16x8 hf[2][4], wfr[2][6];
  u32 hc[4] = {ha[0], ha[1], ha[2], ha[3]}, wc = wa;
  u32 so = 0;
  auto rd = [&](int set) {
    wfr[set][0] = *(LAS bf16x8*)(wc);
#pragma unroll
    for (int j = 0; j < 4; ++j) hf[set][j] = tr_frag(hc[j], hc[j] + 1024);
#pragma unroll
    for (int i = 1; i < 6; ++i) wfr[set][i] = *(LAS bf16x8*)(wc + i * 1024);
  };
  auto adv = [&]() {
    u32 dl = so == (G1_NS - 1) * G1_STG ? (u32)(-(G1_NS - 1) * G1_STG) : (u32)G1_STG;
    so += dl;
    asm volatile("" : "+s"(dl));
#pragma unroll
    for (int j = 0; j < 4; ++j) hc[j] += dl;
    wc += dl;
  };
  auto mma = [&](int set) {
#pragma unroll
    for (int i = 0; i < 6; ++i)
#pragma unroll
      for (int j = 0; j < 4; ++j) acc[i][j] = mfma16(wfr[set][i], hf[set][j], acc[i][j]);
  };
  auto stat = [&](int set) {
    st1 = mfma16(ones, hf[set][0], st1);
    st2 = mfma16(hf[set][0], hf[set][0], st2);
  };
#define SG(m, n) __builtin_amdgcn_sched_group_barrier(m, n, 0)
#define G1_SCHED4(extra) do { \
    SG(0x008, 1); SG(0x100, 1); SG(0x008, 1); SG(0x100, 1); SG(0x008, 1); SG(0x100, 1); SG(0x008, 1); SG(0x100, 1); \
    SG(0x008, 1); SG(0x100, 1); SG(0x008, 1); SG(0x100, 1); SG(0x008, 1); SG(0x100, 1); SG(0x008, 1); SG(0x100, 1); \
    SG(0x008, 1); SG(0x100, 1); SG(0x008, 2); SG(0x100, 1); SG(0x008, 2); SG(0x100, 1); SG(0x008, 2); SG(0x100, 1); \
    SG(0x008, 2); SG(0x100, 1); SG(0x008, 1); SG(0x100, 1); SG(0x008, 1); SG(0x002, 1); SG(0x008, 1); SG(0x002, 1); \
    SG(0x008, 1); SG(0x002, 1); SG(0x008, 1); SG(0x002, 1); SG(0x008, 1); SG(0x002, 1); SG(0x008, 1 + (extra)); } while (0)
  BARRIER();
  STAMP(stp,0,1);
  KST(stp, 0);
  rd(0); adv();
#pragma unroll
  for (int i = 1; i < G1_NK - 1; i += 2) {
    BARRIER();
    KST(stp, i);
    SB(); rd(1); mma(0); adv();
    if (i - 1 < 5) { stat(0); G1_SCHED4(2); } else { G1_SCHED4(0); }
    SB();
    BARRIER();
    KST(stp, i + 1);
    SB(); rd(0); mma(1); adv();
    if (i < 5) { stat(1); G1_SCHED4(2); } else { G1_SCHED4(0); }
    SB();
  }
  BARRIER();
  KST(stp, 19);
  SB(); rd(1); mma(0); G1_SCHED4(0); SB();
#pragma unroll
  for (int i = 0; i < 3; ++i)
#pragma unroll
    for (int j = 0; j < 4; ++j) {
      acc[i][j] = mfma16(wfr[1][i], hf[1][j], acc[i][j]);
      acc[3 + i][j] = mfma16(wfr[1][3 + i], hf[1][j], acc[3 + i][j]);
    }
  asm volatile("s_waitcnt vmcnt(0)" :: "v"(pfsink) : "memory");
  STAMP(stp,0,2);
  {
    char* ob = (char*)hmid + ((size_t)(pm * 8 + 4 * wt) * 48 + 3 * pn) * 1024 + (lane & 15) * 16 + (g >> 1) * 256 + (g & 1) * 8;
#pragma unroll
    for (int j = 0; j < 4; ++j) {
      char* oj = ob + (size_t)((j + rot) & 3) * 49152;
#pragma unroll
      for (int i = 0; i < 3; ++i) {
        const f32x4 gv = acc[i][j], uv = acc[3 + i][j];
        const u32x2 o = {pack_bf16(fmaxf(gv[0], 0.f) * uv[0], fmaxf(gv[1], 0.f) * uv[1]),
                         pack_bf16(fmaxf(gv[2], 0.f) * uv[2], fmaxf(gv[3], 0.f) * uv[3])};
        const int E = 6 * wf + 2 * i;
        *(u32x2*)(oj + (E >> 2) * 1024 + (E & 3) * 256) = o;
      }
    }
  }
  if (wf == 0) {
    const int tl = lane & 15;
    const float dg = (tl & 3) == 0 ? st2[0] : (tl & 3) == 1 ? st2[1] : (tl & 3) == 2 ? st2[2] : st2[3];
    if (g == (tl >> 2)) *(f32x2*)(part + ((size_t)(pn & 3) * L + t0 + (4 * wt + rot) * 16 + tl) * 2) = (f32x2){st1[0], dg};
  }
  STAMP(stp,0,4);
  STAMP(stp,0,5);
}

constexpr int G2_STG = 36864, G2_NS = 4, G2_NK = 12;
constexpr int G2_OFF_H = G2_STG * G2_NS;
constexpr int G2_LDS = G2_OFF_H + 10240;
constexpr int G2_OST = 176;

__global__ __launch_bounds__(512, 2) void k4_gemm2(const u16* __restrict__ hmid, const u16* __restrict__ Wdt, const u16* __restrict__ hT,
                                                   const float* __restrict__ part, u16* __restrict__ out, u64* stp) {
  STAMP(stp,0,0);
  extern __shared__ __attribute__((aligned(16))) char smem[];
  const u32 lds0 = (u32)(size_t)(LAS char*)smem;
  const int tid = threadIdx.x, lane = tid & 63;
  const int wave = __builtin_amdgcn_readfirstlane(tid >> 6);
  const int xcd = blockIdx.x & 7, idx = blockIdx.x >> 3;
  const int pm = (xcd & 3) * 8 + (idx & 7), pn = (xcd >> 2) * 4 + (idx >> 3);
  const int t0 = pm * 64, d0 = pn * 80;
  if (wave >= 4) {
    const int lw = wave - 4;
#define G2ROT 0
    int kidx = G2ROT ? ((idx & 7) * 3 + (idx >> 3)) % G2_NK : 0;
    const char* srcP = (const char*)hmid + (size_t)(pm * 4 + lw) * 49152 + kidx * 4096 + lane * 16;
    const char* srcW = (const char*)Wdt + (size_t)pn * 245760 + (size_t)kidx * 20480 + lw * 5120 + lane * 16;
    const u32 dP = lds0 + lw * 4096, dW = lds0 + 16384 + lw * 5120;
    auto issue = [&](u32 so) {
      glds16(srcP, dP + so); glds16(srcW, dW + so);
      glds16(srcP + 1024, dP + so + 1024); glds16(srcW + 1024, dW + so + 1024);
      glds16(srcP + 2048, dP + so + 2048); glds16(srcW + 2048, dW + so + 2048);
      glds16(srcP + 3072, dP + so + 3072); glds16(srcW + 3072, dW + so + 3072);
      glds16(srcW + 4096, dW + so + 4096);
      if (++kidx == G2_NK) { kidx = 0; srcP -= 11 * 4096; srcW -= 11 * 20480; }
      else { srcP += 4096; srcW += 20480; }
    };
    issue(0); issue(G2_STG);
    WAIT_VM(9); BARRIER();
    issue(2 * G2_STG); issue(3 * G2_STG);
#pragma unroll
    for (int k = 0; k < 3; ++k) {
      const int jj = lw + 4 * k < 10 ? lw + 4 * k : lw + 4;
      glds16((const char*)hT + (size_t)(d0 + 8 * jj + (lane >> 3)) * 4096 + t0 * 2 + (lane & 7) * 16,
             __builtin_amdgcn_readfirstlane(lds0 + G2_OFF_H + jj * 1024));
    }
    WAIT_VM(21); BARRIER();
    issue(0);
    WAIT_VM(21); BARRIER();
    u32 so = G2_STG;
    for (int i = 2; i < G2_NK - 3; ++i) {
      issue(so);
      so = so == 3 * G2_STG ? 0 : so + G2_STG;
      WAIT_VM(18); BARRIER();
    }
    WAIT_VM(9); BARRIER();
    WAIT_VM(0); BARRIER();
    BARRIER();
    BARRIER();
  } else {
    u32 pfsink = 0;
    const int wt = wave & 1, wk = wave >> 1;
    const u32 pa = lds0 + (wt * 2) * 4096 + wk * 2048 + lane * 16;
    const u32 wa = lds0 + 16384 + wk * 2 * 5120 + lane * 16;
    f32x4 acc[5][2];
#pragma unroll
    for (int i = 0; i < 5; ++i) { acc[i][0] = (f32x4){0.f, 0.f, 0.f, 0.f}; acc[i][1] = acc[i][0]; }
    bf16x8 pf[2][2][2], wf[2][2][5];
    u32 pc = pa, wc = wa, so = 0;
    auto rd = [&](int set) {
#pragma unroll
      for (int ks = 0; ks < 2; ++ks) {
        wf[set][ks][0] = *(LAS bf16x8*)(wc + ks * 5120);
#pragma unroll
        for (int j = 0; j < 2; ++j) pf[set][ks][j] = *(LAS bf16x8*)(pc + j * 4096 + ks * 1024);
#pragma unroll
        for (int i = 1; i < 5; ++i) wf[set][ks][i] = *(LAS bf16x8*)(wc + ks * 5120 + i * 1024);
      }
    };
    auto adv = [&]() {
      u32 dl = so == 3 * G2_STG ? (u32)(-3 * G2_STG) : (u32)G2_STG;
      so += dl;
      asm volatile("" : "+s"(dl));
      pc += dl; wc += dl;
    };
    auto mma = [&](int set) {
#pragma unroll
      for (int ks = 0; ks < 2; ++ks)
#pragma unroll
        for (int i = 0; i < 5; ++i)
#pragma unroll
          for (int j = 0; j < 2; ++j) acc[i][j] = mfma16(wf[set][ks][i], pf[set][ks][j], acc[i][j]);
    };
#define G2_SCHED4() do { _Pragma("unroll") for (int k_ = 0; k_ < 4; ++k_) { SG(0x008, 1); SG(0x100, 1); SG(0x008, 1); SG(0x100, 1); SG(0x008, 1); SG(0x100, 1); SG(0x008, 1); } \
    SG(0x008, 1); SG(0x100, 1); SG(0x008, 1); SG(0x100, 1); SG(0x008, 2); } while (0)
    const int tl = wt * 32 + wk * 16;
    float rr;
    {
      const f32x2* pp = (const f32x2*)part + t0 + tl + (lane & 15);
      const f32x2 s = pp[0] + pp[L] + pp[2 * L] + pp[3 * L];
      const float mean = s.x * (1.f / D);
      const float var = fmaxf(s.y * (1.f / D) - mean * mean, 0.f);
      const float r = __builtin_amdgcn_rcpf(rbf(__builtin_amdgcn_sqrtf(rbf(rbf(var) + 1e-5f))));
      rr = r * r;
    }
    BARRIER();
    STAMP(stp,0,1);
    KST(stp, 0);
    rd(0); adv();
    WAIT_LGKM0();
#pragma unroll
    for (int i = 1; i < G2_NK - 1; i += 2) {
      BARRIER();
      KST(stp, i);
      SB(); rd(1); mma(0); adv(); G2_SCHED4(); SB();
      WAIT_LGKM0();
      BARRIER();
      KST(stp, i + 1);
      SB(); rd(0); mma(1); adv(); G2_SCHED4(); SB();
      WAIT_LGKM0();
    }
    BARRIER();
    KST(stp, 11);
    SB(); rd(1); mma(0); G2_SCHED4(); SB();
    WAIT_LGKM0();
    mma(1);
    asm volatile("s_waitcnt vmcnt(0)" :: "v"(pfsink) : "memory");
    STAMP(stp,0,2);
    const int g = lane >> 4, q = (lane & 15) >> 2, p = lane & 3;
    s16x4 hv[5];
#pragma unroll
    for (int i = 0; i < 5; ++i) hv[i] = tr_read(lds0 + G2_OFF_H + (16 * i + 4 * g + q) * 128 + (tl + 4 * p) * 2);
    BARRIER();
    const u32 ex = lds0 + (wave ^ 2) * 5120 + lane * 16, exr = lds0 + wave * 5120 + lane * 16;
#pragma unroll
    for (int i = 0; i < 5; ++i) { const f32x4 a0 = acc[i][0], a1 = acc[i][1]; *(LAS f32x4*)(ex + i * 1024) = wk ? +a0 : +a1; }
    WAIT_LGKM0();
    BARRIER();
    STAMP(stp,0,3);
    u32 o2[5][2];
#pragma unroll
    for (int i = 0; i < 5; ++i) {
      const f32x4 o = *(LAS f32x4*)(exr + i * 1024);
      const f32x4 a0 = acc[i][0], a1 = acc[i][1];
      const f32x4 m = ((wk ? +a1 : +a0) + o) * rr;
      const u32 m01 = pack_bf16(m[0], m[1]), m23 = pack_bf16(m[2], m[3]);
      o2[i][0] = pack_bf16(bf2f((u16)hv[i][0]) + lo_bf(m01), bf2f((u16)hv[i][1]) + hi_bf(m01));
      o2[i][1] = pack_bf16(bf2f((u16)hv[i][2]) + lo_bf(m23), bf2f((u16)hv[i][3]) + hi_bf(m23));
    }
    char* orow = (char*)out + (size_t)(t0 + tl + (lane & 15)) * 1280 + d0 * 2;
#pragma unroll
    for (int i = 0; i < 4; i += 2) {
      const auto s0 = __builtin_amdgcn_permlane16_swap(o2[i][0], o2[i + 1][0], false, false);
      const auto s1 = __builtin_amdgcn_permlane16_swap(o2[i][1], o2[i + 1][1], false, false);
      *(u32x4*)(orow + (16 * (i + (g & 1)) + 8 * (g >> 1)) * 2) = (u32x4){s0[0], s1[0], s0[1], s1[1]};
    }
    *(u32x2*)(orow + (64 + 4 * g) * 2) = (u32x2){o2[4][0], o2[4][1]};
  }
  STAMP(stp,0,4);
  STAMP(stp,0,5);
}

extern "C" void kernel_launch(void* const* d_in, const int* in_sizes, int n_in, void* d_out, int out_size, void* d_ws,
                              size_t ws_size, hipStream_t stream) {
  const u16* x = (const u16*)d_in[0];
  char* ws = (char*)d_ws;
  char* uT = ws + WS_UT;
  u16* xT = (u16*)(ws + WS_XT);
  u16* hT = (u16*)(ws + WS_HT);
  u16* hm = (u16*)(ws + WS_HM);
  float* K = (float*)(ws + WS_K);
  u16* W1t = (u16*)(ws + WS_W1);
  u16* Wdt = (u16*)(ws + WS_WD);
  float* part = (float*)(ws + WS_R2);
  char* krev = ws + WS_KR;
  float* kinv = (float*)(ws + WS_KI);
  (void)hipFuncSetAttribute((const void*)k1_front, hipFuncAttributeMaxDynamicSharedMemorySize, K1_LDS);
  (void)hipFuncSetAttribute((const void*)k2_conv, hipFuncAttributeMaxDynamicSharedMemorySize, K2_LDS);
  (void)hipFuncSetAttribute((const void*)k3_gemm1, hipFuncAttributeMaxDynamicSharedMemorySize, G1_LDS);
  (void)hipFuncSetAttribute((const void*)k4_gemm2, hipFuncAttributeMaxDynamicSharedMemorySize, G2_LDS);
  k1_front<<<416, 512, K1_LDS, stream>>>(x, uT, xT, (const u16*)d_in[1], (const u16*)d_in[2], (const u16*)d_in[3], (const u16*)d_in[4],
                                         (const u16*)d_in[5], (const u16*)d_in[6], (const u16*)d_in[7], krev, kinv,
                                         (const u16*)d_in[10], (const u16*)d_in[11], (const u16*)d_in[12], K, (u64*)(ws + (67u << 20)));
  k2_conv<<<256, 512, K2_LDS, stream>>>(krev, kinv, (const u16*)d_in[8], uT, xT, hT,
                                        (const u16*)d_in[10], (const u16*)d_in[11], (const u16*)d_in[12], W1t, Wdt, (u64*)(ws + (66u << 20)));
  k3_gemm1<<<256, 512, G1_LDS, stream>>>(hT, W1t, hm, part, Wdt, (u64*)(ws + (64u << 20)));
  k4_gemm2<<<256, 512, G2_LDS, stream>>>(hm, Wdt, hT, part, (u16*)d_out, (u64*)(ws + (65u << 20)));
}
```

```cpp
#include <hip/hip_runtime.h>
#include <hip/hip_bf16.h>
#include <stdint.h>

typedef unsigned short u16;
typedef unsigned u32;
typedef unsigned long long u64;
typedef short s16x4 __attribute__((ext_vector_type(4)));
typedef __bf16 bf16x8 __attribute__((ext_vector_type(8)));
typedef __bf16 bf16x2 __attribute__((ext_vector_type(2)));
typedef _Float16 f16x2 __attribute__((ext_vector_type(2)));
typedef _Float16 f16x8 __attribute__((ext_vector_type(8)));
typedef float f32x16 __attribute__((ext_vector_type(16)));
typedef float f32x4 __attribute__((ext_vector_type(4)));
typedef float f32x2 __attribute__((ext_vector_type(2)));
typedef u32 u32x4 __attribute__((ext_vector_type(4)));
typedef u32 u32x2 __attribute__((ext_vector_type(2)));
typedef __attribute__((address_space(1))) u32 gu32;
typedef __attribute__((address_space(1))) u64 gu64;
#define LAS __attribute__((address_space(3)))
#define LDS(T, a) ((LAS T*)(size_t)(a))

constexpr int L = 2048, D = 640, NS = 192, F = 1536;

constexpr size_t WS_UT = 0;
constexpr size_t WS_LN = 4u << 20;
constexpr size_t WS_HT = 8u << 20;
constexpr size_t WS_HM = 12u << 20;
constexpr size_t WS_W1 = 32u << 20;
constexpr size_t WS_WD = 36u << 20;
constexpr size_t WS_PT = 40u << 20;
constexpr size_t WS_FL = 52u << 20;
constexpr size_t WS_DBG = 24u << 20;
constexpr int FL_STARTED = 1024;
constexpr int FL_U = 2048;
constexpr int FL_W = FL_U + 64 * 32;
constexpr int FL_H = FL_W + 96 * 32;
constexpr int FL_P = FL_H + 160 * 32;
constexpr int UT_ROW = 4160;

struct Args {
  const u16 *x, *Are, *Aim, *Bre, *Bim, *Cre, *Cim, *logdt, *Dskip, *Wg, *Wu, *Wd;
  u16* out;
  char* ws;
};

__device__ __forceinline__ float bf2f(u16 v) { return __uint_as_float(((u32)v) << 16); }
__device__ __forceinline__ u32 pack_bf16(float lo, float hi) { return __builtin_bit_cast(u32, __builtin_convertvector((f32x2){lo, hi}, bf16x2)); }
__device__ __forceinline__ u32 pack_f16(float lo, float hi) { return __builtin_bit_cast(u32, __builtin_convertvector((f32x2){lo, hi}, f16x2)); }
__device__ __forceinline__ float rbf(float f) { return __uint_as_float(pack_bf16(f, f) << 16); }
__device__ __forceinline__ float lo_bf(u32 v) { return __uint_as_float(v << 16); }
__device__ __forceinline__ float hi_bf(u32 v) { return __uint_as_float(v & 0xffff0000u); }

#define STAMP(i)
#define WAIT_VM(n) asm volatile("s_waitcnt vmcnt(%0)" ::"n"(n) : "memory")
#define WAIT_LGKM0() do { asm volatile("" ::: "memory"); __builtin_amdgcn_s_waitcnt(0xC07F); asm volatile("" ::: "memory"); } while (0)
#define BARRIER() do { asm volatile("" ::: "memory"); __builtin_amdgcn_s_barrier(); asm volatile("" ::: "memory"); } while (0)
#define SB() __builtin_amdgcn_sched_barrier(0)
#define SG(m, n) __builtin_amdgcn_sched_group_barrier(m, n, 0)
#define RLX_AGENT __ATOMIC_RELAXED, __HIP_MEMORY_SCOPE_AGENT

__device__ __forceinline__ void glds16(const void* gsrc, u32 lds_dst) {
  u32 keep;
  asm volatile("s_mov_b32 %0, m0\n\ts_mov_b32 m0, %2\n\ts_nop 0\n\tglobal_load_lds_dwordx4 %1, off\n\ts_mov_b32 m0, %0"
               : "=&s"(keep) : "v"(gsrc), "s"(lds_dst) : "memory");
}
__device__ __forceinline__ void glds16d(const void* gsrc, u32 lds_dst) {
  u32 keep;
  asm volatile("s_mov_b32 %0, m0\n\ts_mov_b32 m0, %2\n\ts_nop 0\n\tglobal_load_lds_dwordx4 %1, off sc1\n\ts_mov_b32 m0, %0"
               : "=&s"(keep) : "v"(gsrc), "s"(lds_dst) : "memory");
}
#define UNI(v) ((u32)__builtin_amdgcn_readfirstlane((int)(v)))
__device__ __forceinline__ void st16d(void* p, u32x4 v) {
  asm volatile("global_store_dwordx4 %0, %1, off sc1\n\ts_nop 1" :: "v"(p), "v"(v) : "memory");
}
constexpr u64 SPIN_TICKS = 300000;
template <int NL>
__device__ __forceinline__ void wait_flags(const u32* flags, int n, u32 tag, int lane) {
  const u64 t0 = __builtin_amdgcn_s_memrealtime();
  for (;;) {
    bool ok = true;
#pragma unroll
    for (int k = 0; k < NL; ++k) {
      const int i = k * 64 + lane;
      ok &= __hip_atomic_load((gu32*)(flags + (i < n ? i : n - 1) * 32), RLX_AGENT) == tag;
    }
    if (__all(ok)) break;
    if (__builtin_amdgcn_s_memrealtime() - t0 > SPIN_TICKS) break;
    __builtin_amdgcn_s_sleep(2);
  }
  asm volatile("" ::: "memory");
}
__device__ __forceinline__ void set_flag(u32* flag, u32 tag) { __hip_atomic_store((gu32*)flag, tag, RLX_AGENT); }

__device__ __forceinline__ s16x4 tr_read(u32 addr) { return __builtin_amdgcn_ds_read_tr16_b64_v4i16(LDS(s16x4, addr)); }
__device__ __forceinline__ u32x4 tr_read2(u32 a0, u32 a1) {
  const s16x4 a = tr_read(a0), b = tr_read(a1);
  return __builtin_bit_cast(u32x4, __builtin_shufflevector(a, b, 0, 1, 2, 3, 4, 5, 6, 7));
}
__device__ __forceinline__ bf16x8 tr_frag(u32 a0, u32 a1) { return __builtin_bit_cast(bf16x8, tr_read2(a0, a1)); }
__device__ __forceinline__ f32x4 mfma16(bf16x8 a, bf16x8 b, f32x4 c) { return __builtin_amdgcn_mfma_f32_16x16x32_bf16(a, b, c, 0, 0, 0); }
#define ONES_BF16 __builtin_bit_cast(bf16x8, (u32x4){0x3f803f80u, 0x3f803f80u, 0x3f803f80u, 0x3f803f80u})
__device__ __forceinline__ float diag16(f32x4 a, int lane) { const int r = lane & 3; return r == 0 ? a[0] : r == 1 ? a[1] : r == 2 ? a[2] : a[3]; }

template <int CTRL> __device__ __forceinline__ float dpp_f(float v) {
  return __builtin_bit_cast(float, __builtin_amdgcn_update_dpp(0, __builtin_bit_cast(int, v), CTRL, 0xf, 0xf, true));
}
__device__ __forceinline__ float wave_max(float v) {
  v = fmaxf(v, dpp_f<0xB1>(v));
  v = fmaxf(v, dpp_f<0x4E>(v));
  v = fmaxf(v, dpp_f<0x141>(v));
  v = fmaxf(v, dpp_f<0x140>(v));
  { auto r = __builtin_amdgcn_permlane16_swap(__float_as_uint(v), __float_as_uint(v), false, false); v = fmaxf(__uint_as_float(r[0]), __uint_as_float(r[1])); }
  { auto r = __builtin_amdgcn_permlane32_swap(__float_as_uint(v), __float_as_uint(v), false, false); v = fmaxf(__uint_as_float(r[0]), __uint_as_float(r[1])); }
  return v;
}
__device__ __forceinline__ u32x4 widen16(u32x2 a, u32x2 b) {
  const auto s0 = __builtin_amdgcn_permlane16_swap(a[0], b[0], false, false);
  const auto s1 = __builtin_amdgcn_permlane16_swap(a[1], b[1], false, false);
  return (u32x4){s0[0], s1[0], s0[1], s1[1]};
}

constexpr int LDS_BYTES = 163840;
constexpr int OFF_TAG = LDS_BYTES - 16;
constexpr int C_ROW = 272;
constexpr int C_SCR = 60 * C_ROW;
constexpr int C_KCH = 4608;
constexpr int C_UCP = 4160, C_UCH = 2 * C_UCP;
constexpr int F_OFF_P = 4 * C_SCR;
constexpr int F_OFF_S = F_OFF_P + 9216;
constexpr int F_OFF_K = 74752;
constexpr int F_OFF_U = F_OFF_K + 4 * C_KCH;
constexpr int F_OFF_L = F_OFF_U + 4 * C_UCH;
constexpr int LN_RS = 1344;
constexpr int G1_STG = 20480, G1_NS = 7, G1_NK = 20;
constexpr int G2_STG = 36864, G2_NK = 12;
constexpr int G2_OFF_H = G2_STG * 4;
static_assert(F_OFF_L + 16384 <= OFF_TAG && G1_STG * G1_NS <= OFF_TAG && G2_OFF_H + 10240 <= OFF_TAG, "LDS");

__device__ __forceinline__ void ln1_task(const Args& A, int task, u32 lds0, int tid, int lane, int wave) {
  char* uT = A.ws + WS_UT;
  const int t0 = task * 32;
  const u32 tX = lds0, tU = lds0 + 40960, tS = tU + 32 * LN_RS;
#pragma unroll
  for (int k = 0; k < 5; ++k) {
    const int P = (wave * 5 + k) * 64 + lane, row = P / 80, chk = (P % 80) ^ (row & 15);
    glds16((const char*)A.x + (size_t)(t0 + row) * 1280 + chk * 16, UNI(tX + (wave * 5 + k) * 1024));
  }
  if (task == 0 || task == 63) {
    const int off = task == 0 ? 0 : 4128;
    for (int k = tid; k < 1280; k += 512) st16d(uT + (size_t)(k >> 1) * UT_ROW + off + (k & 1) * 16, (u32x4){0u, 0u, 0u, 0u});
  }
  WAIT_VM(0);
  BARRIER();
  STAMP(1);
  {
    const int rt = wave & 1, kq = wave >> 1, row = rt * 16 + (lane & 15), g = lane >> 4;
    f32x4 s1 = {0.f, 0.f, 0.f, 0.f}, s2 = s1;
#pragma unroll
    for (int k = 0; k < 5; ++k) {
      const bf16x8 f = *LDS(bf16x8, tX + row * 1280 + (((kq * 5 + k) * 4 + g) ^ (row & 15)) * 16);
      s1 = mfma16(ONES_BF16, f, s1);
      s2 = mfma16(f, f, s2);
    }
    if (g == ((lane & 15) >> 2)) *LDS(f32x2, tS + (kq * 32 + rt * 16 + (lane & 15)) * 8) = (f32x2){s1[0], diag16(s2, lane)};
  }
  WAIT_LGKM0();
  BARRIER();
  {
    const int row = 4 * wave + (lane >> 4);
    const u32 sa = tS + row * 8;
    const f32x2 sv = *LDS(f32x2, sa) + *LDS(f32x2, sa + 256) + *LDS(f32x2, sa + 512) + *LDS(f32x2, sa + 768);
    u32x4 w[5];
#pragma unroll
    for (int k = 0; k < 5; ++k) w[k] = *LDS(u32x4, tX + row * 1280 + (((lane & 15) + 16 * k) ^ (row & 15)) * 16);
    const float mean = sv.x * (1.f / D);
    const float var = fmaxf(sv.y * (1.f / D) - mean * mean, 0.f);
    const float mb = rbf(mean), sd = rbf(__builtin_amdgcn_sqrtf(rbf(rbf(var) + 1e-5f)));
    const float ri = __builtin_amdgcn_rcpf(sd), mi = -mb * ri;
    if ((lane & 15) == 0)
      __hip_atomic_store((gu64*)((u64*)(A.ws + WS_LN) + t0 + row), ((u64)__float_as_uint(mb) << 32) | __float_as_uint(sd), RLX_AGENT);
#pragma unroll
    for (int k = 0; k < 5; ++k) {
      u32x4 o;
#pragma unroll
      for (int e = 0; e < 4; ++e) o[e] = pack_f16(fmaf(lo_bf(w[k][e]), ri, mi), fmaf(hi_bf(w[k][e]), ri, mi));
      *LDS(u32x4, tU + row * LN_RS + ((lane & 15) + 16 * k) * 16) = o;
    }
  }
  WAIT_LGKM0();
  BARRIER();
  STAMP(2);
  {
    const int g = lane >> 4, q = (lane & 15) >> 2, p = lane & 3;
    u32x4 ov[5];
#pragma unroll
    for (int k = 0; k < 5; ++k) {
      const u32 a = tU + (8 * g + q) * LN_RS + ((wave * 5 + k) * 16 + 4 * p) * 2;
      ov[k] = tr_read2(a, a + 4 * LN_RS);
    }
#pragma unroll
    for (int k = 0; k < 5; ++k) st16d(uT + (size_t)((wave * 5 + k) * 16 + (lane & 15)) * UT_ROW + 32 + (t0 + 8 * g) * 2, ov[k]);
  }
  WAIT_VM(0);
  BARRIER();
  if (tid == 0) set_flag((u32*)(A.ws + WS_FL) + FL_U + task * 32, *LDS(u32, lds0 + OFF_TAG));
  STAMP(3);
}

__device__ __forceinline__ void weight_task(const Args& A, int wtask, u32 lds0, int tid, int lane, int wave) {
  const int g = lane >> 4, q = (lane & 15) >> 2, p = lane & 3;
  if (wtask < 48) {
    const u16* W = wtask < 24 ? A.Wg : A.Wu;
    const int c0 = (wtask % 24) * 64;
    {
      const int row = 8 * wave + (lane >> 3), chk = (lane & 7) ^ ((row & 3) << 1);
      const char* src = (const char*)W + (size_t)row * 3072 + c0 * 2 + chk * 16;
#pragma unroll
      for (int jj = 0; jj < 10; ++jj) glds16(src + (size_t)jj * 64 * 3072, UNI(lds0 + (wave + 8 * jj) * 1024));
    }
    WAIT_VM(0);
    BARRIER();
    const int tl = wave & 3, ks0 = (wave >> 2) * 10;
    const u32 a = lds0 + (32 * ks0 + 8 * g + q) * 128 + (((2 * tl + (p >> 1)) ^ (q << 1)) * 16) + 8 * (p & 1);
    bf16x8 fr[10];
    f32x4 cs = {0.f, 0.f, 0.f, 0.f};
#pragma unroll
    for (int k = 0; k < 10; ++k) { fr[k] = tr_frag(a + k * 4096, a + k * 4096 + 512); cs = mfma16(ONES_BF16, fr[k], cs); }
    WAIT_LGKM0();
    BARRIER();
    if (lane < 16) *LDS(float, lds0 + (wave * 16 + lane) * 4) = cs[0];
    WAIT_LGKM0();
    BARRIER();
    const float cm = (*LDS(float, lds0 + (tl * 16 + (lane & 15)) * 4) + *LDS(float, lds0 + ((tl + 4) * 16 + (lane & 15)) * 4)) * (1.f / D);
    const int f = c0 + 16 * tl, pn = f / 96, fi = (f % 96) / 16;
    const int slot = (fi / 3) * 6 + (wtask < 24 ? 0 : 3) + fi % 3;
    char* dst = A.ws + WS_W1 + (((size_t)pn * 20 + ks0) * 12 + slot) * 1024 + lane * 16;
#pragma unroll
    for (int k = 0; k < 10; ++k) {
      const u32x4 w = __builtin_bit_cast(u32x4, fr[k]);
      u32x4 o;
#pragma unroll
      for (int e = 0; e < 4; ++e) o[e] = pack_bf16(lo_bf(w[e]) - cm, hi_bf(w[e]) - cm);
      st16d(dst + (size_t)k * 12288, o);
    }
  } else {
    const int ks = wtask - 48;
#pragma unroll
    for (int jj = 0; jj < 5; ++jj) {
      const int P = 64 * (wave + 8 * jj) + lane, row = P / 80, chk = P % 80;
      glds16((const char*)A.Wd + (size_t)(32 * ks + row) * 1280 + chk * 16, UNI(lds0 + (wave + 8 * jj) * 1024));
    }
    WAIT_VM(0);
    BARRIER();
#pragma unroll
    for (int jj = 0; jj < 5; ++jj) {
      const int fr = wave + 8 * jj;
      const u32 a = lds0 + (8 * g + q) * 1280 + (16 * fr + 4 * p) * 2;
      st16d(A.ws + WS_WD + (((size_t)(fr / 5) * 48 + ks) * 5 + fr % 5) * 1024 + lane * 16, tr_read2(a, a + 4 * 1280));
    }
  }
  WAIT_VM(0);
  BARRIER();
  if (tid == 0) set_flag((u32*)(A.ws + WS_FL) + FL_W + wtask * 32, *LDS(u32, lds0 + OFF_TAG));
}

struct cplx { f32x2 v, r; };
__device__ __forceinline__ cplx mk(f32x2 v) { return cplx{v, (f32x2){-v.y, v.x}}; }
__device__ __forceinline__ f32x2 cmul(f32x2 a, const cplx& w) {
  const f32x2 t = (f32x2){a.x, a.x} * w.v;
  return __builtin_elementwise_fma((f32x2){a.y, a.y}, w.r, t);
}
__device__ __forceinline__ f32x2 cexp(float zr, float zi, float l, float sgn) {
  const float m = __builtin_amdgcn_exp2f(zr * l * 1.44269504088896341f);
  const float a = __builtin_amdgcn_fractf(zi * l * 0.15915494309189535f);
  return (f32x2){m * __builtin_amdgcn_cosf(a), sgn * m * __builtin_amdgcn_sinf(a)};
}
__device__ __forceinline__ u32 pkh(f32x2 v) { return __builtin_bit_cast(u32, __builtin_convertvector(v, f16x2)); }
__device__ __forceinline__ u32 cmulh(u32 a, u32 b) {
  u32 t, r;
  asm("v_pk_mul_f16 %0, %1, %2 op_sel:[0,0] op_sel_hi:[0,1]" : "=v"(t) : "v"(a), "v"(b));
  asm("v_pk_fma_f16 %0, %1, %2, %3 op_sel:[1,1,0] op_sel_hi:[1,0,1] neg_lo:[0,1,0]" : "=v"(r) : "v"(a), "v"(b), "v"(t));
  return r;
}
#define SET_M0(base) asm volatile("s_mov_b32 m0, %0\n\ts_nop 0" ::"s"(base) : "memory")
#define WR4(row, a, b, c, d)                                                                                    \
  asm volatile("ds_write_addtid_b32 %0 offset:%4\n\tds_write_addtid_b32 %1 offset:%4+272\n\t"                    \
               "ds_write_addtid_b32 %2 offset:%4+544\n\tds_write_addtid_b32 %3 offset:%4+816"                    \
               ::"v"(a), "v"(b), "v"(c), "v"(d), "n"((row) * C_ROW) : "memory")

__device__ __forceinline__ void conv_role(const Args& A, int grp, u32 lds0, int tid, int lane, int wave, u32 tag) {
  const int ch = wave & 3, hf = wave >> 2, d0 = grp * 4, d = d0 + ch;
  const u32 sb = UNI(lds0 + ch * C_SCR);
  const int r32 = lane & 31, h32 = lane >> 5;
  const int j = lane & 15, g = lane >> 4;
  {
    const u64 q0 = (u64)A.Are, q1 = (u64)A.Aim, q2 = (u64)A.Bre, q3 = (u64)A.Bim, q4 = (u64)A.Cre, q5 = (u64)A.Cim;
#pragma unroll
    for (int rep = 0; rep < 2; ++rep) {
      if (rep == 1 && wave != 0) break;
      const int i = rep ? 8 : wave;
      const int b = 1024 * i + 16 * lane, a = b / 1536, o = b - a * 1536;
      u64 src = +q0;
      src = a == 1 ? +q1 : src; src = a == 2 ? +q2 : src; src = a == 3 ? +q3 : src; src = a == 4 ? +q4 : src; src = a == 5 ? +q5 : src;
      glds16((const char*)(src + (u64)d0 * 384 + o), UNI(lds0 + F_OFF_P + i * 1024));
    }
  }
  const u32 wdt = ((const u32*)A.logdt)[d >> 1], wds = ((const u32*)A.Dskip)[d >> 1];
  const float dt = rbf(__expf((d & 1) ? hi_bf(wdt) : lo_bf(wdt)));
  WAIT_VM(0);
  BARRIER();
  float zr[3], zi[3];
  const u32 pl = lds0 + F_OFF_P + ch * 384 + lane * 2;
#pragma unroll
  for (int r = 0; r < 3; ++r) {
    zr[r] = dt * bf2f(*LDS(u16, pl + r * 128));
    zi[r] = dt * bf2f(*LDS(u16, pl + 1536 + r * 128));
  }
  u32 an[3][11];
  if (hf == 0) {
    f32x2 cb[3];
    float bound = 0.f;
#pragma unroll
    for (int r = 0; r < 3; ++r) {
      const float br = dt * bf2f(*LDS(u16, pl + 2 * 1536 + r * 128)), bi = dt * bf2f(*LDS(u16, pl + 3 * 1536 + r * 128));
      const float cr = bf2f(*LDS(u16, pl + 4 * 1536 + r * 128)), ci = bf2f(*LDS(u16, pl + 5 * 1536 + r * 128));
      cb[r] = (f32x2){cr * br - ci * bi, -(cr * bi + ci * br)};
      const float grow = __builtin_amdgcn_exp2f(fmaxf(zr[r], 0.f) * (2064.f * 1.44269504f));
      bound = fmaxf(bound, (fabsf(cb[r].x) + fabsf(cb[r].y)) * grow);
    }
    bound = wave_max(bound);
    int ex = (int)((__float_as_uint(bound) >> 23) & 255);
    ex = ex < 20 ? 20 : (ex > 240 ? 240 : ex);
    const float scale = __uint_as_float((u32)(267 - ex) << 23);
    if (lane == 0) *LDS(float, lds0 + F_OFF_S + ch * 4) = __uint_as_float((u32)(ex - 5) << 23);
#pragma unroll
    for (int r = 0; r < 3; ++r) {
      const cplx w1 = mk(cexp(zr[r], zi[r], 1.f, -1.f));
      const cplx w512 = mk(cexp(zr[r], zi[r], 512.f, -1.f));
      const cplx w2 = mk(cmul(w1.v, w1));
      const f32x2 w3 = cmul(w2.v, w1);
      const cplx w4 = mk(cmul(w2.v, w2));
      const f32x2 w1024 = cmul(w512.v, w512), w1536 = cmul(w1024, w512);
      const f32x2 P0 = cb[r] * scale, P4 = cmul(P0, w4), P8 = cmul(P4, w4), P12 = cmul(P8, w4), P16 = cmul(P12, w4);
      an[r][0] = pkh(P0); an[r][1] = pkh(P4); an[r][2] = pkh(P8); an[r][3] = pkh(P12); an[r][4] = pkh(P16);
      an[r][5] = pkh(w1.v); an[r][6] = pkh(w2.v); an[r][7] = pkh(w3);
      an[r][8] = pkh(w512.v); an[r][9] = pkh(w1024); an[r][10] = pkh(w1536);
    }
  } else {
#pragma unroll
    for (int r = 0; r < 3; ++r) {
      const cplx w16 = mk(cexp(zr[r], zi[r], 16.f, 1.f));
      const cplx w128 = mk(cexp(zr[r], zi[r], 128.f, 1.f));
      const cplx w32 = mk(cmul(w16.v, w16));
      const f32x2 w48 = cmul(w32.v, w16);
      const f32x2 w64 = cmul(w32.v, w32);
      const f32x2 w192 = cmul(w64, w128), w256 = cmul(w128.v, w128);
      const f32x2 w320 = cmul(w192, w128), w384 = cmul(w256, w128), w448 = cmul(w320, w128);
      an[r][0] = pkh(w64); an[r][1] = pkh(w128.v); an[r][2] = pkh(w192); an[r][3] = pkh(w256);
      an[r][4] = pkh(w320); an[r][5] = pkh(w384); an[r][6] = pkh(w448);
      an[r][7] = pkh(w16.v); an[r][8] = pkh(w32.v); an[r][9] = pkh(w48); an[r][10] = 0x00003c00u;
    }
  }
  u32 arow;
  {
    const int hr = (r32 >> 2) & 1, rho = (r32 & 3) + 4 * (r32 >> 3);
    const int row = rho <= 8 ? rho + 8 * hr : rho <= 11 ? rho + 8 + 2 * hr : rho <= 13 ? rho + 10 + hr : rho + 11 + hr;
    arow = sb + row * C_ROW + h32 * 16 + hf * 128;
  }
  const u32 brow = sb + (28 + r32) * C_ROW + h32 * 16 + hf * 128;
  f32x16 kacc;
#pragma unroll
  for (int i = 0; i < 16; ++i) kacc[i] = 0.f;
  SET_M0(sb);
#pragma unroll
  for (int r = 0; r < 3; ++r) {
    u32 gv[2][4];
    auto grp4 = [&](int gi, u32 (&o)[4]) {
      const u32* a = an[r];
      if (hf == 0) {
        if (gi < 4) { o[0] = a[gi]; o[1] = cmulh(a[gi], a[5]); o[2] = cmulh(a[gi], a[6]); o[3] = cmulh(a[gi], a[7]); }
        else if (gi == 4) { o[0] = a[4]; o[1] = cmulh(a[0], a[8]); o[2] = cmulh(a[1], a[8]); o[3] = cmulh(a[2], a[8]); }
        else if (gi == 5) { o[0] = cmulh(a[3], a[8]); o[1] = cmulh(a[4], a[8]); o[2] = cmulh(a[0], a[9]); o[3] = cmulh(a[2], a[9]); }
        else if (gi == 6) { o[0] = cmulh(a[4], a[9]); o[1] = cmulh(a[0], a[10]); o[2] = cmulh(a[2], a[10]); o[3] = cmulh(a[4], a[10]); }
      } else {
        if (gi == 0) { o[0] = a[10]; o[1] = a[7]; o[2] = a[8]; o[3] = a[9]; }
        else { o[0] = a[gi - 1]; o[1] = cmulh(a[gi - 1], a[7]); o[2] = cmulh(a[gi - 1], a[8]); o[3] = cmulh(a[gi - 1], a[9]); }
      }
    };
    if (hf == 0) {
      grp4(0, gv[0]);
#pragma unroll
      for (int gi = 0; gi < 7; ++gi) {
        if (gi < 6) grp4(gi + 1, gv[(gi + 1) & 1]);
        SB();
        WR4(4 * gi, gv[gi & 1][0], gv[gi & 1][1], gv[gi & 1][2], gv[gi & 1][3]);
        SB();
      }
    } else {
      grp4(0, gv[0]);
#pragma unroll
      for (int gi = 0; gi < 8; ++gi) {
        if (gi < 7) grp4(gi + 1, gv[(gi + 1) & 1]);
        SB();
        WR4(28 + 4 * gi, gv[gi & 1][0], gv[gi & 1][1], gv[gi & 1][2], gv[gi & 1][3]);
        SB();
      }
    }
    WAIT_LGKM0();
    BARRIER();
    f16x8 fa[4], fb[4];
#pragma unroll
    for (int k = 0; k < 4; ++k) { fa[k] = *LDS(f16x8, arow + k * 32); fb[k] = *LDS(f16x8, brow + k * 32); }
    WAIT_LGKM0();
    BARRIER();
#pragma unroll
    for (int k = 0; k < 4; ++k) kacc = __builtin_amdgcn_mfma_f32_32x32x16_f16(fa[k], fb[k], kacc, 0, 0, 0);
  }
  STAMP(1);
  {
    const u32 ex = sb + hf * 4096 + lane * 16, exr = sb + (hf ^ 1) * 4096 + lane * 16;
    if (hf == 0) {
      *LDS(f32x4, ex) = (f32x4){kacc[12], kacc[13], kacc[14], kacc[15]};
    } else {
      *LDS(f32x4, ex) = (f32x4){kacc[0], kacc[1], kacc[2], kacc[3]};
      *LDS(f32x4, ex + 1024) = (f32x4){kacc[4], kacc[5], kacc[6], kacc[7]};
      *LDS(f32x4, ex + 2048) = (f32x4){kacc[8], kacc[9], kacc[10], kacc[11]};
    }
    WAIT_LGKM0();
    BARRIER();
    const u32 kb = lds0 + F_OFF_K + ch * C_KCH + 4080 - 32 * r32 - 16 * h32;
    auto put = [&](int base, float e0, float e1, float e2, float e3, float e4, float e5, float e6, float e7) {
      *LDS(u32x4, kb - 2 * base) = (u32x4){pack_f16(e7, e6), pack_f16(e5, e4), pack_f16(e3, e2), pack_f16(e1, e0)};
    };
    if (hf == 0) {
      const f32x4 o0 = *LDS(f32x4, exr), o1 = *LDS(f32x4, exr + 1024), o2 = *LDS(f32x4, exr + 2048);
      float v[12];
#pragma unroll
      for (int i = 0; i < 4; ++i) {
        v[i] = (kacc[i] + o0[i]) * (1.f / 256.f); v[4 + i] = (kacc[4 + i] + o1[i]) * (1.f / 256.f); v[8 + i] = (kacc[8 + i] + o2[i]) * (1.f / 256.f);
      }
      const bool far = r32 >= 16;
      put(0, v[0], far ? v[0] : v[1], v[2], far ? v[4] : v[3], v[4], far ? v[4] : v[5], v[6], far ? v[8] : v[7]);
      put(512, v[9], v[9], v[9], v[10], v[10], v[10], v[11], v[11]);
    } else {
      const f32x4 o = *LDS(f32x4, exr);
      const float v12 = (kacc[12] + o[0]) * (1.f / 256.f), v13 = (kacc[13] + o[1]) * (1.f / 256.f),
                  v14 = (kacc[14] + o[2]) * (1.f / 256.f), v15 = (kacc[15] + o[3]) * (1.f / 256.f);
      put(1024, v12, v12, v12, v12, h32 ? v13 : v12, v13, v13, v13);
      put(1536, v14, v14, v14, v14, h32 ? v15 : v14, v15, v15, v15);
    }
  }
  if (hf == 1 && lane < 32) *LDS(u32x4, lds0 + F_OFF_K + ch * C_KCH + 4096 + lane * 16) = (u32x4){0u, 0u, 0u, 0u};
  STAMP(2);
  if (wave == 7) wait_flags<1>((const u32*)(A.ws + WS_FL) + FL_U, 64, tag, lane);
  WAIT_LGKM0();
  BARRIER();
  STAMP(3);
  {
    const char* up = A.ws + WS_UT + (size_t)d * UT_ROW + lane * 16 + hf * 2;
    const u32 dst = UNI(lds0 + F_OFF_U + ch * C_UCH + hf * C_UCP);
#pragma unroll
    for (int k = 0; k < 4; ++k) glds16d(up + k * 1024, dst + k * 1024);
    if (lane < 2) glds16d(up + 4096, dst + 4096);
    glds16d(A.ws + WS_LN + wave * 2048 + lane * 16, UNI(lds0 + F_OFF_L + wave * 2048));
    glds16d(A.ws + WS_LN + wave * 2048 + 1024 + lane * 16, UNI(lds0 + F_OFF_L + wave * 2048 + 1024));
  }
  WAIT_VM(2);
  BARRIER();
  STAMP(4);
  const int cc = (1 + j) & 1;
  const u32 aa = lds0 + F_OFF_U + ch * C_UCH + cc * C_UCP + 2 * (1 + j + 8 * g - cc) + hf * 256;
  const u32 ba = lds0 + F_OFF_K + ch * C_KCH + 32 * j + 16 * g + hf * 256;
  f32x4 acc[8];
#pragma unroll
  for (int i = 0; i < 8; ++i) acc[i] = (f32x4){0.f, 0.f, 0.f, 0.f};
  {
    f16x8 Af[2][8], Bf[2][8];
    auto rd = [&](int set, int r) {
#pragma unroll
      for (int m = 0; m < 8; ++m) {
        const LAS u32* ap = LDS(const u32, aa + 64 * r + 512 * m);
        Af[set][m] = __builtin_bit_cast(f16x8, (u32x4){ap[0], ap[1], ap[2], ap[3]});
        Bf[set][m] = *LDS(f16x8, ba + 64 * r + 512 * (7 - m));
      }
    };
    auto mm = [&](int set) {
#pragma unroll
      for (int sdx = 0; sdx < 8; ++sdx)
#pragma unroll
        for (int m = 0; m <= sdx; ++m) acc[sdx] = __builtin_amdgcn_mfma_f32_16x16x32_f16(Af[set][m], Bf[set][sdx - m], acc[sdx], 0, 0, 0);
    };
#define C_SCHED() do { _Pragma("unroll") for (int k_ = 0; k_ < 24; ++k_) { SG(0x008, 1); SG(0x100, 1); } SG(0x008, 12); } while (0)
    rd(0, 0);
    SB(); rd(1, 1); mm(0); C_SCHED(); SB();
    rd(0, 2); mm(1); C_SCHED(); SB();
    rd(1, 3); mm(0); C_SCHED(); SB();
    mm(1);
  }
  STAMP(5);
  {
    const u32 ex = lds0 + wave * 4096 + lane * 16, exr = lds0 + (wave ^ 4) * 4096 + lane * 16;
    if (hf == 0) {
#pragma unroll
      for (int i = 0; i < 4; ++i) *LDS(f32x4, ex + i * 1024) = acc[4 + i];
    } else {
#pragma unroll
      for (int i = 0; i < 4; ++i) *LDS(f32x4, ex + i * 1024) = acc[i];
    }
    WAIT_VM(0);
    WAIT_LGKM0();
    BARRIER();
    const float it = *LDS(float, lds0 + F_OFF_S + ch * 4);
    const float dsk = (d & 1) ? hi_bf(wds) : lo_bf(wds);
    u32x2 hv[4];
#pragma unroll
    for (int i = 0; i < 4; ++i) {
      const int t = 16 * (16 * (4 * hf + i) + 15 - j) + 4 * g;
      const u32x2 uw = *LDS(u32x2, lds0 + F_OFF_U + ch * C_UCH + 2 * (16 + t));
      const u32 w0 = uw[0], w1 = uw[1];
      const f16x2 u01 = __builtin_bit_cast(f16x2, w0), u23 = __builtin_bit_cast(f16x2, w1);
      const float uf[4] = {(float)u01[0], (float)u01[1], (float)u23[0], (float)u23[1]};
      const u32 d01 = pack_bf16(uf[0] * dsk, uf[1] * dsk), d23 = pack_bf16(uf[2] * dsk, uf[3] * dsk);
      const f32x4 l01 = *LDS(f32x4, lds0 + F_OFF_L + t * 8), l23 = *LDS(f32x4, lds0 + F_OFF_L + t * 8 + 16);
      const u32 x01 = pack_bf16(fmaf(uf[0], l01[0], l01[1]), fmaf(uf[1], l01[2], l01[3]));
      const u32 x23 = pack_bf16(fmaf(uf[2], l23[0], l23[1]), fmaf(uf[3], l23[2], l23[3]));
      const f32x4 o = *LDS(f32x4, exr + i * 1024);
      const f32x4 a0 = acc[i], a1 = acc[4 + i];
      const f32x4 y = ((hf ? +a1 : +a0) + o) * it;
      const u32 y01 = pack_bf16(y[0], y[1]), y23 = pack_bf16(y[2], y[3]);
      const u32 s01 = pack_bf16(lo_bf(y01) + lo_bf(d01), hi_bf(y01) + hi_bf(d01));
      const u32 s23 = pack_bf16(lo_bf(y23) + lo_bf(d23), hi_bf(y23) + hi_bf(d23));
      hv[i] = (u32x2){pack_bf16(lo_bf(x01) + lo_bf(s01), hi_bf(x01) + hi_bf(s01)), pack_bf16(lo_bf(x23) + lo_bf(s23), hi_bf(x23) + hi_bf(s23))};
    }
    char* hrow = A.ws + WS_HT + (size_t)d * 4096;
#pragma unroll
    for (int i = 0; i < 4; i += 2) st16d(hrow + (16 * (16 * (4 * hf + i + (g & 1)) + 15 - j) + 8 * (g >> 1)) * 2, widen16(hv[i], hv[i + 1]));
  }
  WAIT_VM(0);
  BARRIER();
  if (tid == 0) set_flag((u32*)(A.ws + WS_FL) + FL_H + grp * 32, *LDS(u32, lds0 + OFF_TAG));
  STAMP(6);
}

__device__ __forceinline__ void gemm1_phase(const Args& A, u32 lds0, int tid, int lane, int wave, u32 tag) {
  const int xcd = blockIdx.x & 7, idx = blockIdx.x >> 3;
  const int pm = (xcd & 1) * 8 + (idx & 7), pn = (xcd >> 1) * 4 + (idx >> 3);
  const int t0 = pm * 128;
  const u32* flags = (const u32*)(A.ws + WS_FL);
  if (wave >= 4) {
    const int lw = wave - 4;
    int kH = 5 * (pn & 3), kW = kH;
    const int row = 8 * lw + (lane >> 4);
    const int chk = (lane & 15) ^ (((row & 3) << 1) | (((row >> 3) & 1) << 3));
    const char* srcH = A.ws + WS_HT + (size_t)(row + 32 * kH) * 4096 + t0 * 2 + chk * 16;
    const char* srcW = A.ws + WS_W1 + (size_t)pn * 245760 + (size_t)kW * 12288 + lw * 3072 + lane * 16;
    const u32 dH = lds0 + lw * 2048, dW = lds0 + 8192 + lw * 3072;
    auto issueH = [&](u32 so) {
      glds16d(srcH, dH + so);
      glds16d(srcH + 4 * 4096, dH + so + 1024);
      if (++kH == G1_NK) { kH = 0; srcH -= (size_t)19 * 32 * 4096; } else srcH += 32 * 4096;
    };
    auto issueW = [&](u32 so) {
      glds16d(srcW, dW + so);
      glds16d(srcW + 1024, dW + so + 1024);
      glds16d(srcW + 2048, dW + so + 2048);
      if (++kW == G1_NK) { kW = 0; srcW -= 19 * 12288; } else srcW += 12288;
    };
    if (wave == 4) wait_flags<1>(flags + FL_W, 48, tag, lane);
    BARRIER();
#pragma unroll
    for (int s = 0; s < G1_NS; ++s) issueW(s * G1_STG);
    if (wave == 4) wait_flags<3>(flags + FL_H, 160, tag, lane);
    BARRIER();
    STAMP(7);
#pragma unroll
    for (int s = 0; s < G1_NS; ++s) issueH(s * G1_STG);
    WAIT_VM(12); BARRIER();
    WAIT_VM(10); BARRIER();
    WAIT_VM(8); BARRIER();
    u32 so = 0;
    auto issue = [&]() { issueH(so); issueW(so); so = so == (G1_NS - 1) * G1_STG ? 0 : so + G1_STG; };
    issue(); WAIT_VM(11); BARRIER();
    issue(); WAIT_VM(14); BARRIER();
    issue(); WAIT_VM(17); BARRIER();
    for (int i = 5; i < G1_NK - 5; ++i) {
      issue();
      WAIT_VM(20); BARRIER();
    }
    WAIT_VM(15); BARRIER(); WAIT_VM(10); BARRIER(); WAIT_VM(5); BARRIER(); WAIT_VM(0); BARRIER();
    return;
  }
  const int wt = wave & 1, wf = wave >> 1;
  const int g = lane >> 4, q = (lane & 15) >> 2, p = lane & 3;
  const int rot = pn >> 2;
  const u32 hrow = (8 * g + q) * 256 + 8 * (p & 1);
  const u32 hx = q | ((g & 1) << 2);
  u32 hc[4];
#pragma unroll
  for (int j = 0; j < 4; ++j) hc[j] = lds0 + hrow + ((((u32)(4 * wt + ((j + rot) & 3)) ^ hx) << 1) | (p >> 1)) * 16;
  u32 wc = lds0 + 8192 + wf * 6144 + lane * 16, so = 0;
  f32x4 acc[6][4];
#pragma unroll
  for (int i = 0; i < 6; ++i)
#pragma unroll
    for (int j = 0; j < 4; ++j) acc[i][j] = (f32x4){0.f, 0.f, 0.f, 0.f};
  f32x4 st1 = {0.f, 0.f, 0.f, 0.f}, st2 = st1;
  bf16x8 hf[2][4], wfr[2][6];
  auto rd = [&](int set) {
    wfr[set][0] = *LDS(bf16x8, wc);
#pragma unroll
    for (int j = 0; j < 4; ++j) hf[set][j] = tr_frag(hc[j], hc[j] + 1024);
#pragma unroll
    for (int i = 1; i < 6; ++i) wfr[set][i] = *LDS(bf16x8, wc + i * 1024);
  };
  auto adv = [&]() {
    u32 dl = so == (G1_NS - 1) * G1_STG ? (u32)(-(G1_NS - 1) * G1_STG) : (u32)G1_STG;
    so += dl;
    asm volatile("" : "+s"(dl));
#pragma unroll
    for (int j = 0; j < 4; ++j) hc[j] += dl;
    wc += dl;
  };
  auto mma = [&](int set) {
#pragma unroll
    for (int i = 0; i < 6; ++i)
#pragma unroll
      for (int j = 0; j < 4; ++j) acc[i][j] = mfma16(wfr[set][i], hf[set][j], acc[i][j]);
  };
  auto stat = [&](int set) {
    st1 = mfma16(ONES_BF16, hf[set][0], st1);
    st2 = mfma16(hf[set][0], hf[set][0], st2);
  };
#define G1_SCHED(extra) do { \
    SG(0x008, 1); SG(0x100, 1); SG(0x008, 1); SG(0x100, 1); SG(0x008, 1); SG(0x100, 1); SG(0x008, 1); SG(0x100, 1); \
    SG(0x008, 1); SG(0x100, 1); SG(0x008, 1); SG(0x100, 1); SG(0x008, 1); SG(0x100, 1); SG(0x008, 1); SG(0x100, 1); \
    SG(0x008, 1); SG(0x100, 1); SG(0x008, 2); SG(0x100, 1); SG(0x008, 2); SG(0x100, 1); SG(0x008, 2); SG(0x100, 1); \
    SG(0x008, 2); SG(0x100, 1); SG(0x008, 1); SG(0x100, 1); SG(0x008, 1); SG(0x002, 1); SG(0x008, 1); SG(0x002, 1); \
    SG(0x008, 1); SG(0x002, 1); SG(0x008, 1); SG(0x002, 1); SG(0x008, 1); SG(0x002, 1); SG(0x008, 1 + (extra)); } while (0)
  BARRIER();
  BARRIER();
  BARRIER();
  STAMP(8);
  rd(0); adv();
#pragma unroll
  for (int i = 1; i < G1_NK - 1; i += 2) {
    BARRIER();
    SB(); rd(1); mma(0); adv();
    if (i - 1 < 5) { stat(0); G1_SCHED(2); } else { G1_SCHED(0); }
    SB();
    BARRIER();
    SB(); rd(0); mma(1); adv();
    if (i < 5) { stat(1); G1_SCHED(2); } else { G1_SCHED(0); }
    SB();
  }
  BARRIER();
  SB(); rd(1); mma(0); G1_SCHED(0); SB();
  WAIT_LGKM0();
  BARRIER();
#pragma unroll
  for (int i = 0; i < 3; ++i)
#pragma unroll
    for (int j = 0; j < 4; ++j) {
      acc[i][j] = mfma16(wfr[1][i], hf[1][j], acc[i][j]);
      acc[3 + i][j] = mfma16(wfr[1][3 + i], hf[1][j], acc[3 + i][j]);
    }
  STAMP(9);
  {
    char* ob = A.ws + WS_HM + ((size_t)(pm * 8 + 4 * wt) * 48 + 3 * pn) * 1024 + (lane & 15) * 16 + (g >> 1) * 256;
#pragma unroll
    for (int j = 0; j < 4; j += 2) {
      char* oj = ob + (size_t)((j + (g & 1) + rot) & 3) * 49152;
#pragma unroll
      for (int i = 0; i < 3; ++i) {
        u32x2 o[2];
#pragma unroll
        for (int jj = 0; jj < 2; ++jj) {
          const f32x4 gv = acc[i][j + jj], uv = acc[3 + i][j + jj];
          o[jj] = (u32x2){pack_bf16(fmaxf(gv[0], 0.f) * uv[0], fmaxf(gv[1], 0.f) * uv[1]), pack_bf16(fmaxf(gv[2], 0.f) * uv[2], fmaxf(gv[3], 0.f) * uv[3])};
        }
        const int E = 6 * wf + 2 * i;
        st16d(oj + (E >> 2) * 1024 + (E & 3) * 256, widen16(o[0], o[1]));
      }
    }
  }
  if (wf == 0 && g == ((lane & 15) >> 2))
    __hip_atomic_store((gu64*)((u64*)(A.ws + WS_PT) + (size_t)(pn & 3) * L + t0 + (4 * wt + rot) * 16 + (lane & 15)),
                       ((u64)__float_as_uint(diag16(st2, lane)) << 32) | __float_as_uint(st1[0]), RLX_AGENT);
  WAIT_VM(0);
}

__device__ __forceinline__ void gemm2_phase(const Args& A, u32 lds0, int tid, int lane, int wave, u32 tag) {
  const int xcd = blockIdx.x & 7, idx = blockIdx.x >> 3;
  const int pm = (xcd & 3) * 8 + (idx & 7), pn = (xcd >> 2) * 4 + (idx >> 3);
  const int t0 = pm * 64, d0 = pn * 80;
  u32* flags = (u32*)(A.ws + WS_FL);
  if (wave >= 4) {
    const int lw = wave - 4;
    const char* srcP = A.ws + WS_HM + (size_t)(pm * 4 + lw) * 49152 + lane * 16;
    const char* srcW = A.ws + WS_WD + (size_t)pn * 245760 + lw * 5120 + lane * 16;
    const u32 dP = lds0 + lw * 4096, dW = lds0 + 16384 + lw * 5120;
    auto issueP = [&](u32 so) {
      glds16d(srcP, dP + so); glds16d(srcP + 1024, dP + so + 1024); glds16d(srcP + 2048, dP + so + 2048); glds16d(srcP + 3072, dP + so + 3072);
      srcP += 4096;
    };
    auto issueW = [&](u32 so) {
      glds16d(srcW, dW + so); glds16d(srcW + 1024, dW + so + 1024); glds16d(srcW + 2048, dW + so + 2048);
      glds16d(srcW + 3072, dW + so + 3072); glds16d(srcW + 4096, dW + so + 4096);
      srcW += 20480;
    };
    wait_flags<1>(flags + FL_W + 48 * 32, 48, tag, lane);
    BARRIER();
    issueW(0); issueW(G2_STG); issueW(2 * G2_STG); issueW(3 * G2_STG);
#pragma unroll
    for (int k = 0; k < 3; ++k) {
      const int jj = lw + 4 * k < 10 ? lw + 4 * k : lw + 4;
      glds16d(A.ws + WS_HT + (size_t)(d0 + 8 * jj + (lane >> 3)) * 4096 + t0 * 2 + (lane & 7) * 16, UNI(lds0 + G2_OFF_H + jj * 1024));
    }
    BARRIER();
    if (wave == 4) wait_flags<1>(flags + FL_P + (pm >> 1) * 16 * 32, 16, tag, lane);
    BARRIER();
    STAMP(10);
    issueP(0); issueP(G2_STG); issueP(2 * G2_STG); issueP(3 * G2_STG);
    WAIT_VM(12); BARRIER();
    WAIT_VM(8); BARRIER();
    u32 so = 0;
    auto issue = [&]() { issueP(so); issueW(so); so = so == 3 * G2_STG ? 0 : so + G2_STG; };
    issue(); WAIT_VM(13); BARRIER();
    for (int i = 2; i < G2_NK - 3; ++i) {
      issue();
      WAIT_VM(18); BARRIER();
    }
    WAIT_VM(9); BARRIER();
    WAIT_VM(0); BARRIER();
    BARRIER();
    BARRIER();
    return;
  }
  BARRIER();
  if (tid == 0) {
    const int pm1 = (xcd & 1) * 8 + (idx & 7), pn1 = (xcd >> 1) * 4 + (idx >> 3);
    set_flag(flags + FL_P + (pm1 * 16 + pn1) * 32, tag);
  }
  const int wt = wave & 1, wk = wave >> 1;
  u32 pc = lds0 + (wt * 2) * 4096 + wk * 2048 + lane * 16, wc = lds0 + 16384 + wk * 2 * 5120 + lane * 16, so = 0;
  f32x4 acc[5][2];
#pragma unroll
  for (int i = 0; i < 5; ++i) { acc[i][0] = (f32x4){0.f, 0.f, 0.f, 0.f}; acc[i][1] = acc[i][0]; }
  bf16x8 pf[2][2][2], wf[2][2][5];
  auto rd = [&](int set) {
#pragma unroll
    for (int ks = 0; ks < 2; ++ks) {
      wf[set][ks][0] = *LDS(bf16x8, wc + ks * 5120);
#pragma unroll
      for (int j = 0; j < 2; ++j) pf[set][ks][j] = *LDS(bf16x8, pc + j * 4096 + ks * 1024);
#pragma unroll
      for (int i = 1; i < 5; ++i) wf[set][ks][i] = *LDS(bf16x8, wc + ks * 5120 + i * 1024);
    }
  };
  auto adv = [&]() {
    u32 dl = so == 3 * G2_STG ? (u32)(-3 * G2_STG) : (u32)G2_STG;
    so += dl;
    asm volatile("" : "+s"(dl));
    pc += dl; wc += dl;
  };
  auto mma = [&](int set) {
#pragma unroll
    for (int ks = 0; ks < 2; ++ks)
#pragma unroll
      for (int i = 0; i < 5; ++i)
#pragma unroll
        for (int j = 0; j < 2; ++j) acc[i][j] = mfma16(wf[set][ks][i], pf[set][ks][j], acc[i][j]);
  };
#define G2_SCHED() do { _Pragma("unroll") for (int k_ = 0; k_ < 4; ++k_) { SG(0x008, 1); SG(0x100, 1); SG(0x008, 1); SG(0x100, 1); SG(0x008, 1); SG(0x100, 1); SG(0x008, 1); } \
    SG(0x008, 1); SG(0x100, 1); SG(0x008, 1); SG(0x100, 1); SG(0x008, 2); } while (0)
  BARRIER();
  const int tl = wt * 32 + wk * 16;
  u64 pw[4];
#pragma unroll
  for (int k = 0; k < 4; ++k) pw[k] = __hip_atomic_load((gu64*)((u64*)(A.ws + WS_PT) + (size_t)k * L + t0 + tl + (lane & 15)), RLX_AGENT);
  BARRIER();
  STAMP(11);
  rd(0); adv();
  WAIT_LGKM0();
#pragma unroll
  for (int i = 1; i < G2_NK - 1; i += 2) {
    BARRIER();
    SB(); rd(1); mma(0); adv(); G2_SCHED(); SB();
    WAIT_LGKM0();
    BARRIER();
    SB(); rd(0); mma(1); adv(); G2_SCHED(); SB();
    WAIT_LGKM0();
  }
  BARRIER();
  SB(); rd(1); mma(0); G2_SCHED(); SB();
  WAIT_LGKM0();
  mma(1);
  STAMP(12);
  float rr;
  {
    float s1 = 0.f, s2 = 0.f;
#pragma unroll
    for (int k = 0; k < 4; ++k) { s1 += __uint_as_float((u32)pw[k]); s2 += __uint_as_float((u32)(pw[k] >> 32)); }
    const float mean = s1 * (1.f / D);
    const float var = fmaxf(s2 * (1.f / D) - mean * mean, 0.f);
    const float r = __builtin_amdgcn_rcpf(rbf(__builtin_amdgcn_sqrtf(rbf(rbf(var) + 1e-5f))));
    rr = r * r;
  }
  const int g = lane >> 4, q = (lane & 15) >> 2, p = lane & 3;
  s16x4 hv[5];
#pragma unroll
  for (int i = 0; i < 5; ++i) hv[i] = tr_read(lds0 + G2_OFF_H + (16 * i + 4 * g + q) * 128 + (tl + 4 * p) * 2);
  BARRIER();
  const u32 ex = lds0 + (wave ^ 2) * 5120 + lane * 16, exr = lds0 + wave * 5120 + lane * 16;
#pragma unroll
  for (int i = 0; i < 5; ++i) { const f32x4 a0 = acc[i][0], a1 = acc[i][1]; *LDS(f32x4, ex + i * 1024) = wk ? +a0 : +a1; }
  WAIT_LGKM0();
  BARRIER();
  u32x2 o2[5];
#pragma unroll
  for (int i = 0; i < 5; ++i) {
    const f32x4 o = *LDS(f32x4, exr + i * 1024);
    const f32x4 a0 = acc[i][0], a1 = acc[i][1];
    const f32x4 m = ((wk ? +a1 : +a0) + o) * rr;
    const u32 m01 = pack_bf16(m[0], m[1]), m23 = pack_bf16(m[2], m[3]);
    o2[i] = (u32x2){pack_bf16(bf2f((u16)hv[i][0]) + lo_bf(m01), bf2f((u16)hv[i][1]) + hi_bf(m01)),
                    pack_bf16(bf2f((u16)hv[i][2]) + lo_bf(m23), bf2f((u16)hv[i][3]) + hi_bf(m23))};
  }
  char* orow = (char*)A.out + (size_t)(t0 + tl + (lane & 15)) * 1280 + d0 * 2;
  *(u32x4*)(orow + (16 * (0 + (g & 1)) + 8 * (g >> 1)) * 2) = widen16(o2[0], o2[1]);
  *(u32x4*)(orow + (16 * (2 + (g & 1)) + 8 * (g >> 1)) * 2) = widen16(o2[2], o2[3]);
  *(u32x2*)(orow + (64 + 4 * g) * 2) = o2[4];
  STAMP(13);
}

__global__ __launch_bounds__(512, 2) void s4_reglu_block(const Args A) {
  STAMP(0);
  extern __shared__ __attribute__((aligned(16))) char smem[];
  const u32 lds0 = (u32)(size_t)(LAS char*)smem;
  const int tid = threadIdx.x, lane = tid & 63;
  const int wave = __builtin_amdgcn_readfirstlane(tid >> 6);
  const int xcd = blockIdx.x & 7, idx = blockIdx.x >> 3;
  u32* flags = (u32*)(A.ws + WS_FL);
  u32 tag7 = 0;
  if (wave == 7) {
    tag7 = __hip_atomic_load((gu32*)flags, RLX_AGENT) + 1u;
    if (lane == 0) { *LDS(u32, lds0 + OFF_TAG) = tag7; set_flag(flags + FL_STARTED + blockIdx.x, tag7); }
  }
  if (idx < 12) {
    int wtask;
    if (idx < 8) {
      const int task = xcd * 8 + idx;
      ln1_task(A, task, lds0, tid, lane, wave);
      wtask = task < 16 ? 32 + task : 48 + task - 16;
    } else {
      wtask = xcd * 4 + (idx - 8);
    }
    weight_task(A, wtask, lds0, tid, lane, wave);
    STAMP(6);
    if (blockIdx.x == 95 && wave == 7) {
      const u64 t0 = __builtin_amdgcn_s_memrealtime();
      for (;;) {
        bool ok = true;
#pragma unroll
        for (int k = 0; k < 4; ++k) ok &= __hip_atomic_load((gu32*)(flags + FL_STARTED + k * 64 + lane), RLX_AGENT) == tag7;
        if (__all(ok)) { if (lane == 0) set_flag(flags, tag7); break; }
        if (__builtin_amdgcn_s_memrealtime() - t0 > SPIN_TICKS) break;
        __builtin_amdgcn_s_sleep(8);
      }
    }
  } else {
    conv_role(A, xcd * 20 + idx - 12, lds0, tid, lane, wave, tag7);
  }
  WAIT_LGKM0();
  BARRIER();
  const u32 tag = UNI(*LDS(u32, lds0 + OFF_TAG));
  gemm1_phase(A, lds0, tid, lane, wave, tag);
  gemm2_phase(A, lds0, tid, lane, wave, tag);
}

extern "C" void kernel_launch(void* const* d_in, const int* in_sizes, int n_in, void* d_out, int out_size, void* d_ws,
                              size_t ws_size, hipStream_t stream) {
  Args A;
  A.x = (const u16*)d_in[0];
  A.Are = (const u16*)d_in[1]; A.Aim = (const u16*)d_in[2]; A.Bre = (const u16*)d_in[3]; A.Bim = (const u16*)d_in[4];
  A.Cre = (const u16*)d_in[5]; A.Cim = (const u16*)d_in[6]; A.logdt = (const u16*)d_in[7]; A.Dskip = (const u16*)d_in[8];
  A.Wg = (const u16*)d_in[10]; A.Wu = (const u16*)d_in[11]; A.Wd = (const u16*)d_in[12];
  A.out = (u16*)d_out;
  A.ws = (char*)d_ws;
  (void)hipFuncSetAttribute((const void*)s4_reglu_block, hipFuncAttributeMaxDynamicSharedMemorySize, LDS_BYTES);
  s4_reglu_block<<<256, 512, LDS_BYTES, stream>>>(A);
}
```

```cpp
#include <hip/hip_runtime.h>
#include <hip/hip_bf16.h>
#include <stdint.h>

typedef unsigned short u16;
typedef unsigned u32;
typedef unsigned long long u64;
typedef short s16x4 __attribute__((ext_vector_type(4)));
typedef __bf16 bf16x8 __attribute__((ext_vector_type(8)));
typedef __bf16 bf16x2 __attribute__((ext_vector_type(2)));
typedef _Float16 f16x2 __attribute__((ext_vector_type(2)));
typedef _Float16 f16x8 __attribute__((ext_vector_type(8)));
typedef float f32x16 __attribute__((ext_vector_type(16)));
typedef float f32x4 __attribute__((ext_vector_type(4)));
typedef float f32x2 __attribute__((ext_vector_type(2)));
typedef u32 u32x4 __attribute__((ext_vector_type(4)));
typedef u32 u32x2 __attribute__((ext_vector_type(2)));
typedef __attribute__((address_space(1))) u32 gu32;
typedef __attribute__((address_space(1))) u64 gu64;
#define LAS __attribute__((address_space(3)))
#define LDS(T, a) ((LAS T*)(size_t)(a))

constexpr int L = 2048, D = 640, NS = 192, F = 1536;

constexpr size_t WS_UT = 0;
constexpr size_t WS_LN = 4u << 20;
constexpr size_t WS_HT = 8u << 20;
constexpr size_t WS_HM = 12u << 20;
constexpr size_t WS_W1 = 32u << 20;
constexpr size_t WS_WD = 36u << 20;
constexpr size_t WS_PT = 40u << 20;
constexpr size_t WS_FL = 52u << 20;
constexpr size_t WS_DBG = 24u << 20;
constexpr int FL_STARTED = 1024;
constexpr int FL_U = 2048;
constexpr int FL_W = FL_U + 64 * 32;
constexpr int FL_H = FL_W + 96 * 32;
constexpr int FL_P = FL_H + 160 * 32;
constexpr int UT_ROW = 4160;

struct Args {
  const u16 *x, *Are, *Aim, *Bre, *Bim, *Cre, *Cim, *logdt, *Dskip, *Wg, *Wu, *Wd;
  u16* out;
  char* ws;
};

__device__ __forceinline__ float bf2f(u16 v) { return __uint_as_float(((u32)v) << 16); }
__device__ __forceinline__ u32 pack_bf16(float lo, float hi) { return __builtin_bit_cast(u32, __builtin_convertvector((f32x2){lo, hi}, bf16x2)); }
__device__ __forceinline__ u32 pack_f16(float lo, float hi) { return __builtin_bit_cast(u32, __builtin_convertvector((f32x2){lo, hi}, f16x2)); }
__device__ __forceinline__ float rbf(float f) { return __uint_as_float(pack_bf16(f, f) << 16); }
__device__ __forceinline__ float lo_bf(u32 v) { return __uint_as_float(v << 16); }
__device__ __forceinline__ float hi_bf(u32 v) { return __uint_as_float(v & 0xffff0000u); }

#define STAMP(i)
#define WAIT_VM(n) asm volatile("s_waitcnt vmcnt(%0)" ::"n"(n) : "memory")
#define WAIT_LGKM0() do { asm volatile("" ::: "memory"); __builtin_amdgcn_s_waitcnt(0xC07F); asm volatile("" ::: "memory"); } while (0)
#define BARRIER() do { asm volatile("" ::: "memory"); __builtin_amdgcn_s_barrier(); asm volatile("" ::: "memory"); } while (0)
#define SB() __builtin_amdgcn_sched_barrier(0)
#define SG(m, n) __builtin_amdgcn_sched_group_barrier(m, n, 0)
#define RLX_AGENT __ATOMIC_RELAXED, __HIP_MEMORY_SCOPE_AGENT

__device__ __forceinline__ void glds16(const void* gsrc, u32 lds_dst) {
  u32 keep;
  asm volatile("s_mov_b32 %0, m0\n\ts_mov_b32 m0, %2\n\ts_nop 0\n\tglobal_load_lds_dwordx4 %1, off\n\ts_mov_b32 m0, %0"
               : "=&s"(keep) : "v"(gsrc), "s"(lds_dst) : "memory");
}
__device__ __forceinline__ void glds16d(const void* gsrc, u32 lds_dst) {
  u32 keep;
  asm volatile("s_mov_b32 %0, m0\n\ts_mov_b32 m0, %2\n\ts_nop 0\n\tglobal_load_lds_dwordx4 %1, off sc1\n\ts_mov_b32 m0, %0"
               : "=&s"(keep) : "v"(gsrc), "s"(lds_dst) : "memory");
}
#define UNI(v) ((u32)__builtin_amdgcn_readfirstlane((int)(v)))
__device__ __forceinline__ void st16d(void* p, u32x4 v) {
  asm volatile("global_store_dwordx4 %0, %1, off sc1\n\ts_nop 1" :: "v"(p), "v"(v) : "memory");
}
constexpr u64 SPIN_TICKS = 300000;
#define POLL_SLEEP 2
template <int NL>
__device__ __forceinline__ void wait_flags(const u32* flags, int n, u32 tag, int lane) {
  const u64 t0 = __builtin_amdgcn_s_memrealtime();
  for (;;) {
    bool ok = true;
#pragma unroll
    for (int k = 0; k < NL; ++k) {
      const int i = k * 64 + lane;
      ok &= __hip_atomic_load((gu32*)(flags + (i < n ? i : n - 1) * 32), RLX_AGENT) == tag;
    }
    if (__all(ok)) break;
    if (__builtin_amdgcn_s_memrealtime() - t0 > SPIN_TICKS) break;
    __builtin_amdgcn_s_sleep(POLL_SLEEP);
  }
  asm volatile("" ::: "memory");
}
__device__ __forceinline__ void set_flag(u32* flag, u32 tag) { __hip_atomic_store((gu32*)flag, tag, RLX_AGENT); }

__device__ __forceinline__ s16x4 tr_read(u32 addr) { return __builtin_amdgcn_ds_read_tr16_b64_v4i16(LDS(s16x4, addr)); }
__device__ __forceinline__ u32x4 tr_read2(u32 a0, u32 a1) {
  const s16x4 a = tr_read(a0), b = tr_read(a1);
  return __builtin_bit_cast(u32x4, __builtin_shufflevector(a, b, 0, 1, 2, 3, 4, 5, 6, 7));
}
__device__ __forceinline__ bf16x8 tr_frag(u32 a0, u32 a1) { return __builtin_bit_cast(bf16x8, tr_read2(a0, a1)); }
__device__ __forceinline__ f32x4 mfma16(bf16x8 a, bf16x8 b, f32x4 c) { return __builtin_amdgcn_mfma_f32_16x16x32_bf16(a, b, c, 0, 0, 0); }
#define ONES_BF16 __builtin_bit_cast(bf16x8, (u32x4){0x3f803f80u, 0x3f803f80u, 0x3f803f80u, 0x3f803f80u})
__device__ __forceinline__ float diag16(f32x4 a, int lane) { const int r = lane & 3; return r == 0 ? a[0] : r == 1 ? a[1] : r == 2 ? a[2] : a[3]; }

template <int CTRL> __device__ __forceinline__ float dpp_f(float v) {
  return __builtin_bit_cast(float, __builtin_amdgcn_update_dpp(0, __builtin_bit_cast(int, v), CTRL, 0xf, 0xf, true));
}
__device__ __forceinline__ float wave_max(float v) {
  v = fmaxf(v, dpp_f<0xB1>(v));
  v = fmaxf(v, dpp_f<0x4E>(v));
  v = fmaxf(v, dpp_f<0x141>(v));
  v = fmaxf(v, dpp_f<0x140>(v));
  { auto r = __builtin_amdgcn_permlane16_swap(__float_as_uint(v), __float_as_uint(v), false, false); v = fmaxf(__uint_as_float(r[0]), __uint_as_float(r[1])); }
  { auto r = __builtin_amdgcn_permlane32_swap(__float_as_uint(v), __float_as_uint(v), false, false); v = fmaxf(__uint_as_float(r[0]), __uint_as_float(r[1])); }
  return v;
}
__device__ __forceinline__ u32x4 widen16(u32x2 a, u32x2 b) {
  const auto s0 = __builtin_amdgcn_permlane16_swap(a[0], b[0], false, false);
  const auto s1 = __builtin_amdgcn_permlane16_swap(a[1], b[1], false, false);
  return (u32x4){s0[0], s1[0], s0[1], s1[1]};
}

constexpr int LDS_BYTES = 163840;
constexpr int OFF_TAG = LDS_BYTES - 16;
constexpr int C_ROW = 272;
constexpr int C_SCR = 60 * C_ROW;
constexpr int C_KCH = 4608;
constexpr int C_UCP = 4160, C_UCH = 2 * C_UCP;
constexpr int F_OFF_P = 4 * C_SCR;
constexpr int F_OFF_S = F_OFF_P + 9216;
constexpr int F_OFF_K = 74752;
constexpr int F_OFF_U = F_OFF_K + 4 * C_KCH;
constexpr int F_OFF_L = F_OFF_U + 4 * C_UCH;
constexpr int LN_RS = 1344;
constexpr int G1_STG = 20480, G1_NS = 7, G1_NK = 20;
constexpr int G2_STG = 36864, G2_NK = 12;
constexpr int G2_OFF_H = G2_STG * 4;
static_assert(F_OFF_L + 16384 <= OFF_TAG && G1_STG * G1_NS <= OFF_TAG && G2_OFF_H + 10240 <= OFF_TAG, "LDS");

__device__ __forceinline__ void ln1_task(const Args& A, int task, u32 lds0, int tid, int lane, int wave) {
  char* uT = A.ws + WS_UT;
  const int t0 = task * 32;
  const u32 tX = lds0, tU = lds0 + 40960;
#pragma unroll
  for (int k = 0; k < 5; ++k) {
    const int P = (wave * 5 + k) * 64 + lane, row = P / 80, chk = (P % 80) ^ (row & 15);
    glds16((const char*)A.x + (size_t)(t0 + row) * 1280 + chk * 16, UNI(tX + (wave * 5 + k) * 1024));
  }
  if (task == 0 || task == 63) {
    const int off = task == 0 ? 0 : 4128;
    for (int k = tid; k < 1280; k += 512) st16d(uT + (size_t)(k >> 1) * UT_ROW + off + (k & 1) * 16, (u32x4){0u, 0u, 0u, 0u});
  }
  WAIT_VM(0);
  BARRIER();
  STAMP(1);
  {
    const int row = 4 * wave + (lane >> 4);
    f32x2 xv[20];
#pragma unroll
    for (int k = 0; k < 5; ++k) {
      const u32x4 w = *LDS(u32x4, tX + row * 1280 + (((lane & 15) + 16 * k) ^ (row & 15)) * 16);
#pragma unroll
      for (int e = 0; e < 4; ++e) xv[4 * k + e] = (f32x2){lo_bf(w[e]), hi_bf(w[e])};
    }
    f32x2 a1 = xv[0] + xv[1], a2 = xv[0] * xv[0], b1 = xv[2] + xv[3], b2 = xv[1] * xv[1];
    a2 = __builtin_elementwise_fma(xv[2], xv[2], a2); b2 = __builtin_elementwise_fma(xv[3], xv[3], b2);
#pragma unroll
    for (int k = 4; k < 20; k += 2) {
      a1 += xv[k]; b1 += xv[k + 1];
      a2 = __builtin_elementwise_fma(xv[k], xv[k], a2); b2 = __builtin_elementwise_fma(xv[k + 1], xv[k + 1], b2);
    }
    float s1 = (a1.x + a1.y) + (b1.x + b1.y), s2 = (a2.x + a2.y) + (b2.x + b2.y);
    s1 += dpp_f<0xB1>(s1); s2 += dpp_f<0xB1>(s2);
    s1 += dpp_f<0x4E>(s1); s2 += dpp_f<0x4E>(s2);
    s1 += dpp_f<0x141>(s1); s2 += dpp_f<0x141>(s2);
    s1 += dpp_f<0x140>(s1); s2 += dpp_f<0x140>(s2);
    const float mean = s1 * (1.f / D);
    const float var = fmaxf(s2 * (1.f / D) - mean * mean, 0.f);
    const float mb = rbf(mean), sd = rbf(__builtin_amdgcn_sqrtf(rbf(rbf(var) + 1e-5f)));
    const float ri = __builtin_amdgcn_rcpf(sd), mi = -mb * ri;
    if ((lane & 15) == 0)
      __hip_atomic_store((gu64*)((u64*)(A.ws + WS_LN) + t0 + row), ((u64)__float_as_uint(mb) << 32) | __float_as_uint(sd), RLX_AGENT);
#pragma unroll
    for (int k = 0; k < 5; ++k) {
      u32x4 o;
#pragma unroll
      for (int e = 0; e < 4; ++e) {
        const f32x2 u = __builtin_elementwise_fma(xv[4 * k + e], (f32x2){ri, ri}, (f32x2){mi, mi});
        o[e] = pack_f16(u.x, u.y);
      }
      *LDS(u32x4, tU + row * LN_RS + ((lane & 15) + 16 * k) * 16) = o;
    }
  }
  WAIT_LGKM0();
  BARRIER();
  STAMP(2);
  {
    const int g = lane >> 4, q = (lane & 15) >> 2, p = lane & 3;
    u32x4 ov[5];
#pragma unroll
    for (int k = 0; k < 5; ++k) {
      const u32 a = tU + (8 * g + q) * LN_RS + ((wave * 5 + k) * 16 + 4 * p) * 2;
      ov[k] = tr_read2(a, a + 4 * LN_RS);
    }
#pragma unroll
    for (int k = 0; k < 5; ++k) st16d(uT + (size_t)((wave * 5 + k) * 16 + (lane & 15)) * UT_ROW + 32 + (t0 + 8 * g) * 2, ov[k]);
  }
  WAIT_VM(0);
  BARRIER();
  if (tid == 0) set_flag((u32*)(A.ws + WS_FL) + FL_U + task * 32, *LDS(u32, lds0 + OFF_TAG));
  STAMP(3);
}

__device__ __forceinline__ void weight_task(const Args& A, int wtask, u32 lds0, int tid, int lane, int wave) {
  const int g = lane >> 4, q = (lane & 15) >> 2, p = lane & 3;
  if (wtask < 48) {
    const u16* W = wtask < 24 ? A.Wg : A.Wu;
    const int c0 = (wtask % 24) * 64;
    {
      const int row = 8 * wave + (lane >> 3), chk = (lane & 7) ^ ((row & 3) << 1);
      const char* src = (const char*)W + (size_t)row * 3072 + c0 * 2 + chk * 16;
#pragma unroll
      for (int jj = 0; jj < 10; ++jj) glds16(src + (size_t)jj * 64 * 3072, UNI(lds0 + (wave + 8 * jj) * 1024));
    }
    WAIT_VM(0);
    BARRIER();
    const int tl = wave & 3, ks0 = (wave >> 2) * 10;
    const u32 a = lds0 + (32 * ks0 + 8 * g + q) * 128 + (((2 * tl + (p >> 1)) ^ (q << 1)) * 16) + 8 * (p & 1);
    bf16x8 fr[10];
    f32x4 cs = {0.f, 0.f, 0.f, 0.f};
#pragma unroll
    for (int k = 0; k < 10; ++k) { fr[k] = tr_frag(a + k * 4096, a + k * 4096 + 512); cs = mfma16(ONES_BF16, fr[k], cs); }
    WAIT_LGKM0();
    BARRIER();
    if (lane < 16) *LDS(float, lds0 + (wave * 16 + lane) * 4) = cs[0];
    WAIT_LGKM0();
    BARRIER();
    const float cm = (*LDS(float, lds0 + (tl * 16 + (lane & 15)) * 4) + *LDS(float, lds0 + ((tl + 4) * 16 + (lane & 15)) * 4)) * (1.f / D);
    const int f = c0 + 16 * tl, pn = f / 96, fi = (f % 96) / 16;
    const int slot = (fi / 3) * 6 + (wtask < 24 ? 0 : 3) + fi % 3;
    char* dst = A.ws + WS_W1 + (((size_t)pn * 20 + ks0) * 12 + slot) * 1024 + lane * 16;
#pragma unroll
    for (int k = 0; k < 10; ++k) {
      const u32x4 w = __builtin_bit_cast(u32x4, fr[k]);
      u32x4 o;
#pragma unroll
      for (int e = 0; e < 4; ++e) o[e] = pack_bf16(lo_bf(w[e]) - cm, hi_bf(w[e]) - cm);
      st16d(dst + (size_t)k * 12288, o);
    }
  } else {
    const int ks = wtask - 48;
#pragma unroll
    for (int jj = 0; jj < 5; ++jj) {
      const int P = 64 * (wave + 8 * jj) + lane, row = P / 80, chk = P % 80;
      glds16((const char*)A.Wd + (size_t)(32 * ks + row) * 1280 + chk * 16, UNI(lds0 + (wave + 8 * jj) * 1024));
    }
    WAIT_VM(0);
    BARRIER();
#pragma unroll
    for (int jj = 0; jj < 5; ++jj) {
      const int fr = wave + 8 * jj;
      const u32 a = lds0 + (8 * g + q) * 1280 + (16 * fr + 4 * p) * 2;
      st16d(A.ws + WS_WD + (((size_t)(fr / 5) * 48 + ks) * 5 + fr % 5) * 1024 + lane * 16, tr_read2(a, a + 4 * 1280));
    }
  }
  WAIT_VM(0);
  BARRIER();
  if (tid == 0) set_flag((u32*)(A.ws + WS_FL) + FL_W + wtask * 32, *LDS(u32, lds0 + OFF_TAG));
}

struct cplx { f32x2 v, r; };
__device__ __forceinline__ cplx mk(f32x2 v) { return cplx{v, (f32x2){-v.y, v.x}}; }
__device__ __forceinline__ f32x2 cmul(f32x2 a, const cplx& w) {
  const f32x2 t = (f32x2){a.x, a.x} * w.v;
  return __builtin_elementwise_fma((f32x2){a.y, a.y}, w.r, t);
}
__device__ __forceinline__ f32x2 cexp(float zr, float zi, float l, float sgn) {
  const float m = __builtin_amdgcn_exp2f(zr * l * 1.44269504088896341f);
  const float a = __builtin_amdgcn_fractf(zi * l * 0.15915494309189535f);
  return (f32x2){m * __builtin_amdgcn_cosf(a), sgn * m * __builtin_amdgcn_sinf(a)};
}
__device__ __forceinline__ u32 pkh(f32x2 v) { return __builtin_bit_cast(u32, __builtin_convertvector(v, f16x2)); }
__device__ __forceinline__ u32 cmulh(u32 a, u32 b) {
  u32 t, r;
  asm("v_pk_mul_f16 %0, %1, %2 op_sel:[0,0] op_sel_hi:[0,1]" : "=v"(t) : "v"(a), "v"(b));
  asm("v_pk_fma_f16 %0, %1, %2, %3 op_sel:[1,1,0] op_sel_hi:[1,0,1] neg_lo:[0,1,0]" : "=v"(r) : "v"(a), "v"(b), "v"(t));
  return r;
}
#define SET_M0(base) asm volatile("s_mov_b32 m0, %0\n\ts_nop 0" ::"s"(base) : "memory")
#define WR4(row, a, b, c, d)                                                                                    \
  asm volatile("ds_write_addtid_b32 %0 offset:%4\n\tds_write_addtid_b32 %1 offset:%4+272\n\t"                    \
               "ds_write_addtid_b32 %2 offset:%4+544\n\tds_write_addtid_b32 %3 offset:%4+816"                    \
               ::"v"(a), "v"(b), "v"(c), "v"(d), "n"((row) * C_ROW) : "memory")

__device__ __forceinline__ void conv_role(const Args& A, int grp, u32 lds0, int tid, int lane, int wave, u32 tag) {
  const int ch = wave & 3, hf = wave >> 2, d0 = grp * 4, d = d0 + ch;
  const u32 sb = UNI(lds0 + ch * C_SCR);
  const int r32 = lane & 31, h32 = lane >> 5;
  const int j = lane & 15, g = lane >> 4;
  {
    const u64 q0 = (u64)A.Are, q1 = (u64)A.Aim, q2 = (u64)A.Bre, q3 = (u64)A.Bim, q4 = (u64)A.Cre, q5 = (u64)A.Cim;
#pragma unroll
    for (int rep = 0; rep < 2; ++rep) {
      if (rep == 1 && wave != 0) break;
      const int i = rep ? 8 : wave;
      const int b = 1024 * i + 16 * lane, a = b / 1536, o = b - a * 1536;
      u64 src = +q0;
      src = a == 1 ? +q1 : src; src = a == 2 ? +q2 : src; src = a == 3 ? +q3 : src; src = a == 4 ? +q4 : src; src = a == 5 ? +q5 : src;
      glds16((const char*)(src + (u64)d0 * 384 + o), UNI(lds0 + F_OFF_P + i * 1024));
    }
  }
  const u32 wdt = ((const u32*)A.logdt)[d >> 1], wds = ((const u32*)A.Dskip)[d >> 1];
  const float dt = rbf(__expf((d & 1) ? hi_bf(wdt) : lo_bf(wdt)));
  WAIT_VM(0);
  BARRIER();
  float zr[3], zi[3];
  const u32 pl = lds0 + F_OFF_P + ch * 384 + lane * 2;
#pragma unroll
  for (int r = 0; r < 3; ++r) {
    zr[r] = dt * bf2f(*LDS(u16, pl + r * 128));
    zi[r] = dt * bf2f(*LDS(u16, pl + 1536 + r * 128));
  }
  u32 an[3][11];
  if (hf == 0) {
    f32x2 cb[3];
    float bound = 0.f;
#pragma unroll
    for (int r = 0; r < 3; ++r) {
      const float br = dt * bf2f(*LDS(u16, pl + 2 * 1536 + r * 128)), bi = dt * bf2f(*LDS(u16, pl + 3 * 1536 + r * 128));
      const float cr = bf2f(*LDS(u16, pl + 4 * 1536 + r * 128)), ci = bf2f(*LDS(u16, pl + 5 * 1536 + r * 128));
      cb[r] = (f32x2){cr * br - ci * bi, -(cr * bi + ci * br)};
      const float grow = __builtin_amdgcn_exp2f(fmaxf(zr[r], 0.f) * (2064.f * 1.44269504f));
      bound = fmaxf(bound, (fabsf(cb[r].x) + fabsf(cb[r].y)) * grow);
    }
    bound = wave_max(bound);
    int ex = (int)((__float_as_uint(bound) >> 23) & 255);
    ex = ex < 20 ? 20 : (ex > 240 ? 240 : ex);
    const float scale = __uint_as_float((u32)(267 - ex) << 23);
    if (lane == 0) *LDS(float, lds0 + F_OFF_S + ch * 4) = __uint_as_float((u32)(ex - 5) << 23);
#pragma unroll
    for (int r = 0; r < 3; ++r) {
      const cplx w1 = mk(cexp(zr[r], zi[r], 1.f, -1.f));
      const cplx w512 = mk(cexp(zr[r], zi[r], 512.f, -1.f));
      const cplx w2 = mk(cmul(w1.v, w1));
      const f32x2 w3 = cmul(w2.v, w1);
      const cplx w4 = mk(cmul(w2.v, w2));
      const f32x2 w1024 = cmul(w512.v, w512), w1536 = cmul(w1024, w512);
      const f32x2 P0 = cb[r] * scale, P4 = cmul(P0, w4), P8 = cmul(P4, w4), P12 = cmul(P8, w4), P16 = cmul(P12, w4);
      an[r][0] = pkh(P0); an[r][1] = pkh(P4); an[r][2] = pkh(P8); an[r][3] = pkh(P12); an[r][4] = pkh(P16);
      an[r][5] = pkh(w1.v); an[r][6] = pkh(w2.v); an[r][7] = pkh(w3);
      an[r][8] = pkh(w512.v); an[r][9] = pkh(w1024); an[r][10] = pkh(w1536);
    }
  } else {
#pragma unroll
    for (int r = 0; r < 3; ++r) {
      const cplx w16 = mk(cexp(zr[r], zi[r], 16.f, 1.f));
      const cplx w128 = mk(cexp(zr[r], zi[r], 128.f, 1.f));
      const cplx w32 = mk(cmul(w16.v, w16));
      const f32x2 w48 = cmul(w32.v, w16);
      const f32x2 w64 = cmul(w32.v, w32);
      const f32x2 w192 = cmul(w64, w128), w256 = cmul(w128.v, w128);
      const f32x2 w320 = cmul(w192, w128), w384 = cmul(w256, w128), w448 = cmul(w320, w128);
      an[r][0] = pkh(w64); an[r][1] = pkh(w128.v); an[r][2] = pkh(w192); an[r][3] = pkh(w256);
      an[r][4] = pkh(w320); an[r][5] = pkh(w384); an[r][6] = pkh(w448);
      an[r][7] = pkh(w16.v); an[r][8] = pkh(w32.v); an[r][9] = pkh(w48); an[r][10] = 0x00003c00u;
    }
  }
  u32 arow;
  {
    const int hr = (r32 >> 2) & 1, rho = (r32 & 3) + 4 * (r32 >> 3);
    const int row = rho <= 8 ? rho + 8 * hr : rho <= 11 ? rho + 8 + 2 * hr : rho <= 13 ? rho + 10 + hr : rho + 11 + hr;
    arow = sb + row * C_ROW + h32 * 16 + hf * 128;
  }
  const u32 brow = sb + (28 + r32) * C_ROW + h32 * 16 + hf * 128;
  f32x16 kacc;
#pragma unroll
  for (int i = 0; i < 16; ++i) kacc[i] = 0.f;
  SET_M0(sb);
#pragma unroll
  for (int r = 0; r < 3; ++r) {
    u32 gv[2][4];
    auto grp4 = [&](int gi, u32 (&o)[4]) {
      const u32* a = an[r];
      if (hf == 0) {
        if (gi < 4) { o[0] = a[gi]; o[1] = cmulh(a[gi], a[5]); o[2] = cmulh(a[gi], a[6]); o[3] = cmulh(a[gi], a[7]); }
        else if (gi == 4) { o[0] = a[4]; o[1] = cmulh(a[0], a[8]); o[2] = cmulh(a[1], a[8]); o[3] = cmulh(a[2], a[8]); }
        else if (gi == 5) { o[0] = cmulh(a[3], a[8]); o[1] = cmulh(a[4], a[8]); o[2] = cmulh(a[0], a[9]); o[3] = cmulh(a[2], a[9]); }
        else if (gi == 6) { o[0] = cmulh(a[4], a[9]); o[1] = cmulh(a[0], a[10]); o[2] = cmulh(a[2], a[10]); o[3] = cmulh(a[4], a[10]); }
      } else {
        if (gi == 0) { o[0] = a[10]; o[1] = a[7]; o[2] = a[8]; o[3] = a[9]; }
        else { o[0] = a[gi - 1]; o[1] = cmulh(a[gi - 1], a[7]); o[2] = cmulh(a[gi - 1], a[8]); o[3] = cmulh(a[gi - 1], a[9]); }
      }
    };
    if (hf == 0) {
      grp4(0, gv[0]);
#pragma unroll
      for (int gi = 0; gi < 7; ++gi) {
        if (gi < 6) grp4(gi + 1, gv[(gi + 1) & 1]);
        SB();
        WR4(4 * gi, gv[gi & 1][0], gv[gi & 1][1], gv[gi & 1][2], gv[gi & 1][3]);
        SB();
      }
    } else {
      grp4(0, gv[0]);
#pragma unroll
      for (int gi = 0; gi < 8; ++gi) {
        if (gi < 7) grp4(gi + 1, gv[(gi + 1) & 1]);
        SB();
        WR4(28 + 4 * gi, gv[gi & 1][0], gv[gi & 1][1], gv[gi & 1][2], gv[gi & 1][3]);
        SB();
      }
    }
    WAIT_LGKM0();
    BARRIER();
    f16x8 fa[4], fb[4];
#pragma unroll
    for (int k = 0; k < 4; ++k) { fa[k] = *LDS(f16x8, arow + k * 32); fb[k] = *LDS(f16x8, brow + k * 32); }
    WAIT_LGKM0();
    BARRIER();
#pragma unroll
    for (int k = 0; k < 4; ++k) kacc = __builtin_amdgcn_mfma_f32_32x32x16_f16(fa[k], fb[k], kacc, 0, 0, 0);
  }
  STAMP(1);
  {
    const u32 ex = sb + hf * 4096 + lane * 16, exr = sb + (hf ^ 1) * 4096 + lane * 16;
    if (hf == 0) {
      *LDS(f32x4, ex) = (f32x4){kacc[12], kacc[13], kacc[14], kacc[15]};
    } else {
      *LDS(f32x4, ex) = (f32x4){kacc[0], kacc[1], kacc[2], kacc[3]};
      *LDS(f32x4, ex + 1024) = (f32x4){kacc[4], kacc[5], kacc[6], kacc[7]};
      *LDS(f32x4, ex + 2048) = (f32x4){kacc[8], kacc[9], kacc[10], kacc[11]};
    }
    WAIT_LGKM0();
    BARRIER();
    const u32 kb = lds0 + F_OFF_K + ch * C_KCH + 4080 - 32 * r32 - 16 * h32;
    auto put = [&](int base, float e0, float e1, float e2, float e3, float e4, float e5, float e6, float e7) {
      *LDS(u32x4, kb - 2 * base) = (u32x4){pack_f16(e7, e6), pack_f16(e5, e4), pack_f16(e3, e2), pack_f16(e1, e0)};
    };
    if (hf == 0) {
      const f32x4 o0 = *LDS(f32x4, exr), o1 = *LDS(f32x4, exr + 1024), o2 = *LDS(f32x4, exr + 2048);
      float v[12];
#pragma unroll
      for (int i = 0; i < 4; ++i) {
        v[i] = (kacc[i] + o0[i]) * (1.f / 256.f); v[4 + i] = (kacc[4 + i] + o1[i]) * (1.f / 256.f); v[8 + i] = (kacc[8 + i] + o2[i]) * (1.f / 256.f);
      }
      const bool far = r32 >= 16;
      put(0, v[0], far ? v[0] : v[1], v[2], far ? v[4] : v[3], v[4], far ? v[4] : v[5], v[6], far ? v[8] : v[7]);
      put(512, v[9], v[9], v[9], v[10], v[10], v[10], v[11], v[11]);
    } else {
      const f32x4 o = *LDS(f32x4, exr);
      const float v12 = (kacc[12] + o[0]) * (1.f / 256.f), v13 = (kacc[13] + o[1]) * (1.f / 256.f),
                  v14 = (kacc[14] + o[2]) * (1.f / 256.f), v15 = (kacc[15] + o[3]) * (1.f / 256.f);
      put(1024, v12, v12, v12, v12, h32 ? v13 : v12, v13, v13, v13);
      put(1536, v14, v14, v14, v14, h32 ? v15 : v14, v15, v15, v15);
    }
  }
  if (hf == 1 && lane < 32) *LDS(u32x4, lds0 + F_OFF_K + ch * C_KCH + 4096 + lane * 16) = (u32x4){0u, 0u, 0u, 0u};
  STAMP(2);
  if (wave == 7) wait_flags<1>((const u32*)(A.ws + WS_FL) + FL_U, 64, tag, lane);
  WAIT_LGKM0();
  BARRIER();
  STAMP(3);
  {
    const char* up = A.ws + WS_UT + (size_t)d * UT_ROW + lane * 16 + hf * 2;
    const u32 dst = UNI(lds0 + F_OFF_U + ch * C_UCH + hf * C_UCP);
#pragma unroll
    for (int k = 0; k < 4; ++k) glds16d(up + k * 1024, dst + k * 1024);
    if (lane < 2) glds16d(up + 4096, dst + 4096);
    glds16d(A.ws + WS_LN + wave * 2048 + lane * 16, UNI(lds0 + F_OFF_L + wave * 2048));
    glds16d(A.ws + WS_LN + wave * 2048 + 1024 + lane * 16, UNI(lds0 + F_OFF_L + wave * 2048 + 1024));
  }
  WAIT_VM(0);
  BARRIER();
  STAMP(4);
  const int cc = (1 + j) & 1;
  const u32 aa = lds0 + F_OFF_U + ch * C_UCH + cc * C_UCP + 2 * (1 + j + 8 * g - cc) + hf * 256;
  const u32 ba = lds0 + F_OFF_K + ch * C_KCH + 32 * j + 16 * g + hf * 256;
  f32x4 acc[8];
#pragma unroll
  for (int i = 0; i < 8; ++i) acc[i] = (f32x4){0.f, 0.f, 0.f, 0.f};
  {
    f16x8 Af[2][8], Bf[2][8];
    u32 am[4];
#pragma unroll
    for (int k = 0; k < 4; ++k) { am[k] = aa + 1024 * k; asm volatile("" : "+v"(am[k])); }
    auto rd = [&](int set, int r) {
#pragma unroll
      for (int m = 0; m < 8; ++m) {
        const LAS u32* ap = LDS(const u32, am[m >> 1] + 64 * r + 512 * (m & 1));
        Af[set][m] = __builtin_bit_cast(f16x8, (u32x4){ap[0], ap[1], ap[2], ap[3]});
        Bf[set][m] = *LDS(f16x8, ba + 64 * r + 512 * (7 - m));
      }
    };
    auto mm = [&](int set) {
#pragma unroll
      for (int sdx = 0; sdx < 8; ++sdx)
#pragma unroll
        for (int m = 0; m <= sdx; ++m) acc[sdx] = __builtin_amdgcn_mfma_f32_16x16x32_f16(Af[set][m], Bf[set][sdx - m], acc[sdx], 0, 0, 0);
    };
#define C_SCHED() do { _Pragma("unroll") for (int k_ = 0; k_ < 24; ++k_) { SG(0x008, 1); SG(0x100, 1); } SG(0x008, 12); } while (0)
    rd(0, 0);
    SB(); rd(1, 1); mm(0); C_SCHED(); SB();
    rd(0, 2); mm(1); C_SCHED(); SB();
    rd(1, 3); mm(0); C_SCHED(); SB();
    mm(1);
  }
  STAMP(5);
  {
    const u32 ex = lds0 + wave * 4096 + lane * 16, exr = lds0 + (wave ^ 4) * 4096 + lane * 16;
    if (hf == 0) {
#pragma unroll
      for (int i = 0; i < 4; ++i) *LDS(f32x4, ex + i * 1024) = acc[4 + i];
    } else {
#pragma unroll
      for (int i = 0; i < 4; ++i) *LDS(f32x4, ex + i * 1024) = acc[i];
    }
    const float it = *LDS(float, lds0 + F_OFF_S + ch * 4);
    const float dsk = (d & 1) ? hi_bf(wds) : lo_bf(wds);
    u32x2 dv[4], xv[4];
#pragma unroll
    for (int i = 0; i < 4; ++i) {
      const int t = 16 * (16 * (4 * hf + i) + 15 - j) + 4 * g;
      const u32x2 uw = *LDS(u32x2, lds0 + F_OFF_U + ch * C_UCH + 2 * (16 + t));
      const u32 w0 = uw[0], w1 = uw[1];
      const f16x2 u01 = __builtin_bit_cast(f16x2, w0), u23 = __builtin_bit_cast(f16x2, w1);
      const float uf[4] = {(float)u01[0], (float)u01[1], (float)u23[0], (float)u23[1]};
      dv[i] = (u32x2){pack_bf16(uf[0] * dsk, uf[1] * dsk), pack_bf16(uf[2] * dsk, uf[3] * dsk)};
      const f32x4 l01 = *LDS(f32x4, lds0 + F_OFF_L + t * 8), l23 = *LDS(f32x4, lds0 + F_OFF_L + t * 8 + 16);
      xv[i] = (u32x2){pack_bf16(fmaf(uf[0], l01[0], l01[1]), fmaf(uf[1], l01[2], l01[3])), pack_bf16(fmaf(uf[2], l23[0], l23[1]), fmaf(uf[3], l23[2], l23[3]))};
    }
    WAIT_LGKM0();
    BARRIER();
    u32x2 hv[4];
#pragma unroll
    for (int i = 0; i < 4; ++i) {
      const f32x4 o = *LDS(f32x4, exr + i * 1024);
      const f32x4 a0 = acc[i], a1 = acc[4 + i];
      const f32x4 y = ((hf ? +a1 : +a0) + o) * it;
      const u32 y01 = pack_bf16(y[0], y[1]), y23 = pack_bf16(y[2], y[3]);
      const u32 s01 = pack_bf16(lo_bf(y01) + lo_bf(dv[i][0]), hi_bf(y01) + hi_bf(dv[i][0]));
      const u32 s23 = pack_bf16(lo_bf(y23) + lo_bf(dv[i][1]), hi_bf(y23) + hi_bf(dv[i][1]));
      hv[i] = (u32x2){pack_bf16(lo_bf(xv[i][0]) + lo_bf(s01), hi_bf(xv[i][0]) + hi_bf(s01)), pack_bf16(lo_bf(xv[i][1]) + lo_bf(s23), hi_bf(xv[i][1]) + hi_bf(s23))};
    }
    char* hrow = A.ws + WS_HT + (size_t)d * 4096;
#pragma unroll
    for (int i = 0; i < 4; i += 2) st16d(hrow + (16 * (16 * (4 * hf + i + (g & 1)) + 15 - j) + 8 * (g >> 1)) * 2, widen16(hv[i], hv[i + 1]));
  }
  WAIT_VM(0);
  BARRIER();
  if (tid == 0) set_flag((u32*)(A.ws + WS_FL) + FL_H + grp * 32, *LDS(u32, lds0 + OFF_TAG));
  STAMP(6);
}

__device__ __forceinline__ void gemm1_phase(const Args& A, u32 lds0, int tid, int lane, int wave, u32 tag) {
  const int xcd = blockIdx.x & 7, idx = blockIdx.x >> 3;
  const int pm = (xcd & 1) * 8 + (idx & 7), pn = (xcd >> 1) * 4 + (idx >> 3);
  const int t0 = pm * 128;
  const u32* flags = (const u32*)(A.ws + WS_FL);
  if (wave >= 4) {
    const int lw = wave - 4;
    int kH = 5 * (pn & 3), kW = kH;
    const int row = 8 * lw + (lane >> 4);
    const int chk = (lane & 15) ^ (((row & 3) << 1) | (((row >> 3) & 1) << 3));
    const char* srcH = A.ws + WS_HT + (size_t)(row + 32 * kH) * 4096 + t0 * 2 + chk * 16;
    const char* srcW = A.ws + WS_W1 + (size_t)pn * 245760 + (size_t)kW * 12288 + lw * 3072 + lane * 16;
    const u32 dH = lds0 + lw * 2048, dW = lds0 + 8192 + lw * 3072;
    auto issueH = [&](u32 so) {
      glds16d(srcH, dH + so);
      glds16d(srcH + 4 * 4096, dH + so + 1024);
      if (++kH == G1_NK) { kH = 0; srcH -= (size_t)19 * 32 * 4096; } else srcH += 32 * 4096;
    };
    auto issueW = [&](u32 so) {
      glds16d(srcW, dW + so);
      glds16d(srcW + 1024, dW + so + 1024);
      glds16d(srcW + 2048, dW + so + 2048);
      if (++kW == G1_NK) { kW = 0; srcW -= 19 * 12288; } else srcW += 12288;
    };
    if (wave == 4) wait_flags<1>(flags + FL_W, 48, tag, lane);
    BARRIER();
#pragma unroll
    for (int s = 0; s < G1_NS; ++s) issueW(s * G1_STG);
    if (wave == 4) wait_flags<3>(flags + FL_H, 160, tag, lane);
    BARRIER();
    STAMP(7);
    issueH(0); issueH(G1_STG); issueH(2 * G1_STG);
    WAIT_VM(4); BARRIER();
    issueH(3 * G1_STG); issueH(4 * G1_STG);
    WAIT_VM(6); BARRIER();
    issueH(5 * G1_STG); issueH(6 * G1_STG);
    WAIT_VM(8); BARRIER();
    u32 so = 0;
    auto issue = [&]() { issueH(so); issueW(so); so = so == (G1_NS - 1) * G1_STG ? 0 : so + G1_STG; };
    issue(); WAIT_VM(11); BARRIER();
    issue(); WAIT_VM(14); BARRIER();
    issue(); WAIT_VM(17); BARRIER();
    for (int i = 5; i < G1_NK - 5; ++i) {
      issue();
      WAIT_VM(20); BARRIER();
    }
    WAIT_VM(15); BARRIER(); WAIT_VM(10); BARRIER(); WAIT_VM(5); BARRIER(); WAIT_VM(0); BARRIER();
    return;
  }
  const int wt = wave & 1, wf = wave >> 1;
  const int g = lane >> 4, q = (lane & 15) >> 2, p = lane & 3;
  const int rot = pn >> 2;
  const u32 hrow = (8 * g + q) * 256 + 8 * (p & 1);
  const u32 hx = q | ((g & 1) << 2);
  u32 hc[4];
#pragma unroll
  for (int j = 0; j < 4; ++j) hc[j] = lds0 + hrow + ((((u32)(4 * wt + ((j + rot) & 3)) ^ hx) << 1) | (p >> 1)) * 16;
  u32 wc = lds0 + 8192 + wf * 6144 + lane * 16, so = 0;
  f32x4 acc[6][4];
#pragma unroll
  for (int i = 0; i < 6; ++i)
#pragma unroll
    for (int j = 0; j < 4; ++j) acc[i][j] = (f32x4){0.f, 0.f, 0.f, 0.f};
  f32x4 st1 = {0.f, 0.f, 0.f, 0.f}, st2 = st1;
  bf16x8 hf[2][4], wfr[2][6];
  auto rd = [&](int set) {
    wfr[set][0] = *LDS(bf16x8, wc);
#pragma unroll
    for (int j = 0; j < 4; ++j) hf[set][j] = tr_frag(hc[j], hc[j] + 1024);
#pragma unroll
    for (int i = 1; i < 6; ++i) wfr[set][i] = *LDS(bf16x8, wc + i * 1024);
  };
  auto adv = [&]() {
    u32 dl = so == (G1_NS - 1) * G1_STG ? (u32)(-(G1_NS - 1) * G1_STG) : (u32)G1_STG;
    so += dl;
    asm volatile("" : "+s"(dl));
#pragma unroll
    for (int j = 0; j < 4; ++j) hc[j] += dl;
    wc += dl;
  };
  auto mma = [&](int set) {
#pragma unroll
    for (int i = 0; i < 6; ++i)
#pragma unroll
      for (int j = 0; j < 4; ++j) acc[i][j] = mfma16(wfr[set][i], hf[set][j], acc[i][j]);
  };
  auto stat = [&](int set) {
    st1 = mfma16(ONES_BF16, hf[set][0], st1);
    st2 = mfma16(hf[set][0], hf[set][0], st2);
  };
#define G1_SCHED(extra) do { \
    SG(0x008, 1); SG(0x100, 1); SG(0x008, 1); SG(0x100, 1); SG(0x008, 1); SG(0x100, 1); SG(0x008, 1); SG(0x100, 1); \
    SG(0x008, 1); SG(0x100, 1); SG(0x008, 1); SG(0x100, 1); SG(0x008, 1); SG(0x100, 1); SG(0x008, 1); SG(0x100, 1); \
    SG(0x008, 1); SG(0x100, 1); SG(0x008, 2); SG(0x100, 1); SG(0x008, 2); SG(0x100, 1); SG(0x008, 2); SG(0x100, 1); \
    SG(0x008, 2); SG(0x100, 1); SG(0x008, 1); SG(0x100, 1); SG(0x008, 1); SG(0x002, 1); SG(0x008, 1); SG(0x002, 1); \
    SG(0x008, 1); SG(0x002, 1); SG(0x008, 1); SG(0x002, 1); SG(0x008, 1); SG(0x002, 1); SG(0x008, 1 + (extra)); } while (0)
  BARRIER();
  BARRIER();
  BARRIER();
  STAMP(8);
  rd(0); adv();
#pragma unroll
  for (int i = 1; i < G1_NK - 1; i += 2) {
    BARRIER();
    SB(); rd(1); mma(0); adv();
    if (i - 1 < 5) { stat(0); G1_SCHED(2); } else { G1_SCHED(0); }
    SB();
    BARRIER();
    SB(); rd(0); mma(1); adv();
    if (i < 5) { stat(1); G1_SCHED(2); } else { G1_SCHED(0); }
    SB();
  }
  BARRIER();
  SB(); rd(1); mma(0); G1_SCHED(0); SB();
  WAIT_LGKM0();
  BARRIER();
#pragma unroll
  for (int i = 0; i < 3; ++i)
#pragma unroll
    for (int j = 0; j < 4; ++j) {
      acc[i][j] = mfma16(wfr[1][i], hf[1][j], acc[i][j]);
      acc[3 + i][j] = mfma16(wfr[1][3 + i], hf[1][j], acc[3 + i][j]);
    }
  STAMP(9);
  {
    char* ob = A.ws + WS_HM + ((size_t)(pm * 8 + 4 * wt) * 48 + 3 * pn) * 1024 + (lane & 15) * 16 + (g >> 1) * 256;
#pragma unroll
    for (int j = 0; j < 4; j += 2) {
      char* oj = ob + (size_t)((j + (g & 1) + rot) & 3) * 49152;
#pragma unroll
      for (int i = 0; i < 3; ++i) {
        u32x2 o[2];
#pragma unroll
        for (int jj = 0; jj < 2; ++jj) {
          const f32x4 gv = acc[i][j + jj], uv = acc[3 + i][j + jj];
          o[jj] = (u32x2){pack_bf16(fmaxf(gv[0], 0.f) * uv[0], fmaxf(gv[1], 0.f) * uv[1]), pack_bf16(fmaxf(gv[2], 0.f) * uv[2], fmaxf(gv[3], 0.f) * uv[3])};
        }
        const int E = 6 * wf + 2 * i;
        st16d(oj + (E >> 2) * 1024 + (E & 3) * 256, widen16(o[0], o[1]));
      }
    }
  }
  if (wf == 0 && g == ((lane & 15) >> 2))
    __hip_atomic_store((gu64*)((u64*)(A.ws + WS_PT) + (size_t)(pn & 3) * L + t0 + (4 * wt + rot) * 16 + (lane & 15)),
                       ((u64)__float_as_uint(diag16(st2, lane)) << 32) | __float_as_uint(st1[0]), RLX_AGENT);
  WAIT_VM(0);
}

__device__ __forceinline__ void gemm2_phase(const Args& A, u32 lds0, int tid, int lane, int wave, u32 tag) {
  const int xcd = blockIdx.x & 7, idx = blockIdx.x >> 3;
  const int pm = (xcd & 3) * 8 + (idx & 7), pn = (xcd >> 2) * 4 + (idx >> 3);
  const int t0 = pm * 64, d0 = pn * 80;
  u32* flags = (u32*)(A.ws + WS_FL);
  if (wave >= 4) {
    const int lw = wave - 4;
    const char* srcP = A.ws + WS_HM + (size_t)(pm * 4 + lw) * 49152 + lane * 16;
    const char* srcW = A.ws + WS_WD + (size_t)pn * 245760 + lw * 5120 + lane * 16;
    const u32 dP = lds0 + lw * 4096, dW = lds0 + 16384 + lw * 5120;
    auto issueP = [&](u32 so) {
      glds16d(srcP, dP + so); glds16d(srcP + 1024, dP + so + 1024); glds16d(srcP + 2048, dP + so + 2048); glds16d(srcP + 3072, dP + so + 3072);
      srcP += 4096;
    };
    auto issueW = [&](u32 so) {
      glds16d(srcW, dW + so); glds16d(srcW + 1024, dW + so + 1024); glds16d(srcW + 2048, dW + so + 2048);
      glds16d(srcW + 3072, dW + so + 3072); glds16d(srcW + 4096, dW + so + 4096);
      srcW += 20480;
    };
    wait_flags<1>(flags + FL_W + 48 * 32, 48, tag, lane);
    BARRIER();
    issueW(0); issueW(G2_STG); issueW(2 * G2_STG); issueW(3 * G2_STG);
#pragma unroll
    for (int k = 0; k < 3; ++k) {
      const int jj = lw + 4 * k < 10 ? lw + 4 * k : lw + 4;
      glds16d(A.ws + WS_HT + (size_t)(d0 + 8 * jj + (lane >> 3)) * 4096 + t0 * 2 + (lane & 7) * 16, UNI(lds0 + G2_OFF_H + jj * 1024));
    }
    BARRIER();
    if (wave == 4) wait_flags<1>(flags + FL_P + (pm >> 1) * 16 * 32, 16, tag, lane);
    BARRIER();
    STAMP(10);
    issueP(0); issueP(G2_STG);
    WAIT_VM(4); BARRIER();
    issueP(2 * G2_STG); issueP(3 * G2_STG);
    WAIT_VM(8); BARRIER();
    u32 so = 0;
    auto issue = [&]() { issueP(so); issueW(so); so = so == 3 * G2_STG ? 0 : so + G2_STG; };
    issue(); WAIT_VM(13); BARRIER();
    for (int i = 2; i < G2_NK - 3; ++i) {
      issue();
      WAIT_VM(18); BARRIER();
    }
    WAIT_VM(9); BARRIER();
    WAIT_VM(0); BARRIER();
    BARRIER();
    BARRIER();
    return;
  }
  BARRIER();
  if (tid == 0) {
    const int pm1 = (xcd & 1) * 8 + (idx & 7), pn1 = (xcd >> 1) * 4 + (idx >> 3);
    set_flag(flags + FL_P + (pm1 * 16 + pn1) * 32, tag);
  }
  const int wt = wave & 1, wk = wave >> 1;
  u32 pc = lds0 + (wt * 2) * 4096 + wk * 2048 + lane * 16, wc = lds0 + 16384 + wk * 2 * 5120 + lane * 16, so = 0;
  f32x4 acc[5][2];
#pragma unroll
  for (int i = 0; i < 5; ++i) { acc[i][0] = (f32x4){0.f, 0.f, 0.f, 0.f}; acc[i][1] = acc[i][0]; }
  bf16x8 pf[2][2][2], wf[2][2][5];
  auto rd = [&](int set) {
#pragma unroll
    for (int ks = 0; ks < 2; ++ks) {
      wf[set][ks][0] = *LDS(bf16x8, wc + ks * 5120);
#pragma unroll
      for (int j = 0; j < 2; ++j) pf[set][ks][j] = *LDS(bf16x8, pc + j * 4096 + ks * 1024);
#pragma unroll
      for (int i = 1; i < 5; ++i) wf[set][ks][i] = *LDS(bf16x8, wc + ks * 5120 + i * 1024);
    }
  };
  auto adv = [&]() {
    u32 dl = so == 3 * G2_STG ? (u32)(-3 * G2_STG) : (u32)G2_STG;
    so += dl;
    asm volatile("" : "+s"(dl));
    pc += dl; wc += dl;
  };
  auto mma = [&](int set) {
#pragma unroll
    for (int ks = 0; ks < 2; ++ks)
#pragma unroll
      for (int i = 0; i < 5; ++i)
#pragma unroll
        for (int j = 0; j < 2; ++j) acc[i][j] = mfma16(wf[set][ks][i], pf[set][ks][j], acc[i][j]);
  };
#define G2_SCHED() do { _Pragma("unroll") for (int k_ = 0; k_ < 4; ++k_) { SG(0x008, 1); SG(0x100, 1); SG(0x008, 1); SG(0x100, 1); SG(0x008, 1); SG(0x100, 1); SG(0x008, 1); } \
    SG(0x008, 1); SG(0x100, 1); SG(0x008, 1); SG(0x100, 1); SG(0x008, 2); } while (0)
  BARRIER();
  const int tl = wt * 32 + wk * 16;
  u64 pw[4];
#pragma unroll
  for (int k = 0; k < 4; ++k) pw[k] = __hip_atomic_load((gu64*)((u64*)(A.ws + WS_PT) + (size_t)k * L + t0 + tl + (lane & 15)), RLX_AGENT);
  BARRIER();
  STAMP(11);
  rd(0); adv();
  WAIT_LGKM0();
#pragma unroll
  for (int i = 1; i < G2_NK - 1; i += 2) {
    BARRIER();
    SB(); rd(1); mma(0); adv(); G2_SCHED(); SB();
    WAIT_LGKM0();
    BARRIER();
    SB(); rd(0); mma(1); adv(); G2_SCHED(); SB();
    WAIT_LGKM0();
  }
  BARRIER();
  SB(); rd(1); mma(0); G2_SCHED(); SB();
  WAIT_LGKM0();
  mma(1);
  STAMP(12);
  float rr;
  {
    float s1 = 0.f, s2 = 0.f;
#pragma unroll
    for (int k = 0; k < 4; ++k) { s1 += __uint_as_float((u32)pw[k]); s2 += __uint_as_float((u32)(pw[k] >> 32)); }
    const float mean = s1 * (1.f / D);
    const float var = fmaxf(s2 * (1.f / D) - mean * mean, 0.f);
    const float r = __builtin_amdgcn_rcpf(rbf(__builtin_amdgcn_sqrtf(rbf(rbf(var) + 1e-5f))));
    rr = r * r;
  }
  const int g = lane >> 4, q = (lane & 15) >> 2, p = lane & 3;
  s16x4 hv[5];
#pragma unroll
  for (int i = 0; i < 5; ++i) hv[i] = tr_read(lds0 + G2_OFF_H + (16 * i + 4 * g + q) * 128 + (tl + 4 * p) * 2);
  BARRIER();
  const u32 ex = lds0 + (wave ^ 2) * 5120 + lane * 16, exr = lds0 + wave * 5120 + lane * 16;
#pragma unroll
  for (int i = 0; i < 5; ++i) { const f32x4 a0 = acc[i][0], a1 = acc[i][1]; *LDS(f32x4, ex + i * 1024) = wk ? +a0 : +a1; }
  WAIT_LGKM0();
  BARRIER();
  u32x2 o2[5];
#pragma unroll
  for (int i = 0; i < 5; ++i) {
    const f32x4 o = *LDS(f32x4, exr + i * 1024);
    const f32x4 a0 = acc[i][0], a1 = acc[i][1];
    const f32x4 m = ((wk ? +a1 : +a0) + o) * rr;
    const u32 m01 = pack_bf16(m[0], m[1]), m23 = pack_bf16(m[2], m[3]);
    o2[i] = (u32x2){pack_bf16(bf2f((u16)hv[i][0]) + lo_bf(m01), bf2f((u16)hv[i][1]) + hi_bf(m01)),
                    pack_bf16(bf2f((u16)hv[i][2]) + lo_bf(m23), bf2f((u16)hv[i][3]) + hi_bf(m23))};
  }
  char* orow = (char*)A.out + (size_t)(t0 + tl + (lane & 15)) * 1280 + d0 * 2;
  *(u32x4*)(orow + (16 * (0 + (g & 1)) + 8 * (g >> 1)) * 2) = widen16(o2[0], o2[1]);
  *(u32x4*)(orow + (16 * (2 + (g & 1)) + 8 * (g >> 1)) * 2) = widen16(o2[2], o2[3]);
  *(u32x2*)(orow + (64 + 4 * g) * 2) = o2[4];
  STAMP(13);
}

__global__ __launch_bounds__(512, 2) void s4_reglu_block(const Args A) {
  STAMP(0);
  extern __shared__ __attribute__((aligned(16))) char smem[];
  const u32 lds0 = (u32)(size_t)(LAS char*)smem;
  const int tid = threadIdx.x, lane = tid & 63;
  const int wave = __builtin_amdgcn_readfirstlane(tid >> 6);
  const int xcd = blockIdx.x & 7, idx = blockIdx.x >> 3;
  u32* flags = (u32*)(A.ws + WS_FL);
  u32 tag7 = 0;
  if (wave == 7) {
    tag7 = __hip_atomic_load((gu32*)flags, RLX_AGENT) + 1u;
    if (lane == 0) { *LDS(u32, lds0 + OFF_TAG) = tag7; set_flag(flags + FL_STARTED + blockIdx.x, tag7); }
  }
  if (idx < 12) {
    int wtask;
    if (idx < 8) {
      const int task = xcd * 8 + idx;
      ln1_task(A, task, lds0, tid, lane, wave);
      wtask = task < 16 ? 32 + task : 48 + task - 16;
    } else {
      wtask = xcd * 4 + (idx - 8);
    }
    weight_task(A, wtask, lds0, tid, lane, wave);
    STAMP(6);
    if (blockIdx.x == 95 && wave == 7) {
      const u64 t0 = __builtin_amdgcn_s_memrealtime();
      for (;;) {
        bool ok = true;
#pragma unroll
        for (int k = 0; k < 4; ++k) ok &= __hip_atomic_load((gu32*)(flags + FL_STARTED + k * 64 + lane), RLX_AGENT) == tag7;
        if (__all(ok)) { if (lane == 0) set_flag(flags, tag7); break; }
        if (__builtin_amdgcn_s_memrealtime() - t0 > SPIN_TICKS) break;
        __builtin_amdgcn_s_sleep(8);
      }
    }
  } else {
    conv_role(A, xcd * 20 + idx - 12, lds0, tid, lane, wave, tag7);
  }
  WAIT_LGKM0();
  BARRIER();
  const u32 tag = UNI(*LDS(u32, lds0 + OFF_TAG));
  gemm1_phase(A, lds0, tid, lane, wave, tag);
  gemm2_phase(A, lds0, tid, lane, wave, tag);
}

extern "C" void kernel_launch(void* const* d_in, const int* in_sizes, int n_in, void* d_out, int out_size, void* d_ws,
                              size_t ws_size, hipStream_t stream) {
  Args A;
  A.x = (const u16*)d_in[0];
  A.Are = (const u16*)d_in[1]; A.Aim = (const u16*)d_in[2]; A.Bre = (const u16*)d_in[3]; A.Bim = (const u16*)d_in[4];
  A.Cre = (const u16*)d_in[5]; A.Cim = (const u16*)d_in[6]; A.logdt = (const u16*)d_in[7]; A.Dskip = (const u16*)d_in[8];
  A.Wg = (const u16*)d_in[10]; A.Wu = (const u16*)d_in[11]; A.Wd = (const u16*)d_in[12];
  A.out = (u16*)d_out;
  A.ws = (char*)d_ws;
  (void)hipFuncSetAttribute((const void*)s4_reglu_block, hipFuncAttributeMaxDynamicSharedMemorySize, LDS_BYTES);
  s4_reglu_block<<<256, 512, LDS_BYTES, stream>>>(A);
}
```

```cpp
#include <hip/hip_runtime.h>
#include <hip/hip_bf16.h>
#include <stdint.h>

typedef unsigned short u16;
typedef unsigned u32;
typedef unsigned long long u64;
typedef short s16x4 __attribute__((ext_vector_type(4)));
typedef __bf16 bf16x8 __attribute__((ext_vector_type(8)));
typedef __bf16 bf16x2 __attribute__((ext_vector_type(2)));
typedef _Float16 f16x2 __attribute__((ext_vector_type(2)));
typedef _Float16 f16x8 __attribute__((ext_vector_type(8)));
typedef float f32x16 __attribute__((ext_vector_type(16)));
typedef float f32x4 __attribute__((ext_vector_type(4)));
typedef float f32x2 __attribute__((ext_vector_type(2)));
typedef u32 u32x4 __attribute__((ext_vector_type(4)));
typedef u32 u32x2 __attribute__((ext_vector_type(2)));
typedef __attribute__((address_space(1))) u32 gu32;
typedef __attribute__((address_space(1))) u64 gu64;
#define LAS __attribute__((address_space(3)))
#define LDS(T, a) ((LAS T*)(size_t)(a))

constexpr int L = 2048, D = 640, NS = 192, F = 1536;

constexpr size_t WS_UT = 0;
constexpr size_t WS_LN = 4u << 20;
constexpr size_t WS_HT = 8u << 20;
constexpr size_t WS_HM = 12u << 20;
constexpr size_t WS_W1 = 32u << 20;
constexpr size_t WS_WD = 36u << 20;
constexpr size_t WS_PT = 40u << 20;
constexpr size_t WS_FL = 52u << 20;
constexpr size_t WS_DBG = 24u << 20;
constexpr int FL_STARTED = 1024;
constexpr int FL_U = 2048;
constexpr int FL_W = FL_U + 64 * 32;
constexpr int FL_H = FL_W + 96 * 32;
constexpr int FL_P = FL_H + 160 * 32;
constexpr int UT_ROW = 4160;

struct Args {
  const u16 *x, *Are, *Aim, *Bre, *Bim, *Cre, *Cim, *logdt, *Dskip, *Wg, *Wu, *Wd;
  u16* out;
  char* ws;
};

__device__ __forceinline__ float bf2f(u16 v) { return __uint_as_float(((u32)v) << 16); }
__device__ __forceinline__ u32 pack_bf16(float lo, float hi) { return __builtin_bit_cast(u32, __builtin_convertvector((f32x2){lo, hi}, bf16x2)); }
__device__ __forceinline__ u32 pack_f16(float lo, float hi) { return __builtin_bit_cast(u32, __builtin_convertvector((f32x2){lo, hi}, f16x2)); }
__device__ __forceinline__ float rbf(float f) { return __uint_as_float(pack_bf16(f, f) << 16); }
__device__ __forceinline__ float lo_bf(u32 v) { return __uint_as_float(v << 16); }
__device__ __forceinline__ float hi_bf(u32 v) { return __uint_as_float(v & 0xffff0000u); }

#define STAMP(i)
#define WAIT_VM(n) asm volatile("s_waitcnt vmcnt(%0)" ::"n"(n) : "memory")
#define WAIT_LGKM0() do { asm volatile("" ::: "memory"); __builtin_amdgcn_s_waitcnt(0xC07F); asm volatile("" ::: "memory"); } while (0)
#define BARRIER() do { asm volatile("" ::: "memory"); __builtin_amdgcn_s_barrier(); asm volatile("" ::: "memory"); } while (0)
#define SB() __builtin_amdgcn_sched_barrier(0)
#define SG(m, n) __builtin_amdgcn_sched_group_barrier(m, n, 0)
#define RLX_AGENT __ATOMIC_RELAXED, __HIP_MEMORY_SCOPE_AGENT

__device__ __forceinline__ void glds16(const void* gsrc, u32 lds_dst) {
  u32 keep;
  asm volatile("s_mov_b32 %0, m0\n\ts_mov_b32 m0, %2\n\ts_nop 0\n\tglobal_load_lds_dwordx4 %1, off\n\ts_mov_b32 m0, %0"
               : "=&s"(keep) : "v"(gsrc), "s"(lds_dst) : "memory");
}
__device__ __forceinline__ void glds16d(const void* gsrc, u32 lds_dst) {
  u32 keep;
  asm volatile("s_mov_b32 %0, m0\n\ts_mov_b32 m0, %2\n\ts_nop 0\n\tglobal_load_lds_dwordx4 %1, off sc1\n\ts_mov_b32 m0, %0"
               : "=&s"(keep) : "v"(gsrc), "s"(lds_dst) : "memory");
}
#define UNI(v) ((u32)__builtin_amdgcn_readfirstlane((int)(v)))
__device__ __forceinline__ void st16d(void* p, u32x4 v) {
  asm volatile("global_store_dwordx4 %0, %1, off sc1\n\ts_nop 1" :: "v"(p), "v"(v) : "memory");
}
constexpr u64 SPIN_TICKS = 300000;
#define POLL_SLEEP 2
template <int NL>
__device__ __forceinline__ void wait_flags(const u32* flags, int n, u32 tag, int lane) {
  const u64 t0 = __builtin_amdgcn_s_memrealtime();
  for (;;) {
    bool ok = true;
#pragma unroll
    for (int k = 0; k < NL; ++k) {
      const int i = k * 64 + lane;
      ok &= __hip_atomic_load((gu32*)(flags + (i < n ? i : n - 1) * 32), RLX_AGENT) == tag;
    }
    if (__all(ok)) break;
    if (__builtin_amdgcn_s_memrealtime() - t0 > SPIN_TICKS) break;
    __builtin_amdgcn_s_sleep(POLL_SLEEP);
  }
  asm volatile("" ::: "memory");
}
__device__ __forceinline__ void set_flag(u32* flag, u32 tag) { __hip_atomic_store((gu32*)flag, tag, RLX_AGENT); }

__device__ __forceinline__ s16x4 tr_read(u32 addr) { return __builtin_amdgcn_ds_read_tr16_b64_v4i16(LDS(s16x4, addr)); }
__device__ __forceinline__ u32x4 tr_read2(u32 a0, u32 a1) {
  const s16x4 a = tr_read(a0), b = tr_read(a1);
  return __builtin_bit_cast(u32x4, __builtin_shufflevector(a, b, 0, 1, 2, 3, 4, 5, 6, 7));
}
__device__ __forceinline__ bf16x8 tr_frag(u32 a0, u32 a1) { return __builtin_bit_cast(bf16x8, tr_read2(a0, a1)); }
__device__ __forceinline__ f32x4 mfma16(bf16x8 a, bf16x8 b, f32x4 c) { return __builtin_amdgcn_mfma_f32_16x16x32_bf16(a, b, c, 0, 0, 0); }
#define ONES_BF16 __builtin_bit_cast(bf16x8, (u32x4){0x3f803f80u, 0x3f803f80u, 0x3f803f80u, 0x3f803f80u})
__device__ __forceinline__ float diag16(f32x4 a, int lane) { const int r = lane & 3; return r == 0 ? a[0] : r == 1 ? a[1] : r == 2 ? a[2] : a[3]; }

template <int CTRL> __device__ __forceinline__ float dpp_f(float v) {
  return __builtin_bit_cast(float, __builtin_amdgcn_update_dpp(0, __builtin_bit_cast(int, v), CTRL, 0xf, 0xf, true));
}
__device__ __forceinline__ float wave_max(float v) {
  v = fmaxf(v, dpp_f<0xB1>(v));
  v = fmaxf(v, dpp_f<0x4E>(v));
  v = fmaxf(v, dpp_f<0x141>(v));
  v = fmaxf(v, dpp_f<0x140>(v));
  { auto r = __builtin_amdgcn_permlane16_swap(__float_as_uint(v), __float_as_uint(v), false, false); v = fmaxf(__uint_as_float(r[0]), __uint_as_float(r[1])); }
  { auto r = __builtin_amdgcn_permlane32_swap(__float_as_uint(v), __float_as_uint(v), false, false); v = fmaxf(__uint_as_float(r[0]), __uint_as_float(r[1])); }
  return v;
}
__device__ __forceinline__ u32x4 widen16(u32x2 a, u32x2 b) {
  const auto s0 = __builtin_amdgcn_permlane16_swap(a[0], b[0], false, false);
  const auto s1 = __builtin_amdgcn_permlane16_swap(a[1], b[1], false, false);
  return (u32x4){s0[0], s1[0], s0[1], s1[1]};
}

constexpr int LDS_BYTES = 163840;
constexpr int OFF_TAG = LDS_BYTES - 16;
constexpr int C_ROW = 272;
constexpr int C_SCR = 60 * C_ROW;
constexpr int C_KCH = 4608;
constexpr int C_UCP = 4160, C_UCH = 2 * C_UCP;
constexpr int F_OFF_P = 4 * C_SCR;
constexpr int F_OFF_S = F_OFF_P + 9216;
constexpr int F_OFF_K = 74752;
constexpr int F_OFF_U = F_OFF_K + 4 * C_KCH;
constexpr int F_OFF_L = F_OFF_U + 4 * C_UCH;
constexpr int LN_RS = 1344;
constexpr int G1_STG = 20480, G1_NS = 7, G1_NK = 20;
constexpr int G2_STG = 36864, G2_NK = 12;
constexpr int G2_OFF_H = G2_STG * 4;
static_assert(F_OFF_L + 16384 <= OFF_TAG && G1_STG * G1_NS <= OFF_TAG && G2_OFF_H + 10240 <= OFF_TAG, "LDS");

__device__ __forceinline__ void ln1_task(const Args& A, int task, u32 lds0, int tid, int lane, int wave) {
  char* uT = A.ws + WS_UT;
  const int t0 = task * 32;
  const u32 tX = lds0, tU = lds0 + 40960;
#pragma unroll
  for (int k = 0; k < 5; ++k) {
    const int P = (wave * 5 + k) * 64 + lane, row = P / 80, chk = (P % 80) ^ (row & 15);
    glds16((const char*)A.x + (size_t)(t0 + row) * 1280 + chk * 16, UNI(tX + (wave * 5 + k) * 1024));
  }
  WAIT_VM(0);
  BARRIER();
  STAMP(1);
  {
    const int row = 4 * wave + (lane >> 4);
    f32x2 xv[20];
#pragma unroll
    for (int k = 0; k < 5; ++k) {
      const u32x4 w = *LDS(u32x4, tX + row * 1280 + (((lane & 15) + 16 * k) ^ (row & 15)) * 16);
#pragma unroll
      for (int e = 0; e < 4; ++e) xv[4 * k + e] = (f32x2){lo_bf(w[e]), hi_bf(w[e])};
    }
    f32x2 a1 = xv[0] + xv[1], a2 = xv[0] * xv[0], b1 = xv[2] + xv[3], b2 = xv[1] * xv[1];
    a2 = __builtin_elementwise_fma(xv[2], xv[2], a2); b2 = __builtin_elementwise_fma(xv[3], xv[3], b2);
#pragma unroll
    for (int k = 4; k < 20; k += 2) {
      a1 += xv[k]; b1 += xv[k + 1];
      a2 = __builtin_elementwise_fma(xv[k], xv[k], a2); b2 = __builtin_elementwise_fma(xv[k + 1], xv[k + 1], b2);
    }
    float s1 = (a1.x + a1.y) + (b1.x + b1.y), s2 = (a2.x + a2.y) + (b2.x + b2.y);
    s1 += dpp_f<0xB1>(s1); s2 += dpp_f<0xB1>(s2);
    s1 += dpp_f<0x4E>(s1); s2 += dpp_f<0x4E>(s2);
    s1 += dpp_f<0x141>(s1); s2 += dpp_f<0x141>(s2);
    s1 += dpp_f<0x140>(s1); s2 += dpp_f<0x140>(s2);
    const float mean = s1 * (1.f / D);
    const float var = fmaxf(s2 * (1.f / D) - mean * mean, 0.f);
    const float mb = rbf(mean), sd = rbf(__builtin_amdgcn_sqrtf(rbf(rbf(var) + 1e-5f)));
    const float ri = __builtin_amdgcn_rcpf(sd), mi = -mb * ri;
    if ((lane & 15) == 0)
      __hip_atomic_store((gu64*)((u64*)(A.ws + WS_LN) + t0 + row), ((u64)__float_as_uint(mb) << 32) | __float_as_uint(sd), RLX_AGENT);
#pragma unroll
    for (int k = 0; k < 5; ++k) {
      u32x4 o;
#pragma unroll
      for (int e = 0; e < 4; ++e) {
        const f32x2 u = __builtin_elementwise_fma(xv[4 * k + e], (f32x2){ri, ri}, (f32x2){mi, mi});
        o[e] = pack_f16(u.x, u.y);
      }
      *LDS(u32x4, tU + row * LN_RS + ((lane & 15) + 16 * k) * 16) = o;
    }
  }
  WAIT_LGKM0();
  BARRIER();
  STAMP(2);
  {
    const int g = lane >> 4, q = (lane & 15) >> 2, p = lane & 3;
    u32x4 ov[5];
#pragma unroll
    for (int k = 0; k < 5; ++k) {
      const u32 a = tU + (8 * g + q) * LN_RS + ((wave * 5 + k) * 16 + 4 * p) * 2;
      ov[k] = tr_read2(a, a + 4 * LN_RS);
    }
#pragma unroll
    for (int k = 0; k < 5; ++k) st16d(uT + (size_t)((wave * 5 + k) * 16 + (lane & 15)) * UT_ROW + 32 + (t0 + 8 * g) * 2, ov[k]);
  }
  WAIT_VM(0);
  BARRIER();
  if (tid == 0) set_flag((u32*)(A.ws + WS_FL) + FL_U + task * 32, *LDS(u32, lds0 + OFF_TAG));
  STAMP(3);
}

__device__ __forceinline__ void weight_task(const Args& A, int wtask, u32 lds0, int tid, int lane, int wave) {
  const int g = lane >> 4, q = (lane & 15) >> 2, p = lane & 3;
  if (wtask < 48) {
    const u16* W = wtask < 24 ? A.Wg : A.Wu;
    const int c0 = (wtask % 24) * 64;
    {
      const int row = 8 * wave + (lane >> 3), chk = (lane & 7) ^ ((row & 3) << 1);
      const char* src = (const char*)W + (size_t)row * 3072 + c0 * 2 + chk * 16;
#pragma unroll
      for (int jj = 0; jj < 10; ++jj) glds16(src + (size_t)jj * 64 * 3072, UNI(lds0 + (wave + 8 * jj) * 1024));
    }
    WAIT_VM(0);
    BARRIER();
    const int tl = wave & 3, ks0 = (wave >> 2) * 10;
    const u32 a = lds0 + (32 * ks0 + 8 * g + q) * 128 + (((2 * tl + (p >> 1)) ^ (q << 1)) * 16) + 8 * (p & 1);
    bf16x8 fr[10];
    f32x4 cs = {0.f, 0.f, 0.f, 0.f};
#pragma unroll
    for (int k = 0; k < 10; ++k) { fr[k] = tr_frag(a + k * 4096, a + k * 4096 + 512); cs = mfma16(ONES_BF16, fr[k], cs); }
    WAIT_LGKM0();
    BARRIER();
    if (lane < 16) *LDS(float, lds0 + (wave * 16 + lane) * 4) = cs[0];
    WAIT_LGKM0();
    BARRIER();
    const float cm = (*LDS(float, lds0 + (tl * 16 + (lane & 15)) * 4) + *LDS(float, lds0 + ((tl + 4) * 16 + (lane & 15)) * 4)) * (1.f / D);
    const int f = c0 + 16 * tl, pn = f / 96, fi = (f % 96) / 16;
    const int slot = (fi / 3) * 6 + (wtask < 24 ? 0 : 3) + fi % 3;
    char* dst = A.ws + WS_W1 + (((size_t)pn * 20 + ks0) * 12 + slot) * 1024 + lane * 16;
#pragma unroll
    for (int k = 0; k < 10; ++k) {
      const u32x4 w = __builtin_bit_cast(u32x4, fr[k]);
      u32x4 o;
#pragma unroll
      for (int e = 0; e < 4; ++e) o[e] = pack_bf16(lo_bf(w[e]) - cm, hi_bf(w[e]) - cm);
      st16d(dst + (size_t)k * 12288, o);
    }
  } else {
    const int ks = wtask - 48;
#pragma unroll
    for (int jj = 0; jj < 5; ++jj) {
      const int P = 64 * (wave + 8 * jj) + lane, row = P / 80, chk = P % 80;
      glds16((const char*)A.Wd + (size_t)(32 * ks + row) * 1280 + chk * 16, UNI(lds0 + (wave + 8 * jj) * 1024));
    }
    WAIT_VM(0);
    BARRIER();
#pragma unroll
    for (int jj = 0; jj < 5; ++jj) {
      const int fr = wave + 8 * jj;
      const u32 a = lds0 + (8 * g + q) * 1280 + (16 * fr + 4 * p) * 2;
      st16d(A.ws + WS_WD + (((size_t)(fr / 5) * 48 + ks) * 5 + fr % 5) * 1024 + lane * 16, tr_read2(a, a + 4 * 1280));
    }
  }
  WAIT_VM(0);
  BARRIER();
  if (tid == 0) set_flag((u32*)(A.ws + WS_FL) + FL_W + wtask * 32, *LDS(u32, lds0 + OFF_TAG));
}

struct cplx { f32x2 v, r; };
__device__ __forceinline__ cplx mk(f32x2 v) { return cplx{v, (f32x2){-v.y, v.x}}; }
__device__ __forceinline__ f32x2 cmul(f32x2 a, const cplx& w) {
  const f32x2 t = (f32x2){a.x, a.x} * w.v;
  return __builtin_elementwise_fma((f32x2){a.y, a.y}, w.r, t);
}
__device__ __forceinline__ f32x2 cexp(float zr, float zi, float l, float sgn) {
  const float m = __builtin_amdgcn_exp2f(zr * l * 1.44269504088896341f);
  const float a = __builtin_amdgcn_fractf(zi * l * 0.15915494309189535f);
  return (f32x2){m * __builtin_amdgcn_cosf(a), sgn * m * __builtin_amdgcn_sinf(a)};
}
__device__ __forceinline__ u32 pkh(f32x2 v) { return __builtin_bit_cast(u32, __builtin_convertvector(v, f16x2)); }
__device__ __forceinline__ u32 cmulh(u32 a, u32 b) {
  u32 t, r;
  asm("v_pk_mul_f16 %0, %1, %2 op_sel:[0,0] op_sel_hi:[0,1]" : "=v"(t) : "v"(a), "v"(b));
  asm("v_pk_fma_f16 %0, %1, %2, %3 op_sel:[1,1,0] op_sel_hi:[1,0,1] neg_lo:[0,1,0]" : "=v"(r) : "v"(a), "v"(b), "v"(t));
  return r;
}
#define SET_M0(base) asm volatile("s_mov_b32 m0, %0\n\ts_nop 0" ::"s"(base) : "memory")
#define WR4(row, a, b, c, d)                                                                                    \
  asm volatile("ds_write_addtid_b32 %0 offset:%4\n\tds_write_addtid_b32 %1 offset:%4+272\n\t"                    \
               "ds_write_addtid_b32 %2 offset:%4+544\n\tds_write_addtid_b32 %3 offset:%4+816"                    \
               ::"v"(a), "v"(b), "v"(c), "v"(d), "n"((row) * C_ROW) : "memory")

__device__ __forceinline__ void conv_role(const Args& A, int grp, u32 lds0, int tid, int lane, int wave, u32 tag) {
  const int ch = wave & 3, hf = wave >> 2, d0 = grp * 4, d = d0 + ch;
  const u32 sb = UNI(lds0 + ch * C_SCR);
  const int r32 = lane & 31, h32 = lane >> 5;
  const int j = lane & 15, g = lane >> 4;
  {
    const u64 q0 = (u64)A.Are, q1 = (u64)A.Aim, q2 = (u64)A.Bre, q3 = (u64)A.Bim, q4 = (u64)A.Cre, q5 = (u64)A.Cim;
#pragma unroll
    for (int rep = 0; rep < 2; ++rep) {
      if (rep == 1 && wave != 0) break;
      const int i = rep ? 8 : wave;
      const int b = 1024 * i + 16 * lane, a = b / 1536, o = b - a * 1536;
      u64 src = +q0;
      src = a == 1 ? +q1 : src; src = a == 2 ? +q2 : src; src = a == 3 ? +q3 : src; src = a == 4 ? +q4 : src; src = a == 5 ? +q5 : src;
      glds16((const char*)(src + (u64)d0 * 384 + o), UNI(lds0 + F_OFF_P + i * 1024));
    }
  }
  const u32 wdt = ((const u32*)A.logdt)[d >> 1], wds = ((const u32*)A.Dskip)[d >> 1];
  const float dt = rbf(__expf((d & 1) ? hi_bf(wdt) : lo_bf(wdt)));
  WAIT_VM(0);
  BARRIER();
  float zr[3], zi[3];
  const u32 pl = lds0 + F_OFF_P + ch * 384 + lane * 2;
#pragma unroll
  for (int r = 0; r < 3; ++r) {
    zr[r] = dt * bf2f(*LDS(u16, pl + r * 128));
    zi[r] = dt * bf2f(*LDS(u16, pl + 1536 + r * 128));
  }
  u32 an[3][11];
  if (hf == 0) {
    f32x2 cb[3];
    float bound = 0.f;
#pragma unroll
    for (int r = 0; r < 3; ++r) {
      const float br = dt * bf2f(*LDS(u16, pl + 2 * 1536 + r * 128)), bi = dt * bf2f(*LDS(u16, pl + 3 * 1536 + r * 128));
      const float cr = bf2f(*LDS(u16, pl + 4 * 1536 + r * 128)), ci = bf2f(*LDS(u16, pl + 5 * 1536 + r * 128));
      cb[r] = (f32x2){cr * br - ci * bi, -(cr * bi + ci * br)};
      const float grow = __builtin_amdgcn_exp2f(fmaxf(zr[r], 0.f) * (2064.f * 1.44269504f));
      bound = fmaxf(bound, (fabsf(cb[r].x) + fabsf(cb[r].y)) * grow);
    }
    bound = wave_max(bound);
    int ex = (int)((__float_as_uint(bound) >> 23) & 255);
    ex = ex < 20 ? 20 : (ex > 240 ? 240 : ex);
    const float scale = __uint_as_float((u32)(267 - ex) << 23);
    if (lane == 0) *LDS(float, lds0 + F_OFF_S + ch * 4) = __uint_as_float((u32)(ex - 5) << 23);
#pragma unroll
    for (int r = 0; r < 3; ++r) {
      const cplx w1 = mk(cexp(zr[r], zi[r], 1.f, -1.f));
      const cplx w512 = mk(cexp(zr[r], zi[r], 512.f, -1.f));
      const cplx w2 = mk(cmul(w1.v, w1));
      const f32x2 w3 = cmul(w2.v, w1);
      const cplx w4 = mk(cmul(w2.v, w2));
      const f32x2 w1024 = cmul(w512.v, w512), w1536 = cmul(w1024, w512);
      const f32x2 P0 = cb[r] * scale, P4 = cmul(P0, w4), P8 = cmul(P4, w4), P12 = cmul(P8, w4), P16 = cmul(P12, w4);
      an[r][0] = pkh(P0); an[r][1] = pkh(P4); an[r][2] = pkh(P8); an[r][3] = pkh(P12); an[r][4] = pkh(P16);
      an[r][5] = pkh(w1.v); an[r][6] = pkh(w2.v); an[r][7] = pkh(w3);
      an[r][8] = pkh(w512.v); an[r][9] = pkh(w1024); an[r][10] = pkh(w1536);
    }
  } else {
#pragma unroll
    for (int r = 0; r < 3; ++r) {
      const cplx w16 = mk(cexp(zr[r], zi[r], 16.f, 1.f));
      const cplx w128 = mk(cexp(zr[r], zi[r], 128.f, 1.f));
      const cplx w32 = mk(cmul(w16.v, w16));
      const f32x2 w48 = cmul(w32.v, w16);
      const f32x2 w64 = cmul(w32.v, w32);
      const f32x2 w192 = cmul(w64, w128), w256 = cmul(w128.v, w128);
      const f32x2 w320 = cmul(w192, w128), w384 = cmul(w256, w128), w448 = cmul(w320, w128);
      an[r][0] = pkh(w64); an[r][1] = pkh(w128.v); an[r][2] = pkh(w192); an[r][3] = pkh(w256);
      an[r][4] = pkh(w320); an[r][5] = pkh(w384); an[r][6] = pkh(w448);
      an[r][7] = pkh(w16.v); an[r][8] = pkh(w32.v); an[r][9] = pkh(w48); an[r][10] = 0x00003c00u;
    }
  }
  u32 arow;
  {
    const int hr = (r32 >> 2) & 1, rho = (r32 & 3) + 4 * (r32 >> 3);
    const int row = rho <= 8 ? rho + 8 * hr : rho <= 11 ? rho + 8 + 2 * hr : rho <= 13 ? rho + 10 + hr : rho + 11 + hr;
    arow = sb + row * C_ROW + h32 * 16 + hf * 128;
  }
  const u32 brow = sb + (28 + r32) * C_ROW + h32 * 16 + hf * 128;
  f32x16 kacc;
#pragma unroll
  for (int i = 0; i < 16; ++i) kacc[i] = 0.f;
  SET_M0(sb);
#pragma unroll
  for (int r = 0; r < 3; ++r) {
    u32 gv[2][4];
    auto grp4 = [&](int gi, u32 (&o)[4]) {
      const u32* a = an[r];
      if (hf == 0) {
        if (gi < 4) { o[0] = a[gi]; o[1] = cmulh(a[gi], a[5]); o[2] = cmulh(a[gi], a[6]); o[3] = cmulh(a[gi], a[7]); }
        else if (gi == 4) { o[0] = a[4]; o[1] = cmulh(a[0], a[8]); o[2] = cmulh(a[1], a[8]); o[3] = cmulh(a[2], a[8]); }
        else if (gi == 5) { o[0] = cmulh(a[3], a[8]); o[1] = cmulh(a[4], a[8]); o[2] = cmulh(a[0], a[9]); o[3] = cmulh(a[2], a[9]); }
        else if (gi == 6) { o[0] = cmulh(a[4], a[9]); o[1] = cmulh(a[0], a[10]); o[2] = cmulh(a[2], a[10]); o[3] = cmulh(a[4], a[10]); }
      } else {
        if (gi == 0) { o[0] = a[10]; o[1] = a[7]; o[2] = a[8]; o[3] = a[9]; }
        else { o[0] = a[gi - 1]; o[1] = cmulh(a[gi - 1], a[7]); o[2] = cmulh(a[gi - 1], a[8]); o[3] = cmulh(a[gi - 1], a[9]); }
      }
    };
    if (hf == 0) {
      grp4(0, gv[0]);
#pragma unroll
      for (int gi = 0; gi < 7; ++gi) {
        if (gi < 6) grp4(gi + 1, gv[(gi + 1) & 1]);
        SB();
        WR4(4 * gi, gv[gi & 1][0], gv[gi & 1][1], gv[gi & 1][2], gv[gi & 1][3]);
        SB();
      }
    } else {
      grp4(0, gv[0]);
#pragma unroll
      for (int gi = 0; gi < 8; ++gi) {
        if (gi < 7) grp4(gi + 1, gv[(gi + 1) & 1]);
        SB();
        WR4(28 + 4 * gi, gv[gi & 1][0], gv[gi & 1][1], gv[gi & 1][2], gv[gi & 1][3]);
        SB();
      }
    }
    WAIT_LGKM0();
    BARRIER();
    f16x8 fa[4], fb[4];
#pragma unroll
    for (int k = 0; k < 4; ++k) { fa[k] = *LDS(f16x8, arow + k * 32); fb[k] = *LDS(f16x8, brow + k * 32); }
    WAIT_LGKM0();
    BARRIER();
#pragma unroll
    for (int k = 0; k < 4; ++k) kacc = __builtin_amdgcn_mfma_f32_32x32x16_f16(fa[k], fb[k], kacc, 0, 0, 0);
  }
  STAMP(1);
  {
    const u32 ex = sb + hf * 4096 + lane * 16, exr = sb + (hf ^ 1) * 4096 + lane * 16;
    if (hf == 0) {
      *LDS(f32x4, ex) = (f32x4){kacc[12], kacc[13], kacc[14], kacc[15]};
    } else {
      *LDS(f32x4, ex) = (f32x4){kacc[0], kacc[1], kacc[2], kacc[3]};
      *LDS(f32x4, ex + 1024) = (f32x4){kacc[4], kacc[5], kacc[6], kacc[7]};
      *LDS(f32x4, ex + 2048) = (f32x4){kacc[8], kacc[9], kacc[10], kacc[11]};
    }
    WAIT_LGKM0();
    BARRIER();
    const u32 kb = lds0 + F_OFF_K + ch * C_KCH + 4080 - 32 * r32 - 16 * h32;
    auto put = [&](int base, float e0, float e1, float e2, float e3, float e4, float e5, float e6, float e7) {
      *LDS(u32x4, kb - 2 * base) = (u32x4){pack_f16(e7, e6), pack_f16(e5, e4), pack_f16(e3, e2), pack_f16(e1, e0)};
    };
    if (hf == 0) {
      const f32x4 o0 = *LDS(f32x4, exr), o1 = *LDS(f32x4, exr + 1024), o2 = *LDS(f32x4, exr + 2048);
      float v[12];
#pragma unroll
      for (int i = 0; i < 4; ++i) {
        v[i] = (kacc[i] + o0[i]) * (1.f / 256.f); v[4 + i] = (kacc[4 + i] + o1[i]) * (1.f / 256.f); v[8 + i] = (kacc[8 + i] + o2[i]) * (1.f / 256.f);
      }
      const bool far = r32 >= 16;
      put(0, v[0], far ? v[0] : v[1], v[2], far ? v[4] : v[3], v[4], far ? v[4] : v[5], v[6], far ? v[8] : v[7]);
      put(512, v[9], v[9], v[9], v[10], v[10], v[10], v[11], v[11]);
    } else {
      const f32x4 o = *LDS(f32x4, exr);
      const float v12 = (kacc[12] + o[0]) * (1.f / 256.f), v13 = (kacc[13] + o[1]) * (1.f / 256.f),
                  v14 = (kacc[14] + o[2]) * (1.f / 256.f), v15 = (kacc[15] + o[3]) * (1.f / 256.f);
      put(1024, v12, v12, v12, v12, h32 ? v13 : v12, v13, v13, v13);
      put(1536, v14, v14, v14, v14, h32 ? v15 : v14, v15, v15, v15);
    }
  }
  if (hf == 1 && lane < 32) *LDS(u32x4, lds0 + F_OFF_K + ch * C_KCH + 4096 + lane * 16) = (u32x4){0u, 0u, 0u, 0u};
  STAMP(2);
  if (wave == 7) wait_flags<1>((const u32*)(A.ws + WS_FL) + FL_U, 64, tag, lane);
  WAIT_LGKM0();
  BARRIER();
  STAMP(3);
  {
    const char* up = A.ws + WS_UT + (size_t)d * UT_ROW + lane * 16 + hf * 2;
    const u32 dst = UNI(lds0 + F_OFF_U + ch * C_UCH + hf * C_UCP);
#pragma unroll
    for (int k = 0; k < 4; ++k) glds16d(up + k * 1024, dst + k * 1024);
    if (lane < 2) glds16d(up + 4096, dst + 4096);
    glds16d(A.ws + WS_LN + wave * 2048 + lane * 16, UNI(lds0 + F_OFF_L + wave * 2048));
    glds16d(A.ws + WS_LN + wave * 2048 + 1024 + lane * 16, UNI(lds0 + F_OFF_L + wave * 2048 + 1024));
  }
  WAIT_VM(0);
  if (lane < 8) {
    const u32 zb = lds0 + F_OFF_U + ch * C_UCH + hf * C_UCP;
    if (lane < 7 || hf == 0) *LDS(u32, zb + lane * 4) = 0u; else *LDS(u16, zb + 28) = (u16)0;
  }
  WAIT_LGKM0();
  BARRIER();
  STAMP(4);
  const int cc = (1 + j) & 1;
  const u32 aa = lds0 + F_OFF_U + ch * C_UCH + cc * C_UCP + 2 * (1 + j + 8 * g - cc) + hf * 256;
  const u32 ba = lds0 + F_OFF_K + ch * C_KCH + 32 * j + 16 * g + hf * 256;
  f32x4 acc[8];
#pragma unroll
  for (int i = 0; i < 8; ++i) acc[i] = (f32x4){0.f, 0.f, 0.f, 0.f};
  {
    f16x8 Af[2][8], Bf[2][8];
    u32 am[4];
#pragma unroll
    for (int k = 0; k < 4; ++k) { am[k] = aa + 1024 * k; asm volatile("" : "+v"(am[k])); }
    auto rd = [&](int set, int r) {
#pragma unroll
      for (int m = 0; m < 8; ++m) {
        const LAS u32* ap = LDS(const u32, am[m >> 1] + 64 * r + 512 * (m & 1));
        Af[set][m] = __builtin_bit_cast(f16x8, (u32x4){ap[0], ap[1], ap[2], ap[3]});
        Bf[set][m] = *LDS(f16x8, ba + 64 * r + 512 * (7 - m));
      }
    };
    auto mm = [&](int set) {
#pragma unroll
      for (int sdx = 0; sdx < 8; ++sdx)
#pragma unroll
        for (int m = 0; m <= sdx; ++m) acc[sdx] = __builtin_amdgcn_mfma_f32_16x16x32_f16(Af[set][m], Bf[set][sdx - m], acc[sdx], 0, 0, 0);
    };
#define C_SCHED() do { _Pragma("unroll") for (int k_ = 0; k_ < 24; ++k_) { SG(0x008, 1); SG(0x100, 1); } SG(0x008, 12); } while (0)
    rd(0, 0);
    SB(); rd(1, 1); mm(0); C_SCHED(); SB();
    rd(0, 2); mm(1); C_SCHED(); SB();
    rd(1, 3); mm(0); C_SCHED(); SB();
    mm(1);
  }
  STAMP(5);
  {
    const u32 ex = lds0 + wave * 4096 + lane * 16, exr = lds0 + (wave ^ 4) * 4096 + lane * 16;
    if (hf == 0) {
#pragma unroll
      for (int i = 0; i < 4; ++i) *LDS(f32x4, ex + i * 1024) = acc[4 + i];
    } else {
#pragma unroll
      for (int i = 0; i < 4; ++i) *LDS(f32x4, ex + i * 1024) = acc[i];
    }
    const float it = *LDS(float, lds0 + F_OFF_S + ch * 4);
    const float dsk = (d & 1) ? hi_bf(wds) : lo_bf(wds);
    u32x2 dv[4], xv[4];
#pragma unroll
    for (int i = 0; i < 4; ++i) {
      const int t = 16 * (16 * (4 * hf + i) + 15 - j) + 4 * g;
      const u32x2 uw = *LDS(u32x2, lds0 + F_OFF_U + ch * C_UCH + 2 * (16 + t));
      const u32 w0 = uw[0], w1 = uw[1];
      const f16x2 u01 = __builtin_bit_cast(f16x2, w0), u23 = __builtin_bit_cast(f16x2, w1);
      const float uf[4] = {(float)u01[0], (float)u01[1], (float)u23[0], (float)u23[1]};
      dv[i] = (u32x2){pack_bf16(uf[0] * dsk, uf[1] * dsk), pack_bf16(uf[2] * dsk, uf[3] * dsk)};
      const f32x4 l01 = *LDS(f32x4, lds0 + F_OFF_L + t * 8), l23 = *LDS(f32x4, lds0 + F_OFF_L + t * 8 + 16);
      xv[i] = (u32x2){pack_bf16(fmaf(uf[0], l01[0], l01[1]), fmaf(uf[1], l01[2], l01[3])), pack_bf16(fmaf(uf[2], l23[0], l23[1]), fmaf(uf[3], l23[2], l23[3]))};
    }
    WAIT_LGKM0();
    BARRIER();
    f32x4 y[4];
#pragma unroll
    for (int i = 0; i < 4; ++i) y[i] = *LDS(f32x4, exr + i * 1024);
    SB();
#pragma unroll
    for (int i = 0; i < 4; ++i) { const f32x4 a0 = acc[i], a1 = acc[4 + i]; y[i] = ((hf ? +a1 : +a0) + y[i]) * it; }
    u32x2 w[4];
#pragma unroll
    for (int i = 0; i < 4; ++i) w[i] = (u32x2){pack_bf16(y[i][0], y[i][1]), pack_bf16(y[i][2], y[i][3])};
#pragma unroll
    for (int i = 0; i < 4; ++i)
      w[i] = (u32x2){pack_bf16(lo_bf(w[i][0]) + lo_bf(dv[i][0]), hi_bf(w[i][0]) + hi_bf(dv[i][0])), pack_bf16(lo_bf(w[i][1]) + lo_bf(dv[i][1]), hi_bf(w[i][1]) + hi_bf(dv[i][1]))};
    u32x2 hv[4];
#pragma unroll
    for (int i = 0; i < 4; ++i)
      hv[i] = (u32x2){pack_bf16(lo_bf(xv[i][0]) + lo_bf(w[i][0]), hi_bf(xv[i][0]) + hi_bf(w[i][0])), pack_bf16(lo_bf(xv[i][1]) + lo_bf(w[i][1]), hi_bf(xv[i][1]) + hi_bf(w[i][1]))};
    char* hrow = A.ws + WS_HT + (size_t)d * 4096;
#pragma unroll
    for (int i = 0; i < 4; i += 2) st16d(hrow + (16 * (16 * (4 * hf + i + (g & 1)) + 15 - j) + 8 * (g >> 1)) * 2, widen16(hv[i], hv[i + 1]));
  }
  WAIT_VM(0);
  BARRIER();
  if (tid == 0) set_flag((u32*)(A.ws + WS_FL) + FL_H + grp * 32, *LDS(u32, lds0 + OFF_TAG));
  STAMP(6);
}

__device__ __forceinline__ void gemm1_loader(const Args& A, u32 lds0, int lane, int wave, u32 tag) {
  const int xcd = blockIdx.x & 7, idx = blockIdx.x >> 3;
  const int pm = (xcd & 1) * 8 + (idx & 7), pn = (xcd >> 1) * 4 + (idx >> 3);
  const int t0 = pm * 128;
  const u32* flags = (const u32*)(A.ws + WS_FL);
    const int lw = wave - 4;
    int kH = 5 * (pn & 3), kW = kH;
    const int row = 8 * lw + (lane >> 4);
    const int chk = (lane & 15) ^ (((row & 3) << 1) | (((row >> 3) & 1) << 3));
    const char* srcH = A.ws + WS_HT + (size_t)(row + 32 * kH) * 4096 + t0 * 2 + chk * 16;
    const char* srcW = A.ws + WS_W1 + (size_t)pn * 245760 + (size_t)kW * 12288 + lw * 3072 + lane * 16;
    const u32 dH = lds0 + lw * 2048, dW = lds0 + 8192 + lw * 3072;
    auto issueH = [&](u32 so) {
      glds16d(srcH, dH + so);
      glds16d(srcH + 4 * 4096, dH + so + 1024);
      if (++kH == G1_NK) { kH = 0; srcH -= (size_t)19 * 32 * 4096; } else srcH += 32 * 4096;
    };
    auto issueW = [&](u32 so) {
      glds16d(srcW, dW + so);
      glds16d(srcW + 1024, dW + so + 1024);
      glds16d(srcW + 2048, dW + so + 2048);
      if (++kW == G1_NK) { kW = 0; srcW -= 19 * 12288; } else srcW += 12288;
    };
    if (wave == 4) wait_flags<2>(flags + FL_W, 96, tag, lane);
    BARRIER();
#pragma unroll
    for (int s = 0; s < G1_NS; ++s) issueW(s * G1_STG);
    if (wave == 4) wait_flags<3>(flags + FL_H, 160, tag, lane);
    BARRIER();
    STAMP(7);
    issueH(0); issueH(G1_STG); issueH(2 * G1_STG);
    WAIT_VM(4); BARRIER();
    issueH(3 * G1_STG); issueH(4 * G1_STG);
    WAIT_VM(6); BARRIER();
    issueH(5 * G1_STG); issueH(6 * G1_STG);
    WAIT_VM(8); BARRIER();
    u32 so = 0;
    auto issue = [&]() { issueH(so); issueW(so); so = so == (G1_NS - 1) * G1_STG ? 0 : so + G1_STG; };
    issue(); WAIT_VM(11); BARRIER();
    issue(); WAIT_VM(14); BARRIER();
    issue(); WAIT_VM(17); BARRIER();
    for (int i = 5; i < G1_NK - 5; ++i) {
      issue();
      WAIT_VM(20); BARRIER();
    }
    WAIT_VM(15); BARRIER(); WAIT_VM(10); BARRIER(); WAIT_VM(5); BARRIER(); WAIT_VM(0); BARRIER();
}

__device__ __forceinline__ void gemm1_compute(const Args& A, u32 lds0, int lane, int wave) {
  const int xcd = blockIdx.x & 7, idx = blockIdx.x >> 3;
  const int pm = (xcd & 1) * 8 + (idx & 7), pn = (xcd >> 1) * 4 + (idx >> 3);
  const int t0 = pm * 128;
  const int wt = wave & 1, wf = wave >> 1;
  const int g = lane >> 4, q = (lane & 15) >> 2, p = lane & 3;
  const int rot = pn >> 2;
  const u32 hrow = (8 * g + q) * 256 + 8 * (p & 1);
  const u32 hx = q | ((g & 1) << 2);
  u32 hc[4];
#pragma unroll
  for (int j = 0; j < 4; ++j) hc[j] = lds0 + hrow + ((((u32)(4 * wt + ((j + rot) & 3)) ^ hx) << 1) | (p >> 1)) * 16;
  u32 wc = lds0 + 8192 + wf * 6144 + lane * 16, so = 0;
  f32x4 acc[6][4];
#pragma unroll
  for (int i = 0; i < 6; ++i)
#pragma unroll
    for (int j = 0; j < 4; ++j) acc[i][j] = (f32x4){0.f, 0.f, 0.f, 0.f};
  f32x4 st1 = {0.f, 0.f, 0.f, 0.f}, st2 = st1;
  bf16x8 hf[2][4], wfr[2][6];
  auto rd = [&](int set) {
    wfr[set][0] = *LDS(bf16x8, wc);
#pragma unroll
    for (int j = 0; j < 4; ++j) hf[set][j] = tr_frag(hc[j], hc[j] + 1024);
#pragma unroll
    for (int i = 1; i < 6; ++i) wfr[set][i] = *LDS(bf16x8, wc + i * 1024);
  };
  auto adv = [&]() {
    u32 dl = so == (G1_NS - 1) * G1_STG ? (u32)(-(G1_NS - 1) * G1_STG) : (u32)G1_STG;
    so += dl;
    asm volatile("" : "+s"(dl));
#pragma unroll
    for (int j = 0; j < 4; ++j) hc[j] += dl;
    wc += dl;
  };
  auto mma = [&](int set) {
#pragma unroll
    for (int i = 0; i < 6; ++i)
#pragma unroll
      for (int j = 0; j < 4; ++j) acc[i][j] = mfma16(wfr[set][i], hf[set][j], acc[i][j]);
  };
  auto stat = [&](int set) {
    st1 = mfma16(ONES_BF16, hf[set][0], st1);
    st2 = mfma16(hf[set][0], hf[set][0], st2);
  };
#define G1_SCHED(extra) do { \
    SG(0x008, 1); SG(0x100, 1); SG(0x008, 1); SG(0x100, 1); SG(0x008, 1); SG(0x100, 1); SG(0x008, 1); SG(0x100, 1); \
    SG(0x008, 1); SG(0x100, 1); SG(0x008, 1); SG(0x100, 1); SG(0x008, 1); SG(0x100, 1); SG(0x008, 1); SG(0x100, 1); \
    SG(0x008, 1); SG(0x100, 1); SG(0x008, 2); SG(0x100, 1); SG(0x008, 2); SG(0x100, 1); SG(0x008, 2); SG(0x100, 1); \
    SG(0x008, 2); SG(0x100, 1); SG(0x008, 1); SG(0x100, 1); SG(0x008, 1); SG(0x002, 1); SG(0x008, 1); SG(0x002, 1); \
    SG(0x008, 1); SG(0x002, 1); SG(0x008, 1); SG(0x002, 1); SG(0x008, 1); SG(0x002, 1); SG(0x008, 1 + (extra)); } while (0)
  BARRIER();
  BARRIER();
  BARRIER();
  STAMP(8);
  rd(0); adv();
#pragma unroll
  for (int i = 1; i < G1_NK - 1; i += 2) {
    BARRIER();
    SB(); rd(1); mma(0); adv();
    if (i - 1 < 5) { stat(0); G1_SCHED(2); } else { G1_SCHED(0); }
    SB();
    BARRIER();
    SB(); rd(0); mma(1); adv();
    if (i < 5) { stat(1); G1_SCHED(2); } else { G1_SCHED(0); }
    SB();
  }
  BARRIER();
  SB(); rd(1); mma(0); G1_SCHED(0); SB();
  WAIT_LGKM0();
  BARRIER();
#pragma unroll
  for (int i = 0; i < 3; ++i)
#pragma unroll
    for (int j = 0; j < 4; ++j) {
      acc[i][j] = mfma16(wfr[1][i], hf[1][j], acc[i][j]);
      acc[3 + i][j] = mfma16(wfr[1][3 + i], hf[1][j], acc[3 + i][j]);
    }
  STAMP(9);
  {
    char* ob = A.ws + WS_HM + ((size_t)(pm * 8 + 4 * wt) * 48 + 3 * pn) * 1024 + (lane & 15) * 16 + (g >> 1) * 256;
#pragma unroll
    for (int j = 0; j < 4; j += 2) {
      char* oj = ob + (size_t)((j + (g & 1) + rot) & 3) * 49152;
#pragma unroll
      for (int i = 0; i < 3; ++i) {
        u32x2 o[2];
#pragma unroll
        for (int jj = 0; jj < 2; ++jj) {
          const f32x4 gv = acc[i][j + jj], uv = acc[3 + i][j + jj];
          o[jj] = (u32x2){pack_bf16(fmaxf(gv[0], 0.f) * uv[0], fmaxf(gv[1], 0.f) * uv[1]), pack_bf16(fmaxf(gv[2], 0.f) * uv[2], fmaxf(gv[3], 0.f) * uv[3])};
        }
        const int E = 6 * wf + 2 * i;
        st16d(oj + (E >> 2) * 1024 + (E & 3) * 256, widen16(o[0], o[1]));
      }
    }
  }
  if (wf == 0 && g == ((lane & 15) >> 2))
    __hip_atomic_store((gu64*)((u64*)(A.ws + WS_PT) + (size_t)(pn & 3) * L + t0 + (4 * wt + rot) * 16 + (lane & 15)),
                       ((u64)__float_as_uint(diag16(st2, lane)) << 32) | __float_as_uint(st1[0]), RLX_AGENT);
  WAIT_VM(0);
}

__device__ __forceinline__ void gemm2_loader(const Args& A, u32 lds0, int lane, int wave, u32 tag) {
  const int xcd = blockIdx.x & 7, idx = blockIdx.x >> 3;
  const int pm = (xcd & 3) * 8 + (idx & 7), pn = (xcd >> 2) * 4 + (idx >> 3);
  const int t0 = pm * 64, d0 = pn * 80;
  u32* flags = (u32*)(A.ws + WS_FL);
    const int lw = wave - 4;
    const char* srcP = A.ws + WS_HM + (size_t)(pm * 4 + lw) * 49152 + lane * 16;
    const char* srcW = A.ws + WS_WD + (size_t)pn * 245760 + lw * 5120 + lane * 16;
    const u32 dP = lds0 + lw * 4096, dW = lds0 + 16384 + lw * 5120;
    auto issueP = [&](u32 so) {
      glds16d(srcP, dP + so); glds16d(srcP + 1024, dP + so + 1024); glds16d(srcP + 2048, dP + so + 2048); glds16d(srcP + 3072, dP + so + 3072);
      srcP += 4096;
    };
    auto issueW = [&](u32 so) {
      glds16d(srcW, dW + so); glds16d(srcW + 1024, dW + so + 1024); glds16d(srcW + 2048, dW + so + 2048);
      glds16d(srcW + 3072, dW + so + 3072); glds16d(srcW + 4096, dW + so + 4096);
      srcW += 20480;
    };
    BARRIER();
    issueW(0); issueW(G2_STG); issueW(2 * G2_STG); issueW(3 * G2_STG);
#pragma unroll
    for (int k = 0; k < 3; ++k) {
      const int jj = lw + 4 * k < 10 ? lw + 4 * k : lw + 4;
      glds16d(A.ws + WS_HT + (size_t)(d0 + 8 * jj + (lane >> 3)) * 4096 + t0 * 2 + (lane & 7) * 16, UNI(lds0 + G2_OFF_H + jj * 1024));
    }
    BARRIER();
    if (wave == 4) wait_flags<1>(flags + FL_P + (pm >> 1) * 16 * 32, 16, tag, lane);
    BARRIER();
    STAMP(10);
    issueP(0); issueP(G2_STG);
    WAIT_VM(4); BARRIER();
    issueP(2 * G2_STG); issueP(3 * G2_STG);
    WAIT_VM(8); BARRIER();
    u32 so = 0;
    auto issue = [&]() { issueP(so); issueW(so); so = so == 3 * G2_STG ? 0 : so + G2_STG; };
    issue(); WAIT_VM(13); BARRIER();
    for (int i = 2; i < G2_NK - 3; ++i) {
      issue();
      WAIT_VM(18); BARRIER();
    }
    WAIT_VM(9); BARRIER();
    WAIT_VM(0); BARRIER();
    BARRIER();
    BARRIER();
}

__device__ __forceinline__ void gemm2_compute(const Args& A, u32 lds0, int tid, int lane, int wave, u32 tag) {
  const int xcd = blockIdx.x & 7, idx = blockIdx.x >> 3;
  const int pm = (xcd & 3) * 8 + (idx & 7), pn = (xcd >> 2) * 4 + (idx >> 3);
  const int t0 = pm * 64, d0 = pn * 80;
  u32* flags = (u32*)(A.ws + WS_FL);
  BARRIER();
  if (tid == 0) {
    const int pm1 = (xcd & 1) * 8 + (idx & 7), pn1 = (xcd >> 1) * 4 + (idx >> 3);
    set_flag(flags + FL_P + (pm1 * 16 + pn1) * 32, tag);
  }
  const int wt = wave & 1, wk = wave >> 1;
  u32 pc = lds0 + (wt * 2) * 4096 + wk * 2048 + lane * 16, wc = lds0 + 16384 + wk * 2 * 5120 + lane * 16, so = 0;
  f32x4 acc[5][2];
#pragma unroll
  for (int i = 0; i < 5; ++i) { acc[i][0] = (f32x4){0.f, 0.f, 0.f, 0.f}; acc[i][1] = acc[i][0]; }
  bf16x8 pf[2][2][2], wf[2][2][5];
  auto rd = [&](int set) {
#pragma unroll
    for (int ks = 0; ks < 2; ++ks) {
      wf[set][ks][0] = *LDS(bf16x8, wc + ks * 5120);
#pragma unroll
      for (int j = 0; j < 2; ++j) pf[set][ks][j] = *LDS(bf16x8, pc + j * 4096 + ks * 1024);
#pragma unroll
      for (int i = 1; i < 5; ++i) wf[set][ks][i] = *LDS(bf16x8, wc + ks * 5120 + i * 1024);
    }
  };
  auto adv = [&]() {
    u32 dl = so == 3 * G2_STG ? (u32)(-3 * G2_STG) : (u32)G2_STG;
    so += dl;
    asm volatile("" : "+s"(dl));
    pc += dl; wc += dl;
  };
  auto mma = [&](int set) {
#pragma unroll
    for (int ks = 0; ks < 2; ++ks)
#pragma unroll
      for (int i = 0; i < 5; ++i)
#pragma unroll
        for (int j = 0; j < 2; ++j) acc[i][j] = mfma16(wf[set][ks][i], pf[set][ks][j], acc[i][j]);
  };
#define G2_SCHED() do { _Pragma("unroll") for (int k_ = 0; k_ < 4; ++k_) { SG(0x008, 1); SG(0x100, 1); SG(0x008, 1); SG(0x100, 1); SG(0x008, 1); SG(0x100, 1); SG(0x008, 1); } \
    SG(0x008, 1); SG(0x100, 1); SG(0x008, 1); SG(0x100, 1); SG(0x008, 2); } while (0)
  BARRIER();
  const int tl = wt * 32 + wk * 16;
  u64 pw[4];
#pragma unroll
  for (int k = 0; k < 4; ++k) pw[k] = __hip_atomic_load((gu64*)((u64*)(A.ws + WS_PT) + (size_t)k * L + t0 + tl + (lane & 15)), RLX_AGENT);
  BARRIER();
  STAMP(11);
  rd(0); adv();
  WAIT_LGKM0();
#pragma unroll
  for (int i = 1; i < G2_NK - 1; i += 2) {
    BARRIER();
    SB(); rd(1); mma(0); adv(); G2_SCHED(); SB();
    WAIT_LGKM0();
    BARRIER();
    SB(); rd(0); mma(1); adv(); G2_SCHED(); SB();
    WAIT_LGKM0();
  }
  BARRIER();
  SB(); rd(1); mma(0); G2_SCHED(); SB();
  WAIT_LGKM0();
  mma(1);
  STAMP(12);
  float rr;
  {
    float s1 = 0.f, s2 = 0.f;
#pragma unroll
    for (int k = 0; k < 4; ++k) { s1 += __uint_as_float((u32)pw[k]); s2 += __uint_as_float((u32)(pw[k] >> 32)); }
    const float mean = s1 * (1.f / D);
    const float var = fmaxf(s2 * (1.f / D) - mean * mean, 0.f);
    const float r = __builtin_amdgcn_rcpf(rbf(__builtin_amdgcn_sqrtf(rbf(rbf(var) + 1e-5f))));
    rr = r * r;
  }
  const int g = lane >> 4, q = (lane & 15) >> 2, p = lane & 3;
  s16x4 hv[5];
#pragma unroll
  for (int i = 0; i < 5; ++i) hv[i] = tr_read(lds0 + G2_OFF_H + (16 * i + 4 * g + q) * 128 + (tl + 4 * p) * 2);
  BARRIER();
  const u32 ex = lds0 + (wave ^ 2) * 5120 + lane * 16, exr = lds0 + wave * 5120 + lane * 16;
#pragma unroll
  for (int i = 0; i < 5; ++i) { const f32x4 a0 = acc[i][0], a1 = acc[i][1]; *LDS(f32x4, ex + i * 1024) = wk ? +a0 : +a1; }
  WAIT_LGKM0();
  BARRIER();
  f32x4 m[5];
#pragma unroll
  for (int i = 0; i < 5; ++i) m[i] = *LDS(f32x4, exr + i * 1024);
  SB();
#pragma unroll
  for (int i = 0; i < 5; ++i) { const f32x4 a0 = acc[i][0], a1 = acc[i][1]; m[i] = ((wk ? +a1 : +a0) + m[i]) * rr; }
  u32x2 o2[5];
#pragma unroll
  for (int i = 0; i < 5; ++i) o2[i] = (u32x2){pack_bf16(m[i][0], m[i][1]), pack_bf16(m[i][2], m[i][3])};
#pragma unroll
  for (int i = 0; i < 5; ++i)
    o2[i] = (u32x2){pack_bf16(bf2f((u16)hv[i][0]) + lo_bf(o2[i][0]), bf2f((u16)hv[i][1]) + hi_bf(o2[i][0])),
                    pack_bf16(bf2f((u16)hv[i][2]) + lo_bf(o2[i][1]), bf2f((u16)hv[i][3]) + hi_bf(o2[i][1]))};
  char* orow = (char*)A.out + (size_t)(t0 + tl + (lane & 15)) * 1280 + d0 * 2;
  *(u32x4*)(orow + (16 * (0 + (g & 1)) + 8 * (g >> 1)) * 2) = widen16(o2[0], o2[1]);
  *(u32x4*)(orow + (16 * (2 + (g & 1)) + 8 * (g >> 1)) * 2) = widen16(o2[2], o2[3]);
  *(u32x2*)(orow + (64 + 4 * g) * 2) = o2[4];
  STAMP(13);
}

__global__ __launch_bounds__(512, 2) void s4_reglu_block(const Args A) {
  STAMP(0);
  extern __shared__ __attribute__((aligned(16))) char smem[];
  const u32 lds0 = (u32)(size_t)(LAS char*)smem;
  const int tid = threadIdx.x, lane = tid & 63;
  const int wave = __builtin_amdgcn_readfirstlane(tid >> 6);
  const int xcd = blockIdx.x & 7, idx = blockIdx.x >> 3;
  u32* flags = (u32*)(A.ws + WS_FL);
  u32 tag7 = 0;
  if (wave == 7) {
    tag7 = __hip_atomic_load((gu32*)flags, RLX_AGENT) + 1u;
    if (lane == 0) { *LDS(u32, lds0 + OFF_TAG) = tag7; set_flag(flags + FL_STARTED + blockIdx.x, tag7); }
  }
  if (idx < 12) {
    int wtask;
    if (idx < 8) {
      const int task = xcd * 8 + idx;
      ln1_task(A, task, lds0, tid, lane, wave);
      wtask = task < 16 ? 32 + task : 48 + task - 16;
    } else {
      wtask = xcd * 4 + (idx - 8);
    }
    weight_task(A, wtask, lds0, tid, lane, wave);
    STAMP(6);
    if (blockIdx.x == 95 && wave == 7) {
      const u64 t0 = __builtin_amdgcn_s_memrealtime();
      for (;;) {
        bool ok = true;
#pragma unroll
        for (int k = 0; k < 4; ++k) ok &= __hip_atomic_load((gu32*)(flags + FL_STARTED + k * 64 + lane), RLX_AGENT) == tag7;
        if (__all(ok)) { if (lane == 0) set_flag(flags, tag7); break; }
        if (__builtin_amdgcn_s_memrealtime() - t0 > SPIN_TICKS) break;
        __builtin_amdgcn_s_sleep(8);
      }
    }
  } else {
    conv_role(A, xcd * 20 + idx - 12, lds0, tid, lane, wave, tag7);
  }
  WAIT_LGKM0();
  BARRIER();
  const u32 tag = UNI(*LDS(u32, lds0 + OFF_TAG));
  if (wave >= 4) {
    gemm1_loader(A, lds0, lane, wave, tag);
    gemm2_loader(A, lds0, lane, wave, tag);
  } else {
    gemm1_compute(A, lds0, lane, wave);
    gemm2_compute(A, lds0, tid, lane, wave, tag);
  }
}

extern "C" void kernel_launch(void* const* d_in, const int* in_sizes, int n_in, void* d_out, int out_size, void* d_ws,
                              size_t ws_size, hipStream_t stream) {
  Args A;
  A.x = (const u16*)d_in[0];
  A.Are = (const u16*)d_in[1]; A.Aim = (const u16*)d_in[2]; A.Bre = (const u16*)d_in[3]; A.Bim = (const u16*)d_in[4];
  A.Cre = (const u16*)d_in[5]; A.Cim = (const u16*)d_in[6]; A.logdt = (const u16*)d_in[7]; A.Dskip = (const u16*)d_in[8];
  A.Wg = (const u16*)d_in[10]; A.Wu = (const u16*)d_in[11]; A.Wd = (const u16*)d_in[12];
  A.out = (u16*)d_out;
  A.ws = (char*)d_ws;
  (void)hipFuncSetAttribute((const void*)s4_reglu_block, hipFuncAttributeMaxDynamicSharedMemorySize, LDS_BYTES);
  s4_reglu_block<<<256, 512, LDS_BYTES, stream>>>(A);
}
```

```cpp
#include <hip/hip_runtime.h>
#include <hip/hip_bf16.h>
#include <stdint.h>

typedef unsigned short u16;
typedef unsigned u32;
typedef unsigned long long u64;
typedef short s16x4 __attribute__((ext_vector_type(4)));
typedef __bf16 bf16x8 __attribute__((ext_vector_type(8)));
typedef __bf16 bf16x2 __attribute__((ext_vector_type(2)));
typedef _Float16 f16x2 __attribute__((ext_vector_type(2)));
typedef _Float16 f16x8 __attribute__((ext_vector_type(8)));
typedef float f32x16 __attribute__((ext_vector_type(16)));
typedef float f32x4 __attribute__((ext_vector_type(4)));
typedef float f32x2 __attribute__((ext_vector_type(2)));
typedef u32 u32x4 __attribute__((ext_vector_type(4)));
typedef u32 u32x2 __attribute__((ext_vector_type(2)));
typedef __attribute__((address_space(1))) u32 gu32;
typedef __attribute__((address_space(1))) u64 gu64;
#define LAS __attribute__((address_space(3)))
#define LDS(T, a) ((LAS T*)(size_t)(a))

constexpr int L = 2048, D = 640, NS = 192, F = 1536;

constexpr size_t WS_UT = 0;
constexpr size_t WS_LN = 4u << 20;
constexpr size_t WS_HT = 8u << 20;
constexpr size_t WS_HM = 12u << 20;
constexpr size_t WS_W1 = 32u << 20;
constexpr size_t WS_WD = 36u << 20;
constexpr size_t WS_PT = 40u << 20;
constexpr size_t WS_FL = 52u << 20;
constexpr size_t WS_DBG = 24u << 20;
constexpr int FL_STARTED = 1024;
constexpr int FL_U = 2048;
constexpr int FL_W = FL_U + 64 * 32;
constexpr int FL_HE = FL_W + 96 * 32;
constexpr int FL_HL = FL_HE + 160 * 32;
constexpr int FL_P = FL_HL + 160 * 32;
constexpr int UT_ROW = 4160;

__host__ __device__ constexpr int ht_row(int d) { return ((d & 2) ? 320 : 0) + (d >> 2) * 2 + (d & 1); }
__host__ __device__ constexpr int ht_chan(int r) { return r < 320 ? (r >> 1) * 4 + (r & 1) : ((r - 320) >> 1) * 4 + 2 + (r & 1); }
static_assert(ht_chan(ht_row(639)) == 639 && ht_chan(ht_row(5)) == 5 && ht_row(2) == 320, "permutation");

struct Args {
  const u16 *x, *Are, *Aim, *Bre, *Bim, *Cre, *Cim, *logdt, *Dskip, *Wg, *Wu, *Wd;
  u16* out;
  char* ws;
};

__device__ __forceinline__ float bf2f(u16 v) { return __uint_as_float(((u32)v) << 16); }
__device__ __forceinline__ u32 pack_bf16(float lo, float hi) { return __builtin_bit_cast(u32, __builtin_convertvector((f32x2){lo, hi}, bf16x2)); }
__device__ __forceinline__ u32 pack_f16(float lo, float hi) { return __builtin_bit_cast(u32, __builtin_convertvector((f32x2){lo, hi}, f16x2)); }
__device__ __forceinline__ float rbf(float f) { return __uint_as_float(pack_bf16(f, f) << 16); }
__device__ __forceinline__ float lo_bf(u32 v) { return __uint_as_float(v << 16); }
__device__ __forceinline__ float hi_bf(u32 v) { return __uint_as_float(v & 0xffff0000u); }

#define STAMP(i)
#define WAIT_VM(n) asm volatile("s_waitcnt vmcnt(%0)" ::"n"(n) : "memory")
#define WAIT_LGKM0() do { asm volatile("" ::: "memory"); __builtin_amdgcn_s_waitcnt(0xC07F); asm volatile("" ::: "memory"); } while (0)
#define BARRIER() do { asm volatile("" ::: "memory"); __builtin_amdgcn_s_barrier(); asm volatile("" ::: "memory"); } while (0)
#define SB() __builtin_amdgcn_sched_barrier(0)
#define SG(m, n) __builtin_amdgcn_sched_group_barrier(m, n, 0)
#define RLX_AGENT __ATOMIC_RELAXED, __HIP_MEMORY_SCOPE_AGENT

__device__ __forceinline__ void glds16(const void* gsrc, u32 lds_dst) {
  u32 keep;
  asm volatile("s_mov_b32 %0, m0\n\ts_mov_b32 m0, %2\n\ts_nop 0\n\tglobal_load_lds_dwordx4 %1, off\n\ts_mov_b32 m0, %0"
               : "=&s"(keep) : "v"(gsrc), "s"(__builtin_amdgcn_readfirstlane(lds_dst)) : "memory");
}
__device__ __forceinline__ void glds16d(const void* gsrc, u32 lds_dst) {
  u32 keep;
  asm volatile("s_mov_b32 %0, m0\n\ts_mov_b32 m0, %2\n\ts_nop 0\n\tglobal_load_lds_dwordx4 %1, off sc1\n\ts_mov_b32 m0, %0"
               : "=&s"(keep) : "v"(gsrc), "s"(__builtin_amdgcn_readfirstlane(lds_dst)) : "memory");
}
#define UNI(v) ((u32)__builtin_amdgcn_readfirstlane((int)(v)))
__device__ __forceinline__ void st16d(void* p, u32x4 v) {
  asm volatile("global_store_dwordx4 %0, %1, off sc1\n\ts_nop 1" :: "v"(p), "v"(v) : "memory");
}
constexpr u64 SPIN_TICKS = 300000;
#define POLL_SLEEP 2
template <int NL>
__device__ __forceinline__ void wait_flags(const u32* flags, int n, u32 tag, int lane) {
  const u64 t0 = __builtin_amdgcn_s_memrealtime();
  for (;;) {
    bool ok = true;
#pragma unroll
    for (int k = 0; k < NL; ++k) {
      const int i = k * 64 + lane;
      ok &= __hip_atomic_load((gu32*)(flags + (i < n ? i : n - 1) * 32), RLX_AGENT) == tag;
    }
    if (__all(ok)) break;
    if (__builtin_amdgcn_s_memrealtime() - t0 > SPIN_TICKS) break;
    __builtin_amdgcn_s_sleep(POLL_SLEEP);
  }
  asm volatile("" ::: "memory");
}
template <int NL>
__device__ __forceinline__ bool poll_once(const u32* flags, int n, u32 tag, int lane) {
  bool ok = true;
#pragma unroll
  for (int k = 0; k < NL; ++k) {
    const int i = k * 64 + lane;
    ok &= __hip_atomic_load((gu32*)(flags + (i < n ? i : n - 1) * 32), RLX_AGENT) == tag;
  }
  return __all(ok);
}
__device__ __forceinline__ void set_flag(u32* flag, u32 tag) { __hip_atomic_store((gu32*)flag, tag, RLX_AGENT); }

__device__ __forceinline__ s16x4 tr_read(u32 addr) { return __builtin_amdgcn_ds_read_tr16_b64_v4i16(LDS(s16x4, addr)); }
__device__ __forceinline__ u32x4 tr_read2(u32 a0, u32 a1) {
  const s16x4 a = tr_read(a0), b = tr_read(a1);
  return __builtin_bit_cast(u32x4, __builtin_shufflevector(a, b, 0, 1, 2, 3, 4, 5, 6, 7));
}
__device__ __forceinline__ bf16x8 tr_frag(u32 a0, u32 a1) { return __builtin_bit_cast(bf16x8, tr_read2(a0, a1)); }
__device__ __forceinline__ f32x4 mfma16(bf16x8 a, bf16x8 b, f32x4 c) { return __builtin_amdgcn_mfma_f32_16x16x32_bf16(a, b, c, 0, 0, 0); }
#define ONES_BF16 __builtin_bit_cast(bf16x8, (u32x4){0x3f803f80u, 0x3f803f80u, 0x3f803f80u, 0x3f803f80u})
__device__ __forceinline__ float diag16(f32x4 a, int lane) { const int r = lane & 3; return r == 0 ? a[0] : r == 1 ? a[1] : r == 2 ? a[2] : a[3]; }

template <int CTRL> __device__ __forceinline__ float dpp_f(float v) {
  return __builtin_bit_cast(float, __builtin_amdgcn_update_dpp(0, __builtin_bit_cast(int, v), CTRL, 0xf, 0xf, true));
}
__device__ __forceinline__ float wave_max(float v) {
  v = fmaxf(v, dpp_f<0xB1>(v));
  v = fmaxf(v, dpp_f<0x4E>(v));
  v = fmaxf(v, dpp_f<0x141>(v));
  v = fmaxf(v, dpp_f<0x140>(v));
  { auto r = __builtin_amdgcn_permlane16_swap(__float_as_uint(v), __float_as_uint(v), false, false); v = fmaxf(__uint_as_float(r[0]), __uint_as_float(r[1])); }
  { auto r = __builtin_amdgcn_permlane32_swap(__float_as_uint(v), __float_as_uint(v), false, false); v = fmaxf(__uint_as_float(r[0]), __uint_as_float(r[1])); }
  return v;
}
__device__ __forceinline__ u32x4 widen16(u32x2 a, u32x2 b) {
  const auto s0 = __builtin_amdgcn_permlane16_swap(a[0], b[0], false, false);
  const auto s1 = __builtin_amdgcn_permlane16_swap(a[1], b[1], false, false);
  return (u32x4){s0[0], s1[0], s0[1], s1[1]};
}

constexpr int LDS_BYTES = 163840;
constexpr int OFF_TAG = LDS_BYTES - 16;
constexpr int C_ROW = 272;
constexpr int C_SCR = 60 * C_ROW;
constexpr int C_KCH = 4608;
constexpr int C_UCP = 4160, C_UCH = 2 * C_UCP;
constexpr int F_OFF_P = 4 * C_SCR;
constexpr int F_OFF_S = F_OFF_P + 9216;
constexpr int F_OFF_X = F_OFF_S + 16;
constexpr int F_OFF_K = 74752;
constexpr int F_OFF_U = F_OFF_K + 4 * C_KCH;
constexpr int F_OFF_L = F_OFF_U + 4 * C_UCH;
constexpr int LN_RS = 1344;
constexpr int G1_STG = 20480, G1_NS = 7, G1_NK = 20;
constexpr int G2_STG = 36864, G2_NK = 12;
constexpr int G2_OFF_H = G2_STG * 4;
static_assert(F_OFF_L + 16384 <= OFF_TAG && G1_STG * G1_NS <= OFF_TAG && G2_OFF_H + 10240 <= OFF_TAG, "LDS");

__device__ __forceinline__ void ln1_task(const Args& A, int task, u32 lds0, int tid, int lane, int wave) {
  char* uT = A.ws + WS_UT;
  const int t0 = task * 32;
  const u32 tX = lds0, tU = lds0 + 40960;
#pragma unroll
  for (int k = 0; k < 5; ++k) {
    const int P = (wave * 5 + k) * 64 + lane, row = P / 80, chk = (P % 80) ^ (row & 15);
    glds16((const char*)A.x + (size_t)(t0 + row) * 1280 + chk * 16, UNI(tX + (wave * 5 + k) * 1024));
  }
  WAIT_VM(0);
  BARRIER();
  STAMP(1);
  {
    const int row = 4 * wave + (lane >> 4);
    f32x2 xv[20];
#pragma unroll
    for (int k = 0; k < 5; ++k) {
      const u32x4 w = *LDS(u32x4, tX + row * 1280 + (((lane & 15) + 16 * k) ^ (row & 15)) * 16);
#pragma unroll
      for (int e = 0; e < 4; ++e) xv[4 * k + e] = (f32x2){lo_bf(w[e]), hi_bf(w[e])};
    }
    f32x2 a1 = xv[0] + xv[1], a2 = xv[0] * xv[0], b1 = xv[2] + xv[3], b2 = xv[1] * xv[1];
    a2 = __builtin_elementwise_fma(xv[2], xv[2], a2); b2 = __builtin_elementwise_fma(xv[3], xv[3], b2);
#pragma unroll
    for (int k = 4; k < 20; k += 2) {
      a1 += xv[k]; b1 += xv[k + 1];
      a2 = __builtin_elementwise_fma(xv[k], xv[k], a2); b2 = __builtin_elementwise_fma(xv[k + 1], xv[k + 1], b2);
    }
    float s1 = (a1.x + a1.y) + (b1.x + b1.y), s2 = (a2.x + a2.y) + (b2.x + b2.y);
    s1 += dpp_f<0xB1>(s1); s2 += dpp_f<0xB1>(s2);
    s1 += dpp_f<0x4E>(s1); s2 += dpp_f<0x4E>(s2);
    s1 += dpp_f<0x141>(s1); s2 += dpp_f<0x141>(s2);
    s1 += dpp_f<0x140>(s1); s2 += dpp_f<0x140>(s2);
    const float mean = s1 * (1.f / D);
    const float var = fmaxf(s2 * (1.f / D) - mean * mean, 0.f);
    const float mb = rbf(mean), sd = rbf(__builtin_amdgcn_sqrtf(rbf(rbf(var) + 1e-5f)));
    const float ri = __builtin_amdgcn_rcpf(sd), mi = -mb * ri;
    if ((lane & 15) == 0)
      __hip_atomic_store((gu64*)((u64*)(A.ws + WS_LN) + t0 + row), ((u64)__float_as_uint(mb) << 32) | __float_as_uint(sd), RLX_AGENT);
#pragma unroll
    for (int k = 0; k < 5; ++k) {
      u32x4 o;
#pragma unroll
      for (int e = 0; e < 4; ++e) {
        const f32x2 u = __builtin_elementwise_fma(xv[4 * k + e], (f32x2){ri, ri}, (f32x2){mi, mi});
        o[e] = pack_f16(u.x, u.y);
      }
      *LDS(u32x4, tU + row * LN_RS + ((lane & 15) + 16 * k) * 16) = o;
    }
  }
  WAIT_LGKM0();
  BARRIER();
  STAMP(2);
  {
    const int g = lane >> 4, q = (lane & 15) >> 2, p = lane & 3;
    u32x4 ov[5];
#pragma unroll
    for (int k = 0; k < 5; ++k) {
      const u32 a = tU + (8 * g + q) * LN_RS + ((wave * 5 + k) * 16 + 4 * p) * 2;
      ov[k] = tr_read2(a, a + 4 * LN_RS);
    }
#pragma unroll
    for (int k = 0; k < 5; ++k) st16d(uT + (size_t)((wave * 5 + k) * 16 + (lane & 15)) * UT_ROW + 32 + (t0 + 8 * g) * 2, ov[k]);
  }
  WAIT_VM(0);
  BARRIER();
  if (tid == 0) set_flag((u32*)(A.ws + WS_FL) + FL_U + task * 32, *LDS(u32, lds0 + OFF_TAG));
  STAMP(3);
}

__device__ __forceinline__ void weight_task(const Args& A, int wtask, u32 lds0, int tid, int lane, int wave) {
  const int g = lane >> 4, q = (lane & 15) >> 2, p = lane & 3;
  if (wtask < 48) {
    const u16* W = wtask < 24 ? A.Wg : A.Wu;
    const int c0 = (wtask % 24) * 64;
    {
      const int row = 8 * wave + (lane >> 3), chk = (lane & 7) ^ ((row & 3) << 1);
      const char* src = (const char*)W + c0 * 2 + chk * 16;
#pragma unroll
      for (int jj = 0; jj < 10; ++jj)
        glds16(src + (size_t)ht_chan(row + 64 * jj) * 3072, UNI(lds0 + (wave + 8 * jj) * 1024));
    }
    WAIT_VM(0);
    BARRIER();
    const int tl = wave & 3, ks0 = (wave >> 2) * 10;
    const u32 a = lds0 + (32 * ks0 + 8 * g + q) * 128 + (((2 * tl + (p >> 1)) ^ (q << 1)) * 16) + 8 * (p & 1);
    bf16x8 fr[10];
    f32x4 cs = {0.f, 0.f, 0.f, 0.f};
#pragma unroll
    for (int k = 0; k < 10; ++k) { fr[k] = tr_frag(a + k * 4096, a + k * 4096 + 512); cs = mfma16(ONES_BF16, fr[k], cs); }
    WAIT_LGKM0();
    BARRIER();
    if (lane < 16) *LDS(float, lds0 + (wave * 16 + lane) * 4) = cs[0];
    WAIT_LGKM0();
    BARRIER();
    const float cm = (*LDS(float, lds0 + (tl * 16 + (lane & 15)) * 4) + *LDS(float, lds0 + ((tl + 4) * 16 + (lane & 15)) * 4)) * (1.f / D);
    const int f = c0 + 16 * tl, pn = f / 96, fi = (f % 96) / 16;
    const int slot = (fi / 3) * 6 + (wtask < 24 ? 0 : 3) + fi % 3;
    char* dst = A.ws + WS_W1 + (((size_t)pn * 20 + ks0) * 12 + slot) * 1024 + lane * 16;
#pragma unroll
    for (int k = 0; k < 10; ++k) {
      const u32x4 w = __builtin_bit_cast(u32x4, fr[k]);
      u32x4 o;
#pragma unroll
      for (int e = 0; e < 4; ++e) o[e] = pack_bf16(lo_bf(w[e]) - cm, hi_bf(w[e]) - cm);
      st16d(dst + (size_t)k * 12288, o);
    }
  } else {
    const int ks = wtask - 48;
#pragma unroll
    for (int jj = 0; jj < 5; ++jj) {
      const int P = 64 * (wave + 8 * jj) + lane, row = P / 80, chk = P % 80;
      glds16((const char*)A.Wd + (size_t)(32 * ks + row) * 1280 + chk * 16, UNI(lds0 + (wave + 8 * jj) * 1024));
    }
    WAIT_VM(0);
    BARRIER();
#pragma unroll
    for (int jj = 0; jj < 5; ++jj) {
      const int fr = wave + 8 * jj;
      const u32 a = lds0 + (8 * g + q) * 1280 + (16 * fr + 4 * p) * 2;
      st16d(A.ws + WS_WD + (((size_t)(fr / 5) * 48 + ks) * 5 + fr % 5) * 1024 + lane * 16, tr_read2(a, a + 4 * 1280));
    }
  }
  WAIT_VM(0);
  BARRIER();
  if (tid == 0) set_flag((u32*)(A.ws + WS_FL) + FL_W + wtask * 32, *LDS(u32, lds0 + OFF_TAG));
}

struct cplx { f32x2 v, r; };
__device__ __forceinline__ cplx mk(f32x2 v) { return cplx{v, (f32x2){-v.y, v.x}}; }
__device__ __forceinline__ f32x2 cmul(f32x2 a, const cplx& w) {
  const f32x2 t = (f32x2){a.x, a.x} * w.v;
  return __builtin_elementwise_fma((f32x2){a.y, a.y}, w.r, t);
}
__device__ __forceinline__ f32x2 cexp(float zr, float zi, float l, float sgn) {
  const float m = __builtin_amdgcn_exp2f(zr * l * 1.44269504088896341f);
  const float a = __builtin_amdgcn_fractf(zi * l * 0.15915494309189535f);
  return (f32x2){m * __builtin_amdgcn_cosf(a), sgn * m * __builtin_amdgcn_sinf(a)};
}
__device__ __forceinline__ u32 pkh(f32x2 v) { return __builtin_bit_cast(u32, __builtin_convertvector(v, f16x2)); }
__device__ __forceinline__ u32 cmulh(u32 a, u32 b) {
  u32 t, r;
  asm("v_pk_mul_f16 %0, %1, %2 op_sel:[0,0] op_sel_hi:[0,1]" : "=v"(t) : "v"(a), "v"(b));
  asm("v_pk_fma_f16 %0, %1, %2, %3 op_sel:[1,1,0] op_sel_hi:[1,0,1] neg_lo:[0,1,0]" : "=v"(r) : "v"(a), "v"(b), "v"(t));
  return r;
}
#define SET_M0(base) asm volatile("s_mov_b32 m0, %0\n\ts_nop 0" ::"s"(base) : "memory")
#define WR4(row, a, b, c, d)                                                                                    \
  asm volatile("ds_write_addtid_b32 %0 offset:%4\n\tds_write_addtid_b32 %1 offset:%4+272\n\t"                    \
               "ds_write_addtid_b32 %2 offset:%4+544\n\tds_write_addtid_b32 %3 offset:%4+816"                    \
               ::"v"(a), "v"(b), "v"(c), "v"(d), "n"((row) * C_ROW) : "memory")

__device__ __forceinline__ void conv_role(const Args& A, int grp, u32 lds0, int tid, int lane, int wave, u32 tag) {
  const int ch = (wave >> 2) * 2 + ((wave >> 1) & 1), hf = wave & 1, d0 = grp * 4, d = d0 + ch;
  const u32 sb = UNI(lds0 + ch * C_SCR);
  const int r32 = lane & 31, h32 = lane >> 5;
  const int j = lane & 15, g = lane >> 4;
  {
    const u64 q0 = (u64)A.Are, q1 = (u64)A.Aim, q2 = (u64)A.Bre, q3 = (u64)A.Bim, q4 = (u64)A.Cre, q5 = (u64)A.Cim;
#pragma unroll
    for (int rep = 0; rep < 2; ++rep) {
      if (rep == 1 && wave != 0) break;
      const int i = rep ? 8 : wave;
      const int b = 1024 * i + 16 * lane, a = b / 1536, o = b - a * 1536;
      u64 src = +q0;
      src = a == 1 ? +q1 : src; src = a == 2 ? +q2 : src; src = a == 3 ? +q3 : src; src = a == 4 ? +q4 : src; src = a == 5 ? +q5 : src;
      glds16((const char*)(src + (u64)d0 * 384 + o), UNI(lds0 + F_OFF_P + i * 1024));
    }
  }
  const u32 wdt = ((const u32*)A.logdt)[d >> 1], wds = ((const u32*)A.Dskip)[d >> 1];
  const float dt = rbf(__expf((d & 1) ? hi_bf(wdt) : lo_bf(wdt)));
  WAIT_VM(0);
  BARRIER();
  float zr[3], zi[3];
  const u32 pl = lds0 + F_OFF_P + ch * 384 + lane * 2;
#pragma unroll
  for (int r = 0; r < 3; ++r) {
    zr[r] = dt * bf2f(*LDS(u16, pl + r * 128));
    zi[r] = dt * bf2f(*LDS(u16, pl + 1536 + r * 128));
  }
  u32 an[3][11];
  if (hf == 0) {
    f32x2 cb[3];
    float bound = 0.f;
#pragma unroll
    for (int r = 0; r < 3; ++r) {
      const float br = dt * bf2f(*LDS(u16, pl + 2 * 1536 + r * 128)), bi = dt * bf2f(*LDS(u16, pl + 3 * 1536 + r * 128));
      const float cr = bf2f(*LDS(u16, pl + 4 * 1536 + r * 128)), ci = bf2f(*LDS(u16, pl + 5 * 1536 + r * 128));
      cb[r] = (f32x2){cr * br - ci * bi, -(cr * bi + ci * br)};
      const float grow = __builtin_amdgcn_exp2f(fmaxf(zr[r], 0.f) * (2064.f * 1.44269504f));
      bound = fmaxf(bound, (fabsf(cb[r].x) + fabsf(cb[r].y)) * grow);
    }
    bound = wave_max(bound);
    int ex = (int)((__float_as_uint(bound) >> 23) & 255);
    ex = ex < 20 ? 20 : (ex > 240 ? 240 : ex);
    const float scale = __uint_as_float((u32)(267 - ex) << 23);
    if (lane == 0) *LDS(float, lds0 + F_OFF_S + ch * 4) = __uint_as_float((u32)(ex - 5) << 23);
#pragma unroll
    for (int r = 0; r < 3; ++r) {
      const cplx w1 = mk(cexp(zr[r], zi[r], 1.f, -1.f));
      const cplx w512 = mk(cexp(zr[r], zi[r], 512.f, -1.f));
      const cplx w2 = mk(cmul(w1.v, w1));
      const f32x2 w3 = cmul(w2.v, w1);
      const cplx w4 = mk(cmul(w2.v, w2));
      const f32x2 w1024 = cmul(w512.v, w512), w1536 = cmul(w1024, w512);
      const f32x2 P0 = cb[r] * scale, P4 = cmul(P0, w4), P8 = cmul(P4, w4), P12 = cmul(P8, w4), P16 = cmul(P12, w4);
      an[r][0] = pkh(P0); an[r][1] = pkh(P4); an[r][2] = pkh(P8); an[r][3] = pkh(P12); an[r][4] = pkh(P16);
      an[r][5] = pkh(w1.v); an[r][6] = pkh(w2.v); an[r][7] = pkh(w3);
      an[r][8] = pkh(w512.v); an[r][9] = pkh(w1024); an[r][10] = pkh(w1536);
    }
  } else {
#pragma unroll
    for (int r = 0; r < 3; ++r) {
      const cplx w16 = mk(cexp(zr[r], zi[r], 16.f, 1.f));
      const cplx w128 = mk(cexp(zr[r], zi[r], 128.f, 1.f));
      const cplx w32 = mk(cmul(w16.v, w16));
      const f32x2 w48 = cmul(w32.v, w16);
      const f32x2 w64 = cmul(w32.v, w32);
      const f32x2 w192 = cmul(w64, w128), w256 = cmul(w128.v, w128);
      const f32x2 w320 = cmul(w192, w128), w384 = cmul(w256, w128), w448 = cmul(w320, w128);
      an[r][0] = pkh(w64); an[r][1] = pkh(w128.v); an[r][2] = pkh(w192); an[r][3] = pkh(w256);
      an[r][4] = pkh(w320); an[r][5] = pkh(w384); an[r][6] = pkh(w448);
      an[r][7] = pkh(w16.v); an[r][8] = pkh(w32.v); an[r][9] = pkh(w48); an[r][10] = 0x00003c00u;
    }
  }
  u32 arow;
  {
    const int hr = (r32 >> 2) & 1, rho = (r32 & 3) + 4 * (r32 >> 3);
    const int row = rho <= 8 ? rho + 8 * hr : rho <= 11 ? rho + 8 + 2 * hr : rho <= 13 ? rho + 10 + hr : rho + 11 + hr;
    arow = sb + row * C_ROW + h32 * 16 + hf * 128;
  }
  const u32 brow = sb + (28 + r32) * C_ROW + h32 * 16 + hf * 128;
  f32x16 kacc;
#pragma unroll
  for (int i = 0; i < 16; ++i) kacc[i] = 0.f;
  SET_M0(sb);
#pragma unroll
  for (int r = 0; r < 3; ++r) {
    u32 gv[2][4];
    auto grp4 = [&](int gi, u32 (&o)[4]) {
      const u32* a = an[r];
      if (hf == 0) {
        if (gi < 4) { o[0] = a[gi]; o[1] = cmulh(a[gi], a[5]); o[2] = cmulh(a[gi], a[6]); o[3] = cmulh(a[gi], a[7]); }
        else if (gi == 4) { o[0] = a[4]; o[1] = cmulh(a[0], a[8]); o[2] = cmulh(a[1], a[8]); o[3] = cmulh(a[2], a[8]); }
        else if (gi == 5) { o[0] = cmulh(a[3], a[8]); o[1] = cmulh(a[4], a[8]); o[2] = cmulh(a[0], a[9]); o[3] = cmulh(a[2], a[9]); }
        else if (gi == 6) { o[0] = cmulh(a[4], a[9]); o[1] = cmulh(a[0], a[10]); o[2] = cmulh(a[2], a[10]); o[3] = cmulh(a[4], a[10]); }
      } else {
        if (gi == 0) { o[0] = a[10]; o[1] = a[7]; o[2] = a[8]; o[3] = a[9]; }
        else { o[0] = a[gi - 1]; o[1] = cmulh(a[gi - 1], a[7]); o[2] = cmulh(a[gi - 1], a[8]); o[3] = cmulh(a[gi - 1], a[9]); }
      }
    };
    if (hf == 0) {
      grp4(0, gv[0]);
#pragma unroll
      for (int gi = 0; gi < 7; ++gi) {
        if (gi < 6) grp4(gi + 1, gv[(gi + 1) & 1]);
        SB();
        WR4(4 * gi, gv[gi & 1][0], gv[gi & 1][1], gv[gi & 1][2], gv[gi & 1][3]);
        SB();
      }
    } else {
      grp4(0, gv[0]);
#pragma unroll
      for (int gi = 0; gi < 8; ++gi) {
        if (gi < 7) grp4(gi + 1, gv[(gi + 1) & 1]);
        SB();
        WR4(28 + 4 * gi, gv[gi & 1][0], gv[gi & 1][1], gv[gi & 1][2], gv[gi & 1][3]);
        SB();
      }
    }
    WAIT_LGKM0();
    BARRIER();
    f16x8 fa[4], fb[4];
#pragma unroll
    for (int k = 0; k < 4; ++k) { fa[k] = *LDS(f16x8, arow + k * 32); fb[k] = *LDS(f16x8, brow + k * 32); }
    WAIT_LGKM0();
    BARRIER();
#pragma unroll
    for (int k = 0; k < 4; ++k) kacc = __builtin_amdgcn_mfma_f32_32x32x16_f16(fa[k], fb[k], kacc, 0, 0, 0);
  }
  STAMP(1);
  {
    const u32 ex = sb + hf * 4096 + lane * 16, exr = sb + (hf ^ 1) * 4096 + lane * 16;
    if (hf == 0) {
      *LDS(f32x4, ex) = (f32x4){kacc[12], kacc[13], kacc[14], kacc[15]};
    } else {
      *LDS(f32x4, ex) = (f32x4){kacc[0], kacc[1], kacc[2], kacc[3]};
      *LDS(f32x4, ex + 1024) = (f32x4){kacc[4], kacc[5], kacc[6], kacc[7]};
      *LDS(f32x4, ex + 2048) = (f32x4){kacc[8], kacc[9], kacc[10], kacc[11]};
    }
    WAIT_LGKM0();
    BARRIER();
    const u32 kb = lds0 + F_OFF_K + ch * C_KCH + 4080 - 32 * r32 - 16 * h32;
    auto put = [&](int base, float e0, float e1, float e2, float e3, float e4, float e5, float e6, float e7) {
      *LDS(u32x4, kb - 2 * base) = (u32x4){pack_f16(e7, e6), pack_f16(e5, e4), pack_f16(e3, e2), pack_f16(e1, e0)};
    };
    if (hf == 0) {
      const f32x4 o0 = *LDS(f32x4, exr), o1 = *LDS(f32x4, exr + 1024), o2 = *LDS(f32x4, exr + 2048);
      float v[12];
#pragma unroll
      for (int i = 0; i < 4; ++i) {
        v[i] = (kacc[i] + o0[i]) * (1.f / 256.f); v[4 + i] = (kacc[4 + i] + o1[i]) * (1.f / 256.f); v[8 + i] = (kacc[8 + i] + o2[i]) * (1.f / 256.f);
      }
      const bool far = r32 >= 16;
      put(0, v[0], far ? v[0] : v[1], v[2], far ? v[4] : v[3], v[4], far ? v[4] : v[5], v[6], far ? v[8] : v[7]);
      put(512, v[9], v[9], v[9], v[10], v[10], v[10], v[11], v[11]);
    } else {
      const f32x4 o = *LDS(f32x4, exr);
      const float v12 = (kacc[12] + o[0]) * (1.f / 256.f), v13 = (kacc[13] + o[1]) * (1.f / 256.f),
                  v14 = (kacc[14] + o[2]) * (1.f / 256.f), v15 = (kacc[15] + o[3]) * (1.f / 256.f);
      put(1024, v12, v12, v12, v12, h32 ? v13 : v12, v13, v13, v13);
      put(1536, v14, v14, v14, v14, h32 ? v15 : v14, v15, v15, v15);
    }
  }
  if (hf == 1 && lane < 32) *LDS(u32x4, lds0 + F_OFF_K + ch * C_KCH + 4096 + lane * 16) = (u32x4){0u, 0u, 0u, 0u};
  if (wave == 0 && lane < 10) *LDS(u32, lds0 + F_OFF_X + lane * 4) = 0u;
  STAMP(2);
  if (wave == 7) wait_flags<1>((const u32*)(A.ws + WS_FL) + FL_U, 64, tag, lane);
  WAIT_LGKM0();
  BARRIER();
  STAMP(3);
  {
    const char* up = A.ws + WS_UT + (size_t)d * UT_ROW + lane * 16 + hf * 2;
    const u32 dst = UNI(lds0 + F_OFF_U + ch * C_UCH + hf * C_UCP);
#pragma unroll
    for (int k = 0; k < 4; ++k) glds16d(up + k * 1024, dst + k * 1024);
    if (lane < 2) glds16d(up + 4096, dst + 4096);
    glds16d(A.ws + WS_LN + wave * 2048 + lane * 16, UNI(lds0 + F_OFF_L + wave * 2048));
    glds16d(A.ws + WS_LN + wave * 2048 + 1024 + lane * 16, UNI(lds0 + F_OFF_L + wave * 2048 + 1024));
  }
  WAIT_VM(0);
  if (lane < 8) {
    const u32 zb = lds0 + F_OFF_U + ch * C_UCH + hf * C_UCP;
    if (lane < 7 || hf == 0) *LDS(u32, zb + lane * 4) = 0u; else *LDS(u16, zb + 28) = (u16)0;
  }
  WAIT_LGKM0();
  BARRIER();
  STAMP(4);
  const int cc = (1 + j) & 1;
  const u32 aa = lds0 + F_OFF_U + ch * C_UCH + cc * C_UCP + 2 * (1 + j + 8 * g - cc) + hf * 256;
  const u32 ba = lds0 + F_OFF_K + ch * C_KCH + 32 * j + 16 * g + hf * 256;
  f32x4 acc[8];
#pragma unroll
  for (int i = 0; i < 8; ++i) acc[i] = (f32x4){0.f, 0.f, 0.f, 0.f};
  if (wave < 4) __builtin_amdgcn_s_setprio(2);
  {
    f16x8 Af[2][8], Bf[2][8];
    u32 am[4];
#pragma unroll
    for (int k = 0; k < 4; ++k) { am[k] = aa + 1024 * k; asm volatile("" : "+v"(am[k])); }
    auto rd = [&](int set, int r) {
#pragma unroll
      for (int m = 0; m < 8; ++m) {
        const LAS u32* ap = LDS(const u32, am[m >> 1] + 64 * r + 512 * (m & 1));
        Af[set][m] = __builtin_bit_cast(f16x8, (u32x4){ap[0], ap[1], ap[2], ap[3]});
        Bf[set][m] = *LDS(f16x8, ba + 64 * r + 512 * (7 - m));
      }
    };
    auto mm = [&](int set) {
#pragma unroll
      for (int sdx = 0; sdx < 8; ++sdx)
#pragma unroll
        for (int m = 0; m <= sdx; ++m) acc[sdx] = __builtin_amdgcn_mfma_f32_16x16x32_f16(Af[set][m], Bf[set][sdx - m], acc[sdx], 0, 0, 0);
    };
#define C_SCHED() do { _Pragma("unroll") for (int k_ = 0; k_ < 24; ++k_) { SG(0x008, 1); SG(0x100, 1); } SG(0x008, 12); } while (0)
    rd(0, 0);
    SB(); rd(1, 1); mm(0); C_SCHED(); SB();
    rd(0, 2); mm(1); C_SCHED(); SB();
    rd(1, 3); mm(0); C_SCHED(); SB();
    mm(1);
  }
  STAMP(5);
  {
    const u32 ex = lds0 + wave * 4096 + lane * 16, exr = lds0 + (wave ^ 1) * 4096 + lane * 16;
    if (hf == 0) {
#pragma unroll
      for (int i = 0; i < 4; ++i) *LDS(f32x4, ex + i * 1024) = acc[4 + i];
    } else {
#pragma unroll
      for (int i = 0; i < 4; ++i) *LDS(f32x4, ex + i * 1024) = acc[i];
    }
    WAIT_LGKM0();
    if (lane == 0) *LDS(volatile u32, lds0 + F_OFF_X + wave * 4) = 1u;
    const float it = *LDS(float, lds0 + F_OFF_S + ch * 4);
    const float dsk = (d & 1) ? hi_bf(wds) : lo_bf(wds);
    u32x2 dv[4], xv[4];
#pragma unroll
    for (int i = 0; i < 4; ++i) {
      const int t = 16 * (16 * (4 * hf + i) + 15 - j) + 4 * g;
      const u32x2 uw = *LDS(u32x2, lds0 + F_OFF_U + ch * C_UCH + 2 * (16 + t));
      const u32 w0 = uw[0], w1 = uw[1];
      const f16x2 u01 = __builtin_bit_cast(f16x2, w0), u23 = __builtin_bit_cast(f16x2, w1);
      const float uf[4] = {(float)u01[0], (float)u01[1], (float)u23[0], (float)u23[1]};
      dv[i] = (u32x2){pack_bf16(uf[0] * dsk, uf[1] * dsk), pack_bf16(uf[2] * dsk, uf[3] * dsk)};
      const f32x4 l01 = *LDS(f32x4, lds0 + F_OFF_L + t * 8), l23 = *LDS(f32x4, lds0 + F_OFF_L + t * 8 + 16);
      xv[i] = (u32x2){pack_bf16(fmaf(uf[0], l01[0], l01[1]), fmaf(uf[1], l01[2], l01[3])), pack_bf16(fmaf(uf[2], l23[0], l23[1]), fmaf(uf[3], l23[2], l23[3]))};
    }
    while (*LDS(volatile u32, lds0 + F_OFF_X + (wave ^ 1) * 4) == 0u) __builtin_amdgcn_s_sleep(1);
    asm volatile("" ::: "memory");
    f32x4 y[4];
#pragma unroll
    for (int i = 0; i < 4; ++i) y[i] = *LDS(f32x4, exr + i * 1024);
    SB();
#pragma unroll
    for (int i = 0; i < 4; ++i) { const f32x4 a0 = acc[i], a1 = acc[4 + i]; y[i] = ((hf ? +a1 : +a0) + y[i]) * it; }
    u32x2 w[4];
#pragma unroll
    for (int i = 0; i < 4; ++i) w[i] = (u32x2){pack_bf16(y[i][0], y[i][1]), pack_bf16(y[i][2], y[i][3])};
#pragma unroll
    for (int i = 0; i < 4; ++i)
      w[i] = (u32x2){pack_bf16(lo_bf(w[i][0]) + lo_bf(dv[i][0]), hi_bf(w[i][0]) + hi_bf(dv[i][0])), pack_bf16(lo_bf(w[i][1]) + lo_bf(dv[i][1]), hi_bf(w[i][1]) + hi_bf(dv[i][1]))};
    u32x2 hv[4];
#pragma unroll
    for (int i = 0; i < 4; ++i)
      hv[i] = (u32x2){pack_bf16(lo_bf(xv[i][0]) + lo_bf(w[i][0]), hi_bf(xv[i][0]) + hi_bf(w[i][0])), pack_bf16(lo_bf(xv[i][1]) + lo_bf(w[i][1]), hi_bf(xv[i][1]) + hi_bf(w[i][1]))};
    char* hrow = A.ws + WS_HT + (size_t)ht_row(d) * 4096;
#pragma unroll
    for (int i = 0; i < 4; i += 2) st16d(hrow + (16 * (16 * (4 * hf + i + (g & 1)) + 15 - j) + 8 * (g >> 1)) * 2, widen16(hv[i], hv[i + 1]));
  }
  __builtin_amdgcn_s_setprio(0);
  if (wave < 4) {
    WAIT_VM(0);
    if (lane == 0 && __hip_atomic_fetch_add(LDS(u32, lds0 + F_OFF_X + 32), 1u, __ATOMIC_RELAXED, __HIP_MEMORY_SCOPE_WORKGROUP) == 3u)
      set_flag((u32*)(A.ws + WS_FL) + FL_HE + grp * 32, *LDS(u32, lds0 + OFF_TAG));
  }
  STAMP(6);
}

__device__ __forceinline__ void gemm1_loader(const Args& A, u32 lds0, int lane, int wave, u32 tag) {
  const bool conv_wg = (blockIdx.x >> 3) >= 12;
  const int xcd = blockIdx.x & 7, idx = blockIdx.x >> 3;
  const int pm = (xcd & 1) * 8 + (idx & 7), pn = (xcd >> 1) * 4 + (idx >> 3);
  const int t0 = pm * 128;
  const u32* flags = (const u32*)(A.ws + WS_FL);
    const int lw = wave - 4;
    const int k0 = 5 * (pn & 1);
    int kH = 0, kW = 0;
    const int row = 8 * lw + (lane >> 4);
    const int chk = (lane & 15) ^ (((row & 3) << 1) | (((row >> 3) & 1) << 3));
    const char* srcH = A.ws + WS_HT + (size_t)(row + 32 * k0) * 4096 + t0 * 2 + chk * 16;
    const char* srcW = A.ws + WS_W1 + (size_t)pn * 245760 + (size_t)k0 * 12288 + lw * 3072 + lane * 16;
    auto kstep = [&](int p) { return (p / 10) * 10 + (p % 10 + k0) % 10; };
    const u32 dH = lds0 + lw * 2048, dW = lds0 + 8192 + lw * 3072;
    auto issueH = [&](u32 so) {
      glds16d(srcH, dH + so);
      glds16d(srcH + 4 * 4096, dH + so + 1024);
      srcH += (ptrdiff_t)(kstep(kH + 1) - kstep(kH)) * (32 * 4096); ++kH;
    };
    auto issueW = [&](u32 so) {
      glds16d(srcW, dW + so);
      glds16d(srcW + 1024, dW + so + 1024);
      glds16d(srcW + 2048, dW + so + 2048);
      srcW += (ptrdiff_t)(kstep(kW + 1) - kstep(kW)) * 12288; ++kW;
    };
    u32 so = 0;
    auto issue = [&]() { issueH(so); issueW(so); so = so == (G1_NS - 1) * G1_STG ? 0 : so + G1_STG; };
    if (conv_wg) {
      bool seen = false;
      if (wave == 4) seen = poll_once<4>(flags + FL_W, 256, tag, lane);
      WAIT_VM(0);
      if (lane == 0 && __hip_atomic_fetch_add(LDS(u32, lds0 + OFF_TAG + 4), 1u, __ATOMIC_RELAXED, __HIP_MEMORY_SCOPE_WORKGROUP) == 3u)
        set_flag((u32*)(A.ws + WS_FL) + FL_HL + (xcd * 20 + idx - 12) * 32, tag);
      if (wave == 4 && !seen) wait_flags<4>(flags + FL_W, 256, tag, lane);
      BARRIER();
      BARRIER();
      issue(); issue();
      WAIT_VM(5); BARRIER();
      issue(); issue();
      WAIT_VM(10); BARRIER();
      issue(); issue();
      WAIT_VM(15); BARRIER();
      issue(); issue();
      WAIT_VM(20); BARRIER();
      issue();
      WAIT_VM(20); BARRIER();
      issue();
      if (wave == 4) wait_flags<3>(flags + FL_HL, 160, tag, lane);
      WAIT_VM(20); BARRIER();
    } else {
      if (wave == 4) wait_flags<2>(flags + FL_W, 96, tag, lane);
      BARRIER();
#pragma unroll
      for (int s = 0; s < G1_NS; ++s) issueW(s * G1_STG);
      if (wave == 4) wait_flags<3>(flags + FL_HE, 160, tag, lane);
      BARRIER();
      issueH(0); issueH(G1_STG); issueH(2 * G1_STG);
      WAIT_VM(4); BARRIER();
      issueH(3 * G1_STG); issueH(4 * G1_STG);
      WAIT_VM(6); BARRIER();
      issueH(5 * G1_STG); issueH(6 * G1_STG);
      WAIT_VM(8); BARRIER();
      issue(); WAIT_VM(11); BARRIER();
      issue(); WAIT_VM(14); BARRIER();
      issue();
      if (wave == 4) wait_flags<3>(flags + FL_HL, 160, tag, lane);
      WAIT_VM(17); BARRIER();
    }
    for (int i = 5; i < G1_NK - 5; ++i) {
      issue();
      WAIT_VM(20); BARRIER();
    }
    WAIT_VM(15); BARRIER(); WAIT_VM(10); BARRIER(); WAIT_VM(5); BARRIER(); WAIT_VM(0); BARRIER();
}

__device__ __forceinline__ void gemm1_compute(const Args& A, u32 lds0, int lane, int wave) {
  const int xcd = blockIdx.x & 7, idx = blockIdx.x >> 3;
  const int pm = (xcd & 1) * 8 + (idx & 7), pn = (xcd >> 1) * 4 + (idx >> 3);
  const int t0 = pm * 128;
  const int wt = wave & 1, wf = wave >> 1;
  const int g = lane >> 4, q = (lane & 15) >> 2, p = lane & 3;
  const int rot = (pn >> 1) & 3;
  const u32 hrow = (8 * g + q) * 256 + 8 * (p & 1);
  const u32 hx = q | ((g & 1) << 2);
  u32 hc[4];
#pragma unroll
  for (int j = 0; j < 4; ++j) hc[j] = lds0 + hrow + ((((u32)(4 * wt + ((j + rot) & 3)) ^ hx) << 1) | (p >> 1)) * 16;
  u32 wc = lds0 + 8192 + wf * 6144 + lane * 16, so = 0;
  f32x4 acc[6][4];
#pragma unroll
  for (int i = 0; i < 6; ++i)
#pragma unroll
    for (int j = 0; j < 4; ++j) acc[i][j] = (f32x4){0.f, 0.f, 0.f, 0.f};
  f32x4 st1 = {0.f, 0.f, 0.f, 0.f}, st2 = st1;
  bf16x8 hf[2][4], wfr[2][6];
  auto rd = [&](int set) {
    wfr[set][0] = *LDS(bf16x8, wc);
#pragma unroll
    for (int j = 0; j < 4; ++j) hf[set][j] = tr_frag(hc[j], hc[j] + 1024);
#pragma unroll
    for (int i = 1; i < 6; ++i) wfr[set][i] = *LDS(bf16x8, wc + i * 1024);
  };
  auto adv = [&]() {
    u32 dl = so == (G1_NS - 1) * G1_STG ? (u32)(-(G1_NS - 1) * G1_STG) : (u32)G1_STG;
    so += dl;
    asm volatile("" : "+s"(dl));
#pragma unroll
    for (int j = 0; j < 4; ++j) hc[j] += dl;
    wc += dl;
  };
  auto mma = [&](int set) {
#pragma unroll
    for (int i = 0; i < 6; ++i)
#pragma unroll
      for (int j = 0; j < 4; ++j) acc[i][j] = mfma16(wfr[set][i], hf[set][j], acc[i][j]);
  };
  auto stat = [&](int set) {
    st1 = mfma16(ONES_BF16, hf[set][0], st1);
    st2 = mfma16(hf[set][0], hf[set][0], st2);
  };
#define G1_SCHED(extra) do { \
    SG(0x008, 1); SG(0x100, 1); SG(0x008, 1); SG(0x100, 1); SG(0x008, 1); SG(0x100, 1); SG(0x008, 1); SG(0x100, 1); \
    SG(0x008, 1); SG(0x100, 1); SG(0x008, 1); SG(0x100, 1); SG(0x008, 1); SG(0x100, 1); SG(0x008, 1); SG(0x100, 1); \
    SG(0x008, 1); SG(0x100, 1); SG(0x008, 2); SG(0x100, 1); SG(0x008, 2); SG(0x100, 1); SG(0x008, 2); SG(0x100, 1); \
    SG(0x008, 2); SG(0x100, 1); SG(0x008, 1); SG(0x100, 1); SG(0x008, 1); SG(0x002, 1); SG(0x008, 1); SG(0x002, 1); \
    SG(0x008, 1); SG(0x002, 1); SG(0x008, 1); SG(0x002, 1); SG(0x008, 1); SG(0x002, 1); SG(0x008, 1 + (extra)); } while (0)
  BARRIER();
  BARRIER();
  BARRIER();
  STAMP(8);
  rd(0); adv();
#pragma unroll
  for (int i = 1; i < G1_NK - 1; i += 2) {
    BARRIER();
    SB(); rd(1); mma(0); adv();
    if ((i - 1) % 10 < 5) { stat(0); G1_SCHED(2); } else { G1_SCHED(0); }
    SB();
    BARRIER();
    SB(); rd(0); mma(1); adv();
    if (i % 10 < 5) { stat(1); G1_SCHED(2); } else { G1_SCHED(0); }
    SB();
  }
  BARRIER();
  SB(); rd(1); mma(0); G1_SCHED(0); SB();
  WAIT_LGKM0();
  BARRIER();
#pragma unroll
  for (int i = 0; i < 3; ++i)
#pragma unroll
    for (int j = 0; j < 4; ++j) {
      acc[i][j] = mfma16(wfr[1][i], hf[1][j], acc[i][j]);
      acc[3 + i][j] = mfma16(wfr[1][3 + i], hf[1][j], acc[3 + i][j]);
    }
  STAMP(9);
  {
    char* ob = A.ws + WS_HM + ((size_t)(pm * 8 + 4 * wt) * 48 + 3 * pn) * 1024 + (lane & 15) * 16 + (g >> 1) * 256;
#pragma unroll
    for (int j = 0; j < 4; j += 2) {
      char* oj = ob + (size_t)((j + (g & 1) + rot) & 3) * 49152;
#pragma unroll
      for (int i = 0; i < 3; ++i) {
        u32x2 o[2];
#pragma unroll
        for (int jj = 0; jj < 2; ++jj) {
          const f32x4 gv = acc[i][j + jj], uv = acc[3 + i][j + jj];
          o[jj] = (u32x2){pack_bf16(fmaxf(gv[0], 0.f) * uv[0], fmaxf(gv[1], 0.f) * uv[1]), pack_bf16(fmaxf(gv[2], 0.f) * uv[2], fmaxf(gv[3], 0.f) * uv[3])};
        }
        const int E = 6 * wf + 2 * i;
        st16d(oj + (E >> 2) * 1024 + (E & 3) * 256, widen16(o[0], o[1]));
      }
    }
  }
  if (wf == 0 && pn < 8 && g == ((lane & 15) >> 2))
    __hip_atomic_store((gu64*)((u64*)(A.ws + WS_PT) + (size_t)(pn & 1) * L + t0 + (4 * wt + rot) * 16 + (lane & 15)),
                       ((u64)__float_as_uint(diag16(st2, lane)) << 32) | __float_as_uint(st1[0]), RLX_AGENT);
  WAIT_VM(0);
}

__device__ __forceinline__ void gemm2_loader(const Args& A, u32 lds0, int lane, int wave, u32 tag) {
  const int xcd = blockIdx.x & 7, idx = blockIdx.x >> 3;
  const int pm = (xcd & 3) * 8 + (idx & 7), pn = (xcd >> 2) * 4 + (idx >> 3);
  const int t0 = pm * 64, d0 = pn * 80;
  u32* flags = (u32*)(A.ws + WS_FL);
    const int lw = wave - 4;
    const char* srcP = A.ws + WS_HM + (size_t)(pm * 4 + lw) * 49152 + lane * 16;
    const char* srcW = A.ws + WS_WD + (size_t)pn * 245760 + lw * 5120 + lane * 16;
    const u32 dP = lds0 + lw * 4096, dW = lds0 + 16384 + lw * 5120;
    auto issueP = [&](u32 so) {
      glds16d(srcP, dP + so); glds16d(srcP + 1024, dP + so + 1024); glds16d(srcP + 2048, dP + so + 2048); glds16d(srcP + 3072, dP + so + 3072);
      srcP += 4096;
    };
    auto issueW = [&](u32 so) {
      glds16d(srcW, dW + so); glds16d(srcW + 1024, dW + so + 1024); glds16d(srcW + 2048, dW + so + 2048);
      glds16d(srcW + 3072, dW + so + 3072); glds16d(srcW + 4096, dW + so + 4096);
      srcW += 20480;
    };
    BARRIER();
    issueW(0); issueW(G2_STG); issueW(2 * G2_STG); issueW(3 * G2_STG);
#pragma unroll
    for (int k = 0; k < 3; ++k) {
      const int jj = lw + 4 * k < 10 ? lw + 4 * k : lw + 4;
      glds16d(A.ws + WS_HT + (size_t)ht_row(d0 + 8 * jj + (lane >> 3)) * 4096 + t0 * 2 + (lane & 7) * 16, UNI(lds0 + G2_OFF_H + jj * 1024));
    }
    BARRIER();
    if (wave == 4) wait_flags<1>(flags + FL_P + (pm >> 1) * 16 * 32, 16, tag, lane);
    BARRIER();
    STAMP(10);
    issueP(0); issueP(G2_STG);
    WAIT_VM(4); BARRIER();
    issueP(2 * G2_STG); issueP(3 * G2_STG);
    WAIT_VM(8); BARRIER();
    u32 so = 0;
    auto issue = [&]() { issueP(so); issueW(so); so = so == 3 * G2_STG ? 0 : so + G2_STG; };
    issue(); WAIT_VM(13); BARRIER();
    for (int i = 2; i < G2_NK - 3; ++i) {
      issue();
      WAIT_VM(18); BARRIER();
    }
    WAIT_VM(9); BARRIER();
    WAIT_VM(0); BARRIER();
    BARRIER();
    BARRIER();
}

__device__ __forceinline__ void gemm2_compute(const Args& A, u32 lds0, int tid, int lane, int wave, u32 tag) {
  const int xcd = blockIdx.x & 7, idx = blockIdx.x >> 3;
  const int pm = (xcd & 3) * 8 + (idx & 7), pn = (xcd >> 2) * 4 + (idx >> 3);
  const int t0 = pm * 64, d0 = pn * 80;
  u32* flags = (u32*)(A.ws + WS_FL);
  BARRIER();
  if (tid == 0) {
    const int pm1 = (xcd & 1) * 8 + (idx & 7), pn1 = (xcd >> 1) * 4 + (idx >> 3);
    set_flag(flags + FL_P + (pm1 * 16 + pn1) * 32, tag);
  }
  const int wt = wave & 1, wk = wave >> 1;
  u32 pc = lds0 + (wt * 2) * 4096 + wk * 2048 + lane * 16, wc = lds0 + 16384 + wk * 2 * 5120 + lane * 16, so = 0;
  f32x4 acc[5][2];
#pragma unroll
  for (int i = 0; i < 5; ++i) { acc[i][0] = (f32x4){0.f, 0.f, 0.f, 0.f}; acc[i][1] = acc[i][0]; }
  bf16x8 pf[2][2][2], wf[2][2][5];
  auto rd = [&](int set) {
#pragma unroll
    for (int ks = 0; ks < 2; ++ks) {
      wf[set][ks][0] = *LDS(bf16x8, wc + ks * 5120);
#pragma unroll
      for (int j = 0; j < 2; ++j) pf[set][ks][j] = *LDS(bf16x8, pc + j * 4096 + ks * 1024);
#pragma unroll
      for (int i = 1; i < 5; ++i) wf[set][ks][i] = *LDS(bf16x8, wc + ks * 5120 + i * 1024);
    }
  };
  auto adv = [&]() {
    u32 dl = so == 3 * G2_STG ? (u32)(-3 * G2_STG) : (u32)G2_STG;
    so += dl;
    asm volatile("" : "+s"(dl));
    pc += dl; wc += dl;
  };
  auto mma = [&](int set) {
#pragma unroll
    for (int ks = 0; ks < 2; ++ks)
#pragma unroll
      for (int i = 0; i < 5; ++i)
#pragma unroll
        for (int j = 0; j < 2; ++j) acc[i][j] = mfma16(wf[set][ks][i], pf[set][ks][j], acc[i][j]);
  };
#define G2_SCHED() do { _Pragma("unroll") for (int k_ = 0; k_ < 4; ++k_) { SG(0x008, 1); SG(0x100, 1); SG(0x008, 1); SG(0x100, 1); SG(0x008, 1); SG(0x100, 1); SG(0x008, 1); } \
    SG(0x008, 1); SG(0x100, 1); SG(0x008, 1); SG(0x100, 1); SG(0x008, 2); } while (0)
  BARRIER();
  const int tl = wt * 32 + wk * 16;
  u64 pw[2];
#pragma unroll
  for (int k = 0; k < 2; ++k) pw[k] = __hip_atomic_load((gu64*)((u64*)(A.ws + WS_PT) + (size_t)k * L + t0 + tl + (lane & 15)), RLX_AGENT);
  BARRIER();
  STAMP(11);
  rd(0); adv();
  WAIT_LGKM0();
#pragma unroll
  for (int i = 1; i < G2_NK - 1; i += 2) {
    BARRIER();
    SB(); rd(1); mma(0); adv(); G2_SCHED(); SB();
    WAIT_LGKM0();
    BARRIER();
    SB(); rd(0); mma(1); adv(); G2_SCHED(); SB();
    WAIT_LGKM0();
  }
  BARRIER();
  SB(); rd(1); mma(0); G2_SCHED(); SB();
  WAIT_LGKM0();
  mma(1);
  STAMP(12);
  float rr;
  {
    float s1 = 0.f, s2 = 0.f;
#pragma unroll
    for (int k = 0; k < 2; ++k) { s1 += __uint_as_float((u32)pw[k]); s2 += __uint_as_float((u32)(pw[k] >> 32)); }
    const float mean = s1 * (1.f / D);
    const float var = fmaxf(s2 * (1.f / D) - mean * mean, 0.f);
    const float r = __builtin_amdgcn_rcpf(rbf(__builtin_amdgcn_sqrtf(rbf(rbf(var) + 1e-5f))));
    rr = r * r;
  }
  const int g = lane >> 4, q = (lane & 15) >> 2, p = lane & 3;
  s16x4 hv[5];
#pragma unroll
  for (int i = 0; i < 5; ++i) hv[i] = tr_read(lds0 + G2_OFF_H + (16 * i + 4 * g + q) * 128 + (tl + 4 * p) * 2);
  BARRIER();
  const u32 ex = lds0 + (wave ^ 2) * 5120 + lane * 16, exr = lds0 + wave * 5120 + lane * 16;
#pragma unroll
  for (int i = 0; i < 5; ++i) { const f32x4 a0 = acc[i][0], a1 = acc[i][1]; *LDS(f32x4, ex + i * 1024) = wk ? +a0 : +a1; }
  WAIT_LGKM0();
  BARRIER();
  f32x4 m[5];
#pragma unroll
  for (int i = 0; i < 5; ++i) m[i] = *LDS(f32x4, exr + i * 1024);
  SB();
#pragma unroll
  for (int i = 0; i < 5; ++i) { const f32x4 a0 = acc[i][0], a1 = acc[i][1]; m[i] = ((wk ? +a1 : +a0) + m[i]) * rr; }
  u32x2 o2[5];
#pragma unroll
  for (int i = 0; i < 5; ++i) o2[i] = (u32x2){pack_bf16(m[i][0], m[i][1]), pack_bf16(m[i][2], m[i][3])};
#pragma unroll
  for (int i = 0; i < 5; ++i)
    o2[i] = (u32x2){pack_bf16(bf2f((u16)hv[i][0]) + lo_bf(o2[i][0]), bf2f((u16)hv[i][1]) + hi_bf(o2[i][0])),
                    pack_bf16(bf2f((u16)hv[i][2]) + lo_bf(o2[i][1]), bf2f((u16)hv[i][3]) + hi_bf(o2[i][1]))};
  char* orow = (char*)A.out + (size_t)(t0 + tl + (lane & 15)) * 1280 + d0 * 2;
  *(u32x4*)(orow + (16 * (0 + (g & 1)) + 8 * (g >> 1)) * 2) = widen16(o2[0], o2[1]);
  *(u32x4*)(orow + (16 * (2 + (g & 1)) + 8 * (g >> 1)) * 2) = widen16(o2[2], o2[3]);
  *(u32x2*)(orow + (64 + 4 * g) * 2) = o2[4];
  STAMP(13);
}

__global__ __launch_bounds__(512, 2) void s4_reglu_block(const Args A) {
  STAMP(0);
  extern __shared__ __attribute__((aligned(16))) char smem[];
  const u32 lds0 = (u32)(size_t)(LAS char*)smem;
  const int tid = threadIdx.x, lane = tid & 63;
  const int wave = __builtin_amdgcn_readfirstlane(tid >> 6);
  const int xcd = blockIdx.x & 7, idx = blockIdx.x >> 3;
  u32* flags = (u32*)(A.ws + WS_FL);
  u32 tag7 = 0;
  if (wave == 7) {
    tag7 = __hip_atomic_load((gu32*)flags, RLX_AGENT) + 1u;
    if (lane == 0) { *LDS(u32, lds0 + OFF_TAG) = tag7; *LDS(u32, lds0 + OFF_TAG + 4) = 0u; set_flag(flags + FL_STARTED + blockIdx.x, tag7); }
  }
  if (idx < 12) {
    int wtask;
    if (idx < 8) {
      const int task = xcd * 8 + idx;
      ln1_task(A, task, lds0, tid, lane, wave);
      wtask = task < 16 ? 32 + task : 48 + task - 16;
    } else {
      wtask = xcd * 4 + (idx - 8);
    }
    weight_task(A, wtask, lds0, tid, lane, wave);
    STAMP(6);
    if (blockIdx.x == 95 && wave == 7) {
      const u64 t0 = __builtin_amdgcn_s_memrealtime();
      for (;;) {
        bool ok = true;
#pragma unroll
        for (int k = 0; k < 4; ++k) ok &= __hip_atomic_load((gu32*)(flags + FL_STARTED + k * 64 + lane), RLX_AGENT) == tag7;
        if (__all(ok)) { if (lane == 0) set_flag(flags, tag7); break; }
        if (__builtin_amdgcn_s_memrealtime() - t0 > SPIN_TICKS) break;
        __builtin_amdgcn_s_sleep(8);
      }
    }
  } else {
    conv_role(A, xcd * 20 + idx - 12, lds0, tid, lane, wave, tag7);
  }
  WAIT_LGKM0();
  BARRIER();
  const u32 tag = UNI(*LDS(u32, lds0 + OFF_TAG));
  if (wave >= 4) {
    gemm1_loader(A, lds0, lane, wave, tag);
    gemm2_loader(A, lds0, lane, wave, tag);
  } else {
    gemm1_compute(A, lds0, lane, wave);
    gemm2_compute(A, lds0, tid, lane, wave, tag);
  }
}

extern "C" void kernel_launch(void* const* d_in, const int* in_sizes, int n_in, void* d_out, int out_size, void* d_ws,
                              size_t ws_size, hipStream_t stream) {
  Args A;
  A.x = (const u16*)d_in[0];
  A.Are = (const u16*)d_in[1]; A.Aim = (const u16*)d_in[2]; A.Bre = (const u16*)d_in[3]; A.Bim = (const u16*)d_in[4];
  A.Cre = (const u16*)d_in[5]; A.Cim = (const u16*)d_in[6]; A.logdt = (const u16*)d_in[7]; A.Dskip = (const u16*)d_in[8];
  A.Wg = (const u16*)d_in[10]; A.Wu = (const u16*)d_in[11]; A.Wd = (const u16*)d_in[12];
  A.out = (u16*)d_out;
  A.ws = (char*)d_ws;
  (void)hipFuncSetAttribute((const void*)s4_reglu_block, hipFuncAttributeMaxDynamicSharedMemorySize, LDS_BYTES);
  s4_reglu_block<<<256, 512, LDS_BYTES, stream>>>(A);
}
```

```cpp
#include <hip/hip_runtime.h>
#include <hip/hip_bf16.h>
#include <stdint.h>

typedef unsigned short u16;
typedef unsigned u32;
typedef unsigned long long u64;
typedef short s16x4 __attribute__((ext_vector_type(4)));
typedef __bf16 bf16x8 __attribute__((ext_vector_type(8)));
typedef __bf16 bf16x2 __attribute__((ext_vector_type(2)));
typedef _Float16 f16x2 __attribute__((ext_vector_type(2)));
typedef _Float16 f16x8 __attribute__((ext_vector_type(8)));
typedef float f32x16 __attribute__((ext_vector_type(16)));
typedef float f32x4 __attribute__((ext_vector_type(4)));
typedef float f32x2 __attribute__((ext_vector_type(2)));
typedef u32 u32x4 __attribute__((ext_vector_type(4)));
typedef u32 u32x2 __attribute__((ext_vector_type(2)));
typedef __attribute__((address_space(1))) u32 gu32;
typedef __attribute__((address_space(1))) u64 gu64;
#define LAS __attribute__((address_space(3)))
#define LDS(T, a) ((LAS T*)(size_t)(a))

constexpr int L = 2048, D = 640, NS = 192, F = 1536;

constexpr size_t WS_UT = 0;
constexpr size_t WS_LN = 4u << 20;
constexpr size_t WS_HT = 8u << 20;
constexpr size_t WS_HM = 12u << 20;
constexpr size_t WS_W1 = 32u << 20;
constexpr size_t WS_WD = 36u << 20;
constexpr size_t WS_PT = 40u << 20;
constexpr size_t WS_FL = 52u << 20;
constexpr int FL_STARTED = 1024;
constexpr int FL_U = 2048;
constexpr int FL_W = FL_U + 64 * 32;
constexpr int FL_HE = FL_W + 96 * 32;
constexpr int FL_HL = FL_HE + 160 * 32;
constexpr int FL_P = FL_HL + 160 * 32;
constexpr int UT_ROW = 4160;

__host__ __device__ constexpr int ht_row(int d) { return ((d & 2) ? 320 : 0) + (d >> 2) * 2 + (d & 1); }
__host__ __device__ constexpr int ht_chan(int r) { return r < 320 ? (r >> 1) * 4 + (r & 1) : ((r - 320) >> 1) * 4 + 2 + (r & 1); }
static_assert(ht_chan(ht_row(639)) == 639 && ht_chan(ht_row(5)) == 5 && ht_row(2) == 320, "permutation");

struct Args {
  const u16 *x, *Are, *Aim, *Bre, *Bim, *Cre, *Cim, *logdt, *Dskip, *Wg, *Wu, *Wd;
  u16* out;
  char* ws;
};

__device__ __forceinline__ float bf2f(u16 v) { return __uint_as_float(((u32)v) << 16); }
__device__ __forceinline__ u32 pack_bf16(float lo, float hi) { return __builtin_bit_cast(u32, __builtin_convertvector((f32x2){lo, hi}, bf16x2)); }
__device__ __forceinline__ u32 pack_f16(float lo, float hi) { return __builtin_bit_cast(u32, __builtin_convertvector((f32x2){lo, hi}, f16x2)); }
__device__ __forceinline__ float rbf(float f) { return __uint_as_float(pack_bf16(f, f) << 16); }
__device__ __forceinline__ float lo_bf(u32 v) { return __uint_as_float(v << 16); }
__device__ __forceinline__ float hi_bf(u32 v) { return __uint_as_float(v & 0xffff0000u); }

#define WAIT_VM(n) asm volatile("s_waitcnt vmcnt(%0)" ::"n"(n) : "memory")
#define WAIT_LGKM0() do { asm volatile("" ::: "memory"); __builtin_amdgcn_s_waitcnt(0xC07F); asm volatile("" ::: "memory"); } while (0)
#define BARRIER() do { asm volatile("" ::: "memory"); __builtin_amdgcn_s_barrier(); asm volatile("" ::: "memory"); } while (0)
#define SB() __builtin_amdgcn_sched_barrier(0)
#define SG(m, n) __builtin_amdgcn_sched_group_barrier(m, n, 0)
#define RLX_AGENT __ATOMIC_RELAXED, __HIP_MEMORY_SCOPE_AGENT

__device__ __forceinline__ void glds16(const void* gsrc, u32 lds_dst) {
  u32 keep;
  asm volatile("s_mov_b32 %0, m0\n\ts_mov_b32 m0, %2\n\ts_nop 0\n\tglobal_load_lds_dwordx4 %1, off\n\ts_mov_b32 m0, %0"
               : "=&s"(keep) : "v"(gsrc), "s"(__builtin_amdgcn_readfirstlane(lds_dst)) : "memory");
}
__device__ __forceinline__ void glds16d(const void* gsrc, u32 lds_dst) {
  u32 keep;
  asm volatile("s_mov_b32 %0, m0\n\ts_mov_b32 m0, %2\n\ts_nop 0\n\tglobal_load_lds_dwordx4 %1, off sc1\n\ts_mov_b32 m0, %0"
               : "=&s"(keep) : "v"(gsrc), "s"(__builtin_amdgcn_readfirstlane(lds_dst)) : "memory");
}
#define UNI(v) ((u32)__builtin_amdgcn_readfirstlane((int)(v)))
__device__ __forceinline__ void st16d(void* p, u32x4 v) {
  asm volatile("global_store_dwordx4 %0, %1, off sc1\n\ts_nop 1" :: "v"(p), "v"(v) : "memory");
}
constexpr u64 SPIN_TICKS = 300000;
constexpr int POLL_SLEEP = 2;
template <int NL>
__device__ __forceinline__ void wait_flags(const u32* flags, int n, u32 tag, int lane) {
  const u64 t0 = __builtin_amdgcn_s_memrealtime();
  for (;;) {
    bool ok = true;
#pragma unroll
    for (int k = 0; k < NL; ++k) {
      const int i = k * 64 + lane;
      ok &= __hip_atomic_load((gu32*)(flags + (i < n ? i : n - 1) * 32), RLX_AGENT) == tag;
    }
    if (__all(ok)) break;
    if (__builtin_amdgcn_s_memrealtime() - t0 > SPIN_TICKS) break;
    __builtin_amdgcn_s_sleep(POLL_SLEEP);
  }
  asm volatile("" ::: "memory");
}
template <int NL>
__device__ __forceinline__ bool poll_once(const u32* flags, int n, u32 tag, int lane) {
  bool ok = true;
#pragma unroll
  for (int k = 0; k < NL; ++k) {
    const int i = k * 64 + lane;
    ok &= __hip_atomic_load((gu32*)(flags + (i < n ? i : n - 1) * 32), RLX_AGENT) == tag;
  }
  return __all(ok);
}
__device__ __forceinline__ void set_flag(u32* flag, u32 tag) { __hip_atomic_store((gu32*)flag, tag, RLX_AGENT); }

__device__ __forceinline__ s16x4 tr_read(u32 addr) { return __builtin_amdgcn_ds_read_tr16_b64_v4i16(LDS(s16x4, addr)); }
__device__ __forceinline__ u32x4 tr_read2(u32 a0, u32 a1) {
  const s16x4 a = tr_read(a0), b = tr_read(a1);
  return __builtin_bit_cast(u32x4, __builtin_shufflevector(a, b, 0, 1, 2, 3, 4, 5, 6, 7));
}
__device__ __forceinline__ bf16x8 tr_frag(u32 a0, u32 a1) { return __builtin_bit_cast(bf16x8, tr_read2(a0, a1)); }
__device__ __forceinline__ f32x4 mfma16(bf16x8 a, bf16x8 b, f32x4 c) { return __builtin_amdgcn_mfma_f32_16x16x32_bf16(a, b, c, 0, 0, 0); }
#define ONES_BF16 __builtin_bit_cast(bf16x8, (u32x4){0x3f803f80u, 0x3f803f80u, 0x3f803f80u, 0x3f803f80u})
__device__ __forceinline__ float diag16(f32x4 a, int lane) { const int r = lane & 3; return r == 0 ? a[0] : r == 1 ? a[1] : r == 2 ? a[2] : a[3]; }

template <int CTRL> __device__ __forceinline__ float dpp_f(float v) {
  return __builtin_bit_cast(float, __builtin_amdgcn_update_dpp(0, __builtin_bit_cast(int, v), CTRL, 0xf, 0xf, true));
}
__device__ __forceinline__ float wave_max(float v) {
  v = fmaxf(v, dpp_f<0xB1>(v));
  v = fmaxf(v, dpp_f<0x4E>(v));
  v = fmaxf(v, dpp_f<0x141>(v));
  v = fmaxf(v, dpp_f<0x140>(v));
  { auto r = __builtin_amdgcn_permlane16_swap(__float_as_uint(v), __float_as_uint(v), false, false); v = fmaxf(__uint_as_float(r[0]), __uint_as_float(r[1])); }
  { auto r = __builtin_amdgcn_permlane32_swap(__float_as_uint(v), __float_as_uint(v), false, false); v = fmaxf(__uint_as_float(r[0]), __uint_as_float(r[1])); }
  return v;
}
__device__ __forceinline__ u32x4 widen16(u32x2 a, u32x2 b) {
  const auto s0 = __builtin_amdgcn_permlane16_swap(a[0], b[0], false, false);
  const auto s1 = __builtin_amdgcn_permlane16_swap(a[1], b[1], false, false);
  return (u32x4){s0[0], s1[0], s0[1], s1[1]};
}

constexpr int LDS_BYTES = 163840;
constexpr int OFF_TAG = LDS_BYTES - 16;
constexpr int C_ROW = 272;
constexpr int C_SCR = 60 * C_ROW;
constexpr int C_KCH = 4608;
constexpr int C_UCP = 4160, C_UCH = 2 * C_UCP;
constexpr int F_OFF_P = 4 * C_SCR;
constexpr int F_OFF_S = F_OFF_P + 9216;
constexpr int F_OFF_X = F_OFF_S + 16;
constexpr int F_OFF_K = 74752;
constexpr int F_OFF_U = F_OFF_K + 4 * C_KCH;
constexpr int F_OFF_L = F_OFF_U + 4 * C_UCH;
constexpr int LN_RS = 1344;
constexpr int G1_STG = 20480, G1_NS = 7, G1_NK = 20;
constexpr int G2_STG = 36864, G2_NK = 12;
constexpr int G2_OFF_H = G2_STG * 4;
static_assert(F_OFF_L + 16384 <= OFF_TAG && G1_STG * G1_NS <= OFF_TAG && G2_OFF_H + 10240 <= OFF_TAG, "LDS");

__device__ __forceinline__ void ln1_task(const Args& A, int task, u32 lds0, int tid, int lane, int wave) {
  char* uT = A.ws + WS_UT;
  const int t0 = task * 32;
  const u32 tX = lds0, tU = lds0 + 40960;
#pragma unroll
  for (int k = 0; k < 5; ++k) {
    const int P = (wave * 5 + k) * 64 + lane, row = P / 80, chk = (P % 80) ^ (row & 15);
    glds16((const char*)A.x + (size_t)(t0 + row) * 1280 + chk * 16, UNI(tX + (wave * 5 + k) * 1024));
  }
  WAIT_VM(0);
  BARRIER();
  {
    const int row = 4 * wave + (lane >> 4);
    f32x2 xv[20];
#pragma unroll
    for (int k = 0; k < 5; ++k) {
      const u32x4 w = *LDS(u32x4, tX + row * 1280 + (((lane & 15) + 16 * k) ^ (row & 15)) * 16);
#pragma unroll
      for (int e = 0; e < 4; ++e) xv[4 * k + e] = (f32x2){lo_bf(w[e]), hi_bf(w[e])};
    }
    f32x2 a1 = xv[0] + xv[1], a2 = xv[0] * xv[0], b1 = xv[2] + xv[3], b2 = xv[1] * xv[1];
    a2 = __builtin_elementwise_fma(xv[2], xv[2], a2); b2 = __builtin_elementwise_fma(xv[3], xv[3], b2);
#pragma unroll
    for (int k = 4; k < 20; k += 2) {
      a1 += xv[k]; b1 += xv[k + 1];
      a2 = __builtin_elementwise_fma(xv[k], xv[k], a2); b2 = __builtin_elementwise_fma(xv[k + 1], xv[k + 1], b2);
    }
    float s1 = (a1.x + a1.y) + (b1.x + b1.y), s2 = (a2.x + a2.y) + (b2.x + b2.y);
    s1 += dpp_f<0xB1>(s1); s2 += dpp_f<0xB1>(s2);
    s1 += dpp_f<0x4E>(s1); s2 += dpp_f<0x4E>(s2);
    s1 += dpp_f<0x141>(s1); s2 += dpp_f<0x141>(s2);
    s1 += dpp_f<0x140>(s1); s2 += dpp_f<0x140>(s2);
    const float mean = s1 * (1.f / D);
    const float var = fmaxf(s2 * (1.f / D) - mean * mean, 0.f);
    const float mb = rbf(mean), sd = rbf(__builtin_amdgcn_sqrtf(rbf(rbf(var) + 1e-5f)));
    const float ri = __builtin_amdgcn_rcpf(sd), mi = -mb * ri;
    if ((lane & 15) == 0)
      __hip_atomic_store((gu64*)((u64*)(A.ws + WS_LN) + t0 + row), ((u64)__float_as_uint(mb) << 32) | __float_as_uint(sd), RLX_AGENT);
#pragma unroll
    for (int k = 0; k < 5; ++k) {
      u32x4 o;
#pragma unroll
      for (int e = 0; e < 4; ++e) {
        const f32x2 u = __builtin_elementwise_fma(xv[4 * k + e], (f32x2){ri, ri}, (f32x2){mi, mi});
        o[e] = pack_f16(u.x, u.y);
      }
      *LDS(u32x4, tU + row * LN_RS + ((lane & 15) + 16 * k) * 16) = o;
    }
  }
  WAIT_LGKM0();
  BARRIER();
  {
    const int g = lane >> 4, q = (lane & 15) >> 2, p = lane & 3;
    u32x4 ov[5];
#pragma unroll
    for (int k = 0; k < 5; ++k) {
      const u32 a = tU + (8 * g + q) * LN_RS + ((wave * 5 + k) * 16 + 4 * p) * 2;
      ov[k] = tr_read2(a, a + 4 * LN_RS);
    }
#pragma unroll
    for (int k = 0; k < 5; ++k) st16d(uT + (size_t)((wave * 5 + k) * 16 + (lane & 15)) * UT_ROW + 32 + (t0 + 8 * g) * 2, ov[k]);
  }
  WAIT_VM(0);
  BARRIER();
  if (tid == 0) set_flag((u32*)(A.ws + WS_FL) + FL_U + task * 32, *LDS(u32, lds0 + OFF_TAG));
}

__device__ __forceinline__ void weight_task(const Args& A, int wtask, u32 lds0, int tid, int lane, int wave) {
  const int g = lane >> 4, q = (lane & 15) >> 2, p = lane & 3;
  if (wtask < 48) {
    const u16* W = wtask < 24 ? A.Wg : A.Wu;
    const int c0 = (wtask % 24) * 64;
    {
      const int row = 8 * wave + (lane >> 3), chk = (lane & 7) ^ ((row & 3) << 1);
      const char* src = (const char*)W + c0 * 2 + chk * 16;
#pragma unroll
      for (int jj = 0; jj < 10; ++jj)
        glds16(src + (size_t)ht_chan(row + 64 * jj) * 3072, UNI(lds0 + (wave + 8 * jj) * 1024));
    }
    WAIT_VM(0);
    BARRIER();
    const int tl = wave & 3, ks0 = (wave >> 2) * 10;
    const u32 a = lds0 + (32 * ks0 + 8 * g + q) * 128 + (((2 * tl + (p >> 1)) ^ (q << 1)) * 16) + 8 * (p & 1);
    bf16x8 fr[10];
    f32x4 cs = {0.f, 0.f, 0.f, 0.f};
#pragma unroll
    for (int k = 0; k < 10; ++k) { fr[k] = tr_frag(a + k * 4096, a + k * 4096 + 512); cs = mfma16(ONES_BF16, fr[k], cs); }
    WAIT_LGKM0();
    BARRIER();
    if (lane < 16) *LDS(float, lds0 + (wave * 16 + lane) * 4) = cs[0];
    WAIT_LGKM0();
    BARRIER();
    const float cm = (*LDS(float, lds0 + (tl * 16 + (lane & 15)) * 4) + *LDS(float, lds0 + ((tl + 4) * 16 + (lane & 15)) * 4)) * (1.f / D);
    const int f = c0 + 16 * tl, pn = f / 96, fi = (f % 96) / 16;
    const int slot = (fi / 3) * 6 + (wtask < 24 ? 0 : 3) + fi % 3;
    char* dst = A.ws + WS_W1 + (((size_t)pn * 20 + ks0) * 12 + slot) * 1024 + lane * 16;
#pragma unroll
    for (int k = 0; k < 10; ++k) {
      const u32x4 w = __builtin_bit_cast(u32x4, fr[k]);
      u32x4 o;
#pragma unroll
      for (int e = 0; e < 4; ++e) o[e] = pack_bf16(lo_bf(w[e]) - cm, hi_bf(w[e]) - cm);
      st16d(dst + (size_t)k * 12288, o);
    }
  } else {
    const int ks = wtask - 48;
#pragma unroll
    for (int jj = 0; jj < 5; ++jj) {
      const int P = 64 * (wave + 8 * jj) + lane, row = P / 80, chk = P % 80;
      glds16((const char*)A.Wd + (size_t)(32 * ks + row) * 1280 + chk * 16, UNI(lds0 + (wave + 8 * jj) * 1024));
    }
    WAIT_VM(0);
    BARRIER();
#pragma unroll
    for (int jj = 0; jj < 5; ++jj) {
      const int fr = wave + 8 * jj;
      const u32 a = lds0 + (8 * g + q) * 1280 + (16 * fr + 4 * p) * 2;
      st16d(A.ws + WS_WD + (((size_t)(fr / 5) * 48 + ks) * 5 + fr % 5) * 1024 + lane * 16, tr_read2(a, a + 4 * 1280));
    }
  }
  WAIT_VM(0);
  BARRIER();
  if (tid == 0) set_flag((u32*)(A.ws + WS_FL) + FL_W + wtask * 32, *LDS(u32, lds0 + OFF_TAG));
}

struct cplx { f32x2 v, r; };
__device__ __forceinline__ cplx mk(f32x2 v) { return cplx{v, (f32x2){-v.y, v.x}}; }
__device__ __forceinline__ f32x2 cmul(f32x2 a, const cplx& w) {
  const f32x2 t = (f32x2){a.x, a.x} * w.v;
  return __builtin_elementwise_fma((f32x2){a.y, a.y}, w.r, t);
}
__device__ __forceinline__ f32x2 cexp(float zr, float zi, float l, float sgn) {
  const float m = __builtin_amdgcn_exp2f(zr * l * 1.44269504088896341f);
  const float a = __builtin_amdgcn_fractf(zi * l * 0.15915494309189535f);
  return (f32x2){m * __builtin_amdgcn_cosf(a), sgn * m * __builtin_amdgcn_sinf(a)};
}
__device__ __forceinline__ u32 pkh(f32x2 v) { return __builtin_bit_cast(u32, __builtin_convertvector(v, f16x2)); }
__device__ __forceinline__ u32 cmulh(u32 a, u32 b) {
  u32 t, r;
  asm("v_pk_mul_f16 %0, %1, %2 op_sel:[0,0] op_sel_hi:[0,1]" : "=v"(t) : "v"(a), "v"(b));
  asm("v_pk_fma_f16 %0, %1, %2, %3 op_sel:[1,1,0] op_sel_hi:[1,0,1] neg_lo:[0,1,0]" : "=v"(r) : "v"(a), "v"(b), "v"(t));
  return r;
}
#define SET_M0(base) asm volatile("s_mov_b32 m0, %0\n\ts_nop 0" ::"s"(base) : "memory")
#define WR4(row, a, b, c, d)                                                                                    \
  asm volatile("ds_write_addtid_b32 %0 offset:%4\n\tds_write_addtid_b32 %1 offset:%4+272\n\t"                    \
               "ds_write_addtid_b32 %2 offset:%4+544\n\tds_write_addtid_b32 %3 offset:%4+816"                    \
               ::"v"(a), "v"(b), "v"(c), "v"(d), "n"((row) * C_ROW) : "memory")

__device__ __forceinline__ void conv_role(const Args& A, int grp, u32 lds0, int tid, int lane, int wave, u32 tag) {
  const int ch = (wave >> 2) * 2 + ((wave >> 1) & 1), hf = wave & 1, d0 = grp * 4, d = d0 + ch;
  const u32 sb = UNI(lds0 + ch * C_SCR);
  const int r32 = lane & 31, h32 = lane >> 5;
  const int j = lane & 15, g = lane >> 4;
  {
    const u64 q0 = (u64)A.Are, q1 = (u64)A.Aim, q2 = (u64)A.Bre, q3 = (u64)A.Bim, q4 = (u64)A.Cre, q5 = (u64)A.Cim;
#pragma unroll
    for (int rep = 0; rep < 2; ++rep) {
      if (rep == 1 && wave != 0) break;
      const int i = rep ? 8 : wave;
      const int b = 1024 * i + 16 * lane, a = b / 1536, o = b - a * 1536;
      u64 src = +q0;
      src = a == 1 ? +q1 : src; src = a == 2 ? +q2 : src; src = a == 3 ? +q3 : src; src = a == 4 ? +q4 : src; src = a == 5 ? +q5 : src;
      glds16((const char*)(src + (u64)d0 * 384 + o), UNI(lds0 + F_OFF_P + i * 1024));
    }
  }
  const u32 wdt = ((const u32*)A.logdt)[d >> 1], wds = ((const u32*)A.Dskip)[d >> 1];
  const float dt = rbf(__expf((d & 1) ? hi_bf(wdt) : lo_bf(wdt)));
  WAIT_VM(0);
  BARRIER();
  float zr[3], zi[3];
  const u32 pl = lds0 + F_OFF_P + ch * 384 + lane * 2;
#pragma unroll
  for (int r = 0; r < 3; ++r) {
    zr[r] = dt * bf2f(*LDS(u16, pl + r * 128));
    zi[r] = dt * bf2f(*LDS(u16, pl + 1536 + r * 128));
  }
  u32 an[3][11];
  if (hf == 0) {
    f32x2 cb[3];
    float bound = 0.f;
#pragma unroll
    for (int r = 0; r < 3; ++r) {
      const float br = dt * bf2f(*LDS(u16, pl + 2 * 1536 + r * 128)), bi = dt * bf2f(*LDS(u16, pl + 3 * 1536 + r * 128));
      const float cr = bf2f(*LDS(u16, pl + 4 * 1536 + r * 128)), ci = bf2f(*LDS(u16, pl + 5 * 1536 + r * 128));
      cb[r] = (f32x2){cr * br - ci * bi, -(cr * bi + ci * br)};
      const float grow = __builtin_amdgcn_exp2f(fmaxf(zr[r], 0.f) * (2064.f * 1.44269504f));
      bound = fmaxf(bound, (fabsf(cb[r].x) + fabsf(cb[r].y)) * grow);
    }
    bound = wave_max(bound);
    int ex = (int)((__float_as_uint(bound) >> 23) & 255);
    ex = ex < 20 ? 20 : (ex > 240 ? 240 : ex);
    const float scale = __uint_as_float((u32)(267 - ex) << 23);
    if (lane == 0) *LDS(float, lds0 + F_OFF_S + ch * 4) = __uint_as_float((u32)(ex - 5) << 23);
#pragma unroll
    for (int r = 0; r < 3; ++r) {
      const cplx w1 = mk(cexp(zr[r], zi[r], 1.f, -1.f));
      const cplx w512 = mk(cexp(zr[r], zi[r], 512.f, -1.f));
      const cplx w2 = mk(cmul(w1.v, w1));
      const f32x2 w3 = cmul(w2.v, w1);
      const cplx w4 = mk(cmul(w2.v, w2));
      const f32x2 w1024 = cmul(w512.v, w512), w1536 = cmul(w1024, w512);
      const f32x2 P0 = cb[r] * scale, P4 = cmul(P0, w4), P8 = cmul(P4, w4), P12 = cmul(P8, w4), P16 = cmul(P12, w4);
      an[r][0] = pkh(P0); an[r][1] = pkh(P4); an[r][2] = pkh(P8); an[r][3] = pkh(P12); an[r][4] = pkh(P16);
      an[r][5] = pkh(w1.v); an[r][6] = pkh(w2.v); an[r][7] = pkh(w3);
      an[r][8] = pkh(w512.v); an[r][9] = pkh(w1024); an[r][10] = pkh(w1536);
    }
  } else {
#pragma unroll
    for (int r = 0; r < 3; ++r) {
      const cplx w16 = mk(cexp(zr[r], zi[r], 16.f, 1.f));
      const cplx w128 = mk(cexp(zr[r], zi[r], 128.f, 1.f));
      const cplx w32 = mk(cmul(w16.v, w16));
      const f32x2 w48 = cmul(w32.v, w16);
      const f32x2 w64 = cmul(w32.v, w32);
      const f32x2 w192 = cmul(w64, w128), w256 = cmul(w128.v, w128);
      const f32x2 w320 = cmul(w192, w128), w384 = cmul(w256, w128), w448 = cmul(w320, w128);
      an[r][0] = pkh(w64); an[r][1] = pkh(w128.v); an[r][2] = pkh(w192); an[r][3] = pkh(w256);
      an[r][4] = pkh(w320); an[r][5] = pkh(w384); an[r][6] = pkh(w448);
      an[r][7] = pkh(w16.v); an[r][8] = pkh(w32.v); an[r][9] = pkh(w48); an[r][10] = 0x00003c00u;
    }
  }
  u32 arow;
  {
    const int hr = (r32 >> 2) & 1, rho = (r32 & 3) + 4 * (r32 >> 3);
    const int row = rho <= 8 ? rho + 8 * hr : rho <= 11 ? rho + 8 + 2 * hr : rho <= 13 ? rho + 10 + hr : rho + 11 + hr;
    arow = sb + row * C_ROW + h32 * 16 + hf * 128;
  }
  const u32 brow = sb + (28 + r32) * C_ROW + h32 * 16 + hf * 128;
  f32x16 kacc;
#pragma unroll
  for (int i = 0; i < 16; ++i) kacc[i] = 0.f;
  SET_M0(sb);
#pragma unroll
  for (int r = 0; r < 3; ++r) {
    u32 gv[2][4];
    auto grp4 = [&](int gi, u32 (&o)[4]) {
      const u32* a = an[r];
      if (hf == 0) {
        if (gi < 4) { o[0] = a[gi]; o[1] = cmulh(a[gi], a[5]); o[2] = cmulh(a[gi], a[6]); o[3] = cmulh(a[gi], a[7]); }
        else if (gi == 4) { o[0] = a[4]; o[1] = cmulh(a[0], a[8]); o[2] = cmulh(a[1], a[8]); o[3] = cmulh(a[2], a[8]); }
        else if (gi == 5) { o[0] = cmulh(a[3], a[8]); o[1] = cmulh(a[4], a[8]); o[2] = cmulh(a[0], a[9]); o[3] = cmulh(a[2], a[9]); }
        else if (gi == 6) { o[0] = cmulh(a[4], a[9]); o[1] = cmulh(a[0], a[10]); o[2] = cmulh(a[2], a[10]); o[3] = cmulh(a[4], a[10]); }
      } else {
        if (gi == 0) { o[0] = a[10]; o[1] = a[7]; o[2] = a[8]; o[3] = a[9]; }
        else { o[0] = a[gi - 1]; o[1] = cmulh(a[gi - 1], a[7]); o[2] = cmulh(a[gi - 1], a[8]); o[3] = cmulh(a[gi - 1], a[9]); }
      }
    };
    if (hf == 0) {
      grp4(0, gv[0]);
#pragma unroll
      for (int gi = 0; gi < 7; ++gi) {
        if (gi < 6) grp4(gi + 1, gv[(gi + 1) & 1]);
        SB();
        WR4(4 * gi, gv[gi & 1][0], gv[gi & 1][1], gv[gi & 1][2], gv[gi & 1][3]);
        SB();
      }
    } else {
      grp4(0, gv[0]);
#pragma unroll
      for (int gi = 0; gi < 8; ++gi) {
        if (gi < 7) grp4(gi + 1, gv[(gi + 1) & 1]);
        SB();
        WR4(28 + 4 * gi, gv[gi & 1][0], gv[gi & 1][1], gv[gi & 1][2], gv[gi & 1][3]);
        SB();
      }
    }
    WAIT_LGKM0();
    BARRIER();
    f16x8 fa[4], fb[4];
#pragma unroll
    for (int k = 0; k < 4; ++k) { fa[k] = *LDS(f16x8, arow + k * 32); fb[k] = *LDS(f16x8, brow + k * 32); }
    WAIT_LGKM0();
    BARRIER();
#pragma unroll
    for (int k = 0; k < 4; ++k) kacc = __builtin_amdgcn_mfma_f32_32x32x16_f16(fa[k], fb[k], kacc, 0, 0, 0);
  }
  {
    const u32 ex = sb + hf * 4096 + lane * 16, exr = sb + (hf ^ 1) * 4096 + lane * 16;
    if (hf == 0) {
      *LDS(f32x4, ex) = (f32x4){kacc[12], kacc[13], kacc[14], kacc[15]};
    } else {
      *LDS(f32x4, ex) = (f32x4){kacc[0], kacc[1], kacc[2], kacc[3]};
      *LDS(f32x4, ex + 1024) = (f32x4){kacc[4], kacc[5], kacc[6], kacc[7]};
      *LDS(f32x4, ex + 2048) = (f32x4){kacc[8], kacc[9], kacc[10], kacc[11]};
    }
    WAIT_LGKM0();
    BARRIER();
    const u32 kb = lds0 + F_OFF_K + ch * C_KCH + 4080 - 32 * r32 - 16 * h32;
    auto put = [&](int base, float e0, float e1, float e2, float e3, float e4, float e5, float e6, float e7) {
      *LDS(u32x4, kb - 2 * base) = (u32x4){pack_f16(e7, e6), pack_f16(e5, e4), pack_f16(e3, e2), pack_f16(e1, e0)};
    };
    if (hf == 0) {
      const f32x4 o0 = *LDS(f32x4, exr), o1 = *LDS(f32x4, exr + 1024), o2 = *LDS(f32x4, exr + 2048);
      float v[12];
#pragma unroll
      for (int i = 0; i < 4; ++i) {
        v[i] = (kacc[i] + o0[i]) * (1.f / 256.f); v[4 + i] = (kacc[4 + i] + o1[i]) * (1.f / 256.f); v[8 + i] = (kacc[8 + i] + o2[i]) * (1.f / 256.f);
      }
      const bool far = r32 >= 16;
      put(0, v[0], far ? v[0] : v[1], v[2], far ? v[4] : v[3], v[4], far ? v[4] : v[5], v[6], far ? v[8] : v[7]);
      put(512, v[9], v[9], v[9], v[10], v[10], v[10], v[11], v[11]);
    } else {
      const f32x4 o = *LDS(f32x4, exr);
      const float v12 = (kacc[12] + o[0]) * (1.f / 256.f), v13 = (kacc[13] + o[1]) * (1.f / 256.f),
                  v14 = (kacc[14] + o[2]) * (1.f / 256.f), v15 = (kacc[15] + o[3]) * (1.f / 256.f);
      put(1024, v12, v12, v12, v12, h32 ? v13 : v12, v13, v13, v13);
      put(1536, v14, v14, v14, v14, h32 ? v15 : v14, v15, v15, v15);
    }
  }
  if (hf == 1 && lane < 32) *LDS(u32x4, lds0 + F_OFF_K + ch * C_KCH + 4096 + lane * 16) = (u32x4){0u, 0u, 0u, 0u};
  if (wave == 0 && lane < 10) *LDS(u32, lds0 + F_OFF_X + lane * 4) = 0u;
  if (wave == 7) wait_flags<1>((const u32*)(A.ws + WS_FL) + FL_U, 64, tag, lane);
  WAIT_LGKM0();
  BARRIER();
  {
    const char* up = A.ws + WS_UT + (size_t)d * UT_ROW + lane * 16 + hf * 2;
    const u32 dst = UNI(lds0 + F_OFF_U + ch * C_UCH + hf * C_UCP);
#pragma unroll
    for (int k = 0; k < 4; ++k) glds16d(up + k * 1024, dst + k * 1024);
    if (lane < 2) glds16d(up + 4096, dst + 4096);
    glds16d(A.ws + WS_LN + wave * 2048 + lane * 16, UNI(lds0 + F_OFF_L + wave * 2048));
    glds16d(A.ws + WS_LN + wave * 2048 + 1024 + lane * 16, UNI(lds0 + F_OFF_L + wave * 2048 + 1024));
  }
  WAIT_VM(0);
  if (lane < 8) {
    const u32 zb = lds0 + F_OFF_U + ch * C_UCH + hf * C_UCP;
    if (lane < 7 || hf == 0) *LDS(u32, zb + lane * 4) = 0u; else *LDS(u16, zb + 28) = (u16)0;
  }
  WAIT_LGKM0();
  BARRIER();
  const int cc = (1 + j) & 1;
  const u32 aa = lds0 + F_OFF_U + ch * C_UCH + cc * C_UCP + 2 * (1 + j + 8 * g - cc) + hf * 256;
  const u32 ba = lds0 + F_OFF_K + ch * C_KCH + 32 * j + 16 * g + hf * 256;
  f32x4 acc[8];
#pragma unroll
  for (int i = 0; i < 8; ++i) acc[i] = (f32x4){0.f, 0.f, 0.f, 0.f};
  if (wave < 4) __builtin_amdgcn_s_setprio(2);
  {
    f16x8 Af[2][8], Bf[2][8];
    u32 am[4];
#pragma unroll
    for (int k = 0; k < 4; ++k) { am[k] = aa + 1024 * k; asm volatile("" : "+v"(am[k])); }
    auto rd = [&](int set, int r) {
#pragma unroll
      for (int m = 0; m < 8; ++m) {
        const LAS u32* ap = LDS(const u32, am[m >> 1] + 64 * r + 512 * (m & 1));
        Af[set][m] = __builtin_bit_cast(f16x8, (u32x4){ap[0], ap[1], ap[2], ap[3]});
        Bf[set][m] = *LDS(f16x8, ba + 64 * r + 512 * (7 - m));
      }
    };
    auto mm = [&](int set) {
#pragma unroll
      for (int sdx = 0; sdx < 8; ++sdx)
#pragma unroll
        for (int m = 0; m <= sdx; ++m) acc[sdx] = __builtin_amdgcn_mfma_f32_16x16x32_f16(Af[set][m], Bf[set][sdx - m], acc[sdx], 0, 0, 0);
    };
#define C_SCHED() do { _Pragma("unroll") for (int k_ = 0; k_ < 24; ++k_) { SG(0x008, 1); SG(0x100, 1); } SG(0x008, 12); } while (0)
    rd(0, 0);
    SB(); rd(1, 1); mm(0); C_SCHED(); SB();
    rd(0, 2); mm(1); C_SCHED(); SB();
    rd(1, 3); mm(0); C_SCHED(); SB();
    mm(1);
  }
  {
    const u32 ex = lds0 + wave * 4096 + lane * 16, exr = lds0 + (wave ^ 1) * 4096 + lane * 16;
    if (hf == 0) {
#pragma unroll
      for (int i = 0; i < 4; ++i) *LDS(f32x4, ex + i * 1024) = acc[4 + i];
    } else {
#pragma unroll
      for (int i = 0; i < 4; ++i) *LDS(f32x4, ex + i * 1024) = acc[i];
    }
    WAIT_LGKM0();
    if (lane == 0) *LDS(volatile u32, lds0 + F_OFF_X + wave * 4) = 1u;
    const float it = *LDS(float, lds0 + F_OFF_S + ch * 4);
    const float dsk = (d & 1) ? hi_bf(wds) : lo_bf(wds);
    u32x2 dv[4], xv[4];
#pragma unroll
    for (int i = 0; i < 4; ++i) {
      const int t = 16 * (16 * (4 * hf + i) + 15 - j) + 4 * g;
      const u32x2 uw = *LDS(u32x2, lds0 + F_OFF_U + ch * C_UCH + 2 * (16 + t));
      const u32 w0 = uw[0], w1 = uw[1];
      const f16x2 u01 = __builtin_bit_cast(f16x2, w0), u23 = __builtin_bit_cast(f16x2, w1);
      const float uf[4] = {(float)u01[0], (float)u01[1], (float)u23[0], (float)u23[1]};
      dv[i] = (u32x2){pack_bf16(uf[0] * dsk, uf[1] * dsk), pack_bf16(uf[2] * dsk, uf[3] * dsk)};
      const f32x4 l01 = *LDS(f32x4, lds0 + F_OFF_L + t * 8), l23 = *LDS(f32x4, lds0 + F_OFF_L + t * 8 + 16);
      xv[i] = (u32x2){pack_bf16(fmaf(uf[0], l01[0], l01[1]), fmaf(uf[1], l01[2], l01[3])), pack_bf16(fmaf(uf[2], l23[0], l23[1]), fmaf(uf[3], l23[2], l23[3]))};
    }
#pragma unroll
    for (int i = 0; i < 4; ++i) asm volatile("" : "+v"(dv[i]), "+v"(xv[i]));
    while (*LDS(volatile u32, lds0 + F_OFF_X + (wave ^ 1) * 4) == 0u) __builtin_amdgcn_s_sleep(1);
    asm volatile("" ::: "memory");
    f32x4 y[4];
#pragma unroll
    for (int i = 0; i < 4; ++i) y[i] = *LDS(f32x4, exr + i * 1024);
    SB();
    if (hf) {
#pragma unroll
      for (int i = 0; i < 4; ++i) y[i] += acc[4 + i];
      asm volatile("" ::: "memory");
    } else {
#pragma unroll
      for (int i = 0; i < 4; ++i) y[i] += acc[i];
    }
#pragma unroll
    for (int i = 0; i < 4; ++i) y[i] *= it;
    u32x2 w[4];
#pragma unroll
    for (int i = 0; i < 4; ++i) w[i] = (u32x2){pack_bf16(y[i][0], y[i][1]), pack_bf16(y[i][2], y[i][3])};
#pragma unroll
    for (int i = 0; i < 4; ++i)
      w[i] = (u32x2){pack_bf16(lo_bf(w[i][0]) + lo_bf(dv[i][0]), hi_bf(w[i][0]) + hi_bf(dv[i][0])), pack_bf16(lo_bf(w[i][1]) + lo_bf(dv[i][1]), hi_bf(w[i][1]) + hi_bf(dv[i][1]))};
    u32x2 hv[4];
#pragma unroll
    for (int i = 0; i < 4; ++i)
      hv[i] = (u32x2){pack_bf16(lo_bf(xv[i][0]) + lo_bf(w[i][0]), hi_bf(xv[i][0]) + hi_bf(w[i][0])), pack_bf16(lo_bf(xv[i][1]) + lo_bf(w[i][1]), hi_bf(xv[i][1]) + hi_bf(w[i][1]))};
    char* hrow = A.ws + WS_HT + (size_t)ht_row(d) * 4096;
#pragma unroll
    for (int i = 0; i < 4; i += 2) st16d(hrow + (16 * (16 * (4 * hf + i + (g & 1)) + 15 - j) + 8 * (g >> 1)) * 2, widen16(hv[i], hv[i + 1]));
  }
  __builtin_amdgcn_s_setprio(0);
  if (wave < 4) {
    WAIT_VM(0);
    if (lane == 0 && __hip_atomic_fetch_add(LDS(u32, lds0 + F_OFF_X + 32), 1u, __ATOMIC_RELAXED, __HIP_MEMORY_SCOPE_WORKGROUP) == 3u)
      set_flag((u32*)(A.ws + WS_FL) + FL_HE + grp * 32, *LDS(u32, lds0 + OFF_TAG));
  }
}

__device__ __forceinline__ void gemm1_loader(const Args& A, u32 lds0, int lane, int wave, u32 tag) {
  const bool conv_wg = (blockIdx.x >> 3) >= 12;
  const int xcd = blockIdx.x & 7, idx = blockIdx.x >> 3;
  const int pm = (xcd & 1) * 8 + (idx & 7), pn = (xcd >> 1) * 4 + (idx >> 3);
  const int t0 = pm * 128;
  const u32* flags = (const u32*)(A.ws + WS_FL);
    const int lw = wave - 4;
    const int k0 = 5 * (pn & 1);
    int kH = 0, kW = 0;
    const int row = 8 * lw + (lane >> 4);
    const int chk = (lane & 15) ^ (((row & 3) << 1) | (((row >> 3) & 1) << 3));
    const char* srcH = A.ws + WS_HT + (size_t)(row + 32 * k0) * 4096 + t0 * 2 + chk * 16;
    const char* srcW = A.ws + WS_W1 + (size_t)pn * 245760 + (size_t)k0 * 12288 + lw * 3072 + lane * 16;
    auto kstep = [&](int p) { return (p / 10) * 10 + (p % 10 + k0) % 10; };
    const u32 dH = lds0 + lw * 2048, dW = lds0 + 8192 + lw * 3072;
    auto issueH = [&](u32 so) {
      glds16d(srcH, dH + so);
      glds16d(srcH + 4 * 4096, dH + so + 1024);
      srcH += (ptrdiff_t)(kstep(kH + 1) - kstep(kH)) * (32 * 4096); ++kH;
    };
    auto issueW = [&](u32 so) {
      glds16d(srcW, dW + so);
      glds16d(srcW + 1024, dW + so + 1024);
      glds16d(srcW + 2048, dW + so + 2048);
      srcW += (ptrdiff_t)(kstep(kW + 1) - kstep(kW)) * 12288; ++kW;
    };
    u32 so = 0;
    auto issue = [&]() { issueH(so); issueW(so); so = so == (G1_NS - 1) * G1_STG ? 0 : so + G1_STG; };
    if (conv_wg) {
      bool seen = false;
      if (wave == 4) seen = poll_once<4>(flags + FL_W, 256, tag, lane);
      WAIT_VM(0);
      if (lane == 0 && __hip_atomic_fetch_add(LDS(u32, lds0 + OFF_TAG + 4), 1u, __ATOMIC_RELAXED, __HIP_MEMORY_SCOPE_WORKGROUP) == 3u)
        set_flag((u32*)(A.ws + WS_FL) + FL_HL + (xcd * 20 + idx - 12) * 32, tag);
      if (wave == 4 && !seen) wait_flags<4>(flags + FL_W, 256, tag, lane);
      BARRIER();
      BARRIER();
      issue(); issue();
      WAIT_VM(5); BARRIER();
      issue(); issue();
      WAIT_VM(10); BARRIER();
      issue(); issue();
      WAIT_VM(15); BARRIER();
      issue(); issue();
      WAIT_VM(20); BARRIER();
      issue();
      WAIT_VM(20); BARRIER();
      issue();
      if (wave == 4) wait_flags<3>(flags + FL_HL, 160, tag, lane);
      WAIT_VM(20); BARRIER();
    } else {
      if (wave == 4) wait_flags<2>(flags + FL_W, 96, tag, lane);
      BARRIER();
#pragma unroll
      for (int s = 0; s < G1_NS; ++s) issueW(s * G1_STG);
      if (wave == 4) wait_flags<3>(flags + FL_HE, 160, tag, lane);
      BARRIER();
      issueH(0); issueH(G1_STG); issueH(2 * G1_STG);
      WAIT_VM(4); BARRIER();
      issueH(3 * G1_STG); issueH(4 * G1_STG);
      WAIT_VM(6); BARRIER();
      issueH(5 * G1_STG); issueH(6 * G1_STG);
      WAIT_VM(8); BARRIER();
      issue(); WAIT_VM(11); BARRIER();
      issue(); WAIT_VM(14); BARRIER();
      issue();
      if (wave == 4) wait_flags<3>(flags + FL_HL, 160, tag, lane);
      WAIT_VM(17); BARRIER();
    }
    for (int i = 5; i < G1_NK - 5; ++i) {
      issue();
      WAIT_VM(20); BARRIER();
    }
    WAIT_VM(15); BARRIER(); WAIT_VM(10); BARRIER(); WAIT_VM(5); BARRIER(); WAIT_VM(0); BARRIER();
}

__device__ __forceinline__ void gemm1_compute(const Args& A, u32 lds0, int lane, int wave) {
  const int xcd = blockIdx.x & 7, idx = blockIdx.x >> 3;
  const int pm = (xcd & 1) * 8 + (idx & 7), pn = (xcd >> 1) * 4 + (idx >> 3);
  const int t0 = pm * 128;
  const int wt = wave & 1, wf = wave >> 1;
  const int g = lane >> 4, q = (lane & 15) >> 2, p = lane & 3;
  const int rot = (pn >> 1) & 3;
  const u32 hrow = (8 * g + q) * 256 + 8 * (p & 1);
  const u32 hx = q | ((g & 1) << 2);
  u32 hc[4];
#pragma unroll
  for (int j = 0; j < 4; ++j) hc[j] = lds0 + hrow + ((((u32)(4 * wt + ((j + rot) & 3)) ^ hx) << 1) | (p >> 1)) * 16;
  u32 wc = lds0 + 8192 + wf * 6144 + lane * 16, so = 0;
  f32x4 acc[6][4];
#pragma unroll
  for (int i = 0; i < 6; ++i)
#pragma unroll
    for (int j = 0; j < 4; ++j) acc[i][j] = (f32x4){0.f, 0.f, 0.f, 0.f};
  f32x4 st1 = {0.f, 0.f, 0.f, 0.f}, st2 = st1;
  bf16x8 hf[2][4], wfr[2][6];
  auto rd = [&](int set) {
    wfr[set][0] = *LDS(bf16x8, wc);
#pragma unroll
    for (int j = 0; j < 4; ++j) hf[set][j] = tr_frag(hc[j], hc[j] + 1024);
#pragma unroll
    for (int i = 1; i < 6; ++i) wfr[set][i] = *LDS(bf16x8, wc + i * 1024);
  };
  auto adv = [&]() {
    u32 dl = so == (G1_NS - 1) * G1_STG ? (u32)(-(G1_NS - 1) * G1_STG) : (u32)G1_STG;
    so += dl;
    asm volatile("" : "+s"(dl));
#pragma unroll
    for (int j = 0; j < 4; ++j) hc[j] += dl;
    wc += dl;
  };
  auto mma = [&](int set) {
#pragma unroll
    for (int i = 0; i < 6; ++i)
#pragma unroll
      for (int j = 0; j < 4; ++j) acc[i][j] = mfma16(wfr[set][i], hf[set][j], acc[i][j]);
  };
  auto stat = [&](int set) {
    st1 = mfma16(ONES_BF16, hf[set][0], st1);
    st2 = mfma16(hf[set][0], hf[set][0], st2);
  };
#define G1_SCHED(extra) do { \
    SG(0x008, 1); SG(0x100, 1); SG(0x008, 1); SG(0x100, 1); SG(0x008, 1); SG(0x100, 1); SG(0x008, 1); SG(0x100, 1); \
    SG(0x008, 1); SG(0x100, 1); SG(0x008, 1); SG(0x100, 1); SG(0x008, 1); SG(0x100, 1); SG(0x008, 1); SG(0x100, 1); \
    SG(0x008, 1); SG(0x100, 1); SG(0x008, 2); SG(0x100, 1); SG(0x008, 2); SG(0x100, 1); SG(0x008, 2); SG(0x100, 1); \
    SG(0x008, 2); SG(0x100, 1); SG(0x008, 1); SG(0x100, 1); SG(0x008, 1); SG(0x002, 1); SG(0x008, 1); SG(0x002, 1); \
    SG(0x008, 1); SG(0x002, 1); SG(0x008, 1); SG(0x002, 1); SG(0x008, 1); SG(0x002, 1); SG(0x008, 1 + (extra)); } while (0)
  BARRIER();
  BARRIER();
  BARRIER();
  rd(0); adv();
#pragma unroll
  for (int i = 1; i < G1_NK - 1; i += 2) {
    BARRIER();
    SB(); rd(1); mma(0); adv();
    if ((i - 1) % 10 < 5) { stat(0); G1_SCHED(2); } else { G1_SCHED(0); }
    SB();
    BARRIER();
    SB(); rd(0); mma(1); adv();
    if (i % 10 < 5) { stat(1); G1_SCHED(2); } else { G1_SCHED(0); }
    SB();
  }
  BARRIER();
  SB(); rd(1); mma(0); G1_SCHED(0); SB();
  WAIT_LGKM0();
  BARRIER();
#pragma unroll
  for (int i = 0; i < 3; ++i)
#pragma unroll
    for (int j = 0; j < 4; ++j) {
      acc[i][j] = mfma16(wfr[1][i], hf[1][j], acc[i][j]);
      acc[3 + i][j] = mfma16(wfr[1][3 + i], hf[1][j], acc[3 + i][j]);
    }
  {
    char* ob = A.ws + WS_HM + ((size_t)(pm * 8 + 4 * wt) * 48 + 3 * pn) * 1024 + (lane & 15) * 16 + (g >> 1) * 256;
#pragma unroll
    for (int j = 0; j < 4; j += 2) {
      char* oj = ob + (size_t)((j + (g & 1) + rot) & 3) * 49152;
#pragma unroll
      for (int i = 0; i < 3; ++i) {
        u32x2 o[2];
#pragma unroll
        for (int jj = 0; jj < 2; ++jj) {
          const f32x4 gv = acc[i][j + jj], uv = acc[3 + i][j + jj];
          o[jj] = (u32x2){pack_bf16(fmaxf(gv[0], 0.f) * uv[0], fmaxf(gv[1], 0.f) * uv[1]), pack_bf16(fmaxf(gv[2], 0.f) * uv[2], fmaxf(gv[3], 0.f) * uv[3])};
        }
        const int E = 6 * wf + 2 * i;
        st16d(oj + (E >> 2) * 1024 + (E & 3) * 256, widen16(o[0], o[1]));
      }
    }
  }
  if (wf == 0 && pn < 8 && g == ((lane & 15) >> 2))
    __hip_atomic_store((gu64*)((u64*)(A.ws + WS_PT) + (size_t)(pn & 1) * L + t0 + (4 * wt + rot) * 16 + (lane & 15)),
                       ((u64)__float_as_uint(diag16(st2, lane)) << 32) | __float_as_uint(st1[0]), RLX_AGENT);
  WAIT_VM(0);
}

__device__ __forceinline__ void gemm2_loader(const Args& A, u32 lds0, int lane, int wave, u32 tag) {
  const int xcd = blockIdx.x & 7, idx = blockIdx.x >> 3;
  const int pm = (xcd & 3) * 8 + (idx & 7), pn = (xcd >> 2) * 4 + (idx >> 3);
  const int t0 = pm * 64, d0 = pn * 80;
  u32* flags = (u32*)(A.ws + WS_FL);
    const int lw = wave - 4;
    const char* srcP = A.ws + WS_HM + (size_t)(pm * 4 + lw) * 49152 + lane * 16;
    const char* srcW = A.ws + WS_WD + (size_t)pn * 245760 + lw * 5120 + lane * 16;
    const u32 dP = lds0 + lw * 4096, dW = lds0 + 16384 + lw * 5120;
    auto issueP = [&](u32 so) {
      glds16d(srcP, dP + so); glds16d(srcP + 1024, dP + so + 1024); glds16d(srcP + 2048, dP + so + 2048); glds16d(srcP + 3072, dP + so + 3072);
      srcP += 4096;
    };
    auto issueW = [&](u32 so) {
      glds16d(srcW, dW + so); glds16d(srcW + 1024, dW + so + 1024); glds16d(srcW + 2048, dW + so + 2048);
      glds16d(srcW + 3072, dW + so + 3072); glds16d(srcW + 4096, dW + so + 4096);
      srcW += 20480;
    };
    BARRIER();
    issueW(0); issueW(G2_STG); issueW(2 * G2_STG); issueW(3 * G2_STG);
#pragma unroll
    for (int k = 0; k < 3; ++k) {
      const int jj = lw + 4 * k < 10 ? lw + 4 * k : lw + 4;
      glds16d(A.ws + WS_HT + (size_t)ht_row(d0 + 8 * jj + (lane >> 3)) * 4096 + t0 * 2 + (lane & 7) * 16, UNI(lds0 + G2_OFF_H + jj * 1024));
    }
    BARRIER();
    if (wave == 4) wait_flags<1>(flags + FL_P + (pm >> 1) * 16 * 32, 16, tag, lane);
    BARRIER();
    issueP(0); issueP(G2_STG);
    WAIT_VM(4); BARRIER();
    issueP(2 * G2_STG); issueP(3 * G2_STG);
    WAIT_VM(8); BARRIER();
    u32 so = 0;
    auto issue = [&]() { issueP(so); issueW(so); so = so == 3 * G2_STG ? 0 : so + G2_STG; };
    issue(); WAIT_VM(13); BARRIER();
    for (int i = 2; i < G2_NK - 3; ++i) {
      issue();
      WAIT_VM(18); BARRIER();
    }
    WAIT_VM(9); BARRIER();
    WAIT_VM(0); BARRIER();
    BARRIER();
    BARRIER();
}

__device__ __forceinline__ void gemm2_compute(const Args& A, u32 lds0, int tid, int lane, int wave, u32 tag) {
  const int xcd = blockIdx.x & 7, idx = blockIdx.x >> 3;
  const int pm = (xcd & 3) * 8 + (idx & 7), pn = (xcd >> 2) * 4 + (idx >> 3);
  const int t0 = pm * 64, d0 = pn * 80;
  u32* flags = (u32*)(A.ws + WS_FL);
  BARRIER();
  if (tid == 0) {
    const int pm1 = (xcd & 1) * 8 + (idx & 7), pn1 = (xcd >> 1) * 4 + (idx >> 3);
    set_flag(flags + FL_P + (pm1 * 16 + pn1) * 32, tag);
  }
  const int wt = wave & 1, wk = wave >> 1;
  u32 pc = lds0 + (wt * 2) * 4096 + wk * 2048 + lane * 16, wc = lds0 + 16384 + wk * 2 * 5120 + lane * 16, so = 0;
  f32x4 acc[5][2];
#pragma unroll
  for (int i = 0; i < 5; ++i) { acc[i][0] = (f32x4){0.f, 0.f, 0.f, 0.f}; acc[i][1] = acc[i][0]; }
  bf16x8 pf[2][2][2], wf[2][2][5];
  auto rd = [&](int set) {
#pragma unroll
    for (int ks = 0; ks < 2; ++ks) {
      wf[set][ks][0] = *LDS(bf16x8, wc + ks * 5120);
#pragma unroll
      for (int j = 0; j < 2; ++j) pf[set][ks][j] = *LDS(bf16x8, pc + j * 4096 + ks * 1024);
#pragma unroll
      for (int i = 1; i < 5; ++i) wf[set][ks][i] = *LDS(bf16x8, wc + ks * 5120 + i * 1024);
    }
  };
  auto adv = [&]() {
    u32 dl = so == 3 * G2_STG ? (u32)(-3 * G2_STG) : (u32)G2_STG;
    so += dl;
    asm volatile("" : "+s"(dl));
    pc += dl; wc += dl;
  };
  auto mma = [&](int set) {
#pragma unroll
    for (int ks = 0; ks < 2; ++ks)
#pragma unroll
      for (int i = 0; i < 5; ++i)
#pragma unroll
        for (int j = 0; j < 2; ++j) acc[i][j] = mfma16(wf[set][ks][i], pf[set][ks][j], acc[i][j]);
  };
#define G2_SCHED() do { _Pragma("unroll") for (int k_ = 0; k_ < 4; ++k_) { SG(0x008, 1); SG(0x100, 1); SG(0x008, 1); SG(0x100, 1); SG(0x008, 1); SG(0x100, 1); SG(0x008, 1); } \
    SG(0x008, 1); SG(0x100, 1); SG(0x008, 1); SG(0x100, 1); SG(0x008, 2); } while (0)
  BARRIER();
  const int tl = wt * 32 + wk * 16;
  u64 pw[2];
#pragma unroll
  for (int k = 0; k < 2; ++k) pw[k] = __hip_atomic_load((gu64*)((u64*)(A.ws + WS_PT) + (size_t)k * L + t0 + tl + (lane & 15)), RLX_AGENT);
  BARRIER();
  rd(0); adv();
  WAIT_LGKM0();
#pragma unroll
  for (int i = 1; i < G2_NK - 1; i += 2) {
    BARRIER();
    SB(); rd(1); mma(0); adv(); G2_SCHED(); SB();
    WAIT_LGKM0();
    BARRIER();
    SB(); rd(0); mma(1); adv(); G2_SCHED(); SB();
    WAIT_LGKM0();
  }
  BARRIER();
  SB(); rd(1); mma(0); G2_SCHED(); SB();
  WAIT_LGKM0();
  mma(1);
  float rr;
  {
    float s1 = 0.f, s2 = 0.f;
#pragma unroll
    for (int k = 0; k < 2; ++k) { s1 += __uint_as_float((u32)pw[k]); s2 += __uint_as_float((u32)(pw[k] >> 32)); }
    const float mean = s1 * (1.f / D);
    const float var = fmaxf(s2 * (1.f / D) - mean * mean, 0.f);
    const float r = __builtin_amdgcn_rcpf(rbf(__builtin_amdgcn_sqrtf(rbf(rbf(var) + 1e-5f))));
    rr = r * r;
  }
  const int g = lane >> 4, q = (lane & 15) >> 2, p = lane & 3;
  s16x4 hv[5];
#pragma unroll
  for (int i = 0; i < 5; ++i) hv[i] = tr_read(lds0 + G2_OFF_H + (16 * i + 4 * g + q) * 128 + (tl + 4 * p) * 2);
  BARRIER();
  const u32 ex = lds0 + (wave ^ 2) * 5120 + lane * 16, exr = lds0 + wave * 5120 + lane * 16;
#pragma unroll
  for (int i = 0; i < 5; ++i) { if (wk) *LDS(f32x4, ex + i * 1024) = acc[i][0]; else *LDS(f32x4, ex + i * 1024) = acc[i][1]; }
  WAIT_LGKM0();
  BARRIER();
  f32x4 m[5];
#pragma unroll
  for (int i = 0; i < 5; ++i) m[i] = *LDS(f32x4, exr + i * 1024);
  SB();
  if (wk) {
#pragma unroll
    for (int i = 0; i < 5; ++i) m[i] += acc[i][1];
    asm volatile("" ::: "memory");
  } else {
#pragma unroll
    for (int i = 0; i < 5; ++i) m[i] += acc[i][0];
  }
#pragma unroll
  for (int i = 0; i < 5; ++i) m[i] *= rr;
  u32x2 o2[5];
#pragma unroll
  for (int i = 0; i < 5; ++i) o2[i] = (u32x2){pack_bf16(m[i][0], m[i][1]), pack_bf16(m[i][2], m[i][3])};
#pragma unroll
  for (int i = 0; i < 5; ++i)
    o2[i] = (u32x2){pack_bf16(bf2f((u16)hv[i][0]) + lo_bf(o2[i][0]), bf2f((u16)hv[i][1]) + hi_bf(o2[i][0])),
                    pack_bf16(bf2f((u16)hv[i][2]) + lo_bf(o2[i][1]), bf2f((u16)hv[i][3]) + hi_bf(o2[i][1]))};
  char* orow = (char*)A.out + (size_t)(t0 + tl + (lane & 15)) * 1280 + d0 * 2;
  *(u32x4*)(orow + (16 * (0 + (g & 1)) + 8 * (g >> 1)) * 2) = widen16(o2[0], o2[1]);
  *(u32x4*)(orow + (16 * (2 + (g & 1)) + 8 * (g >> 1)) * 2) = widen16(o2[2], o2[3]);
  *(u32x2*)(orow + (64 + 4 * g) * 2) = o2[4];
}

__global__ __launch_bounds__(512, 2) void s4_reglu_block(const Args A) {
  extern __shared__ __attribute__((aligned(16))) char smem[];
  const u32 lds0 = (u32)(size_t)(LAS char*)smem;
  const int tid = threadIdx.x, lane = tid & 63;
  const int wave = __builtin_amdgcn_readfirstlane(tid >> 6);
  const int xcd = blockIdx.x & 7, idx = blockIdx.x >> 3;
  u32* flags = (u32*)(A.ws + WS_FL);
  u32 tag7 = 0;
  if (wave == 7) {
    tag7 = __hip_atomic_load((gu32*)flags, RLX_AGENT) + 1u;
    if (lane == 0) { *LDS(u32, lds0 + OFF_TAG) = tag7; *LDS(u32, lds0 + OFF_TAG + 4) = 0u; set_flag(flags + FL_STARTED + blockIdx.x, tag7); }
  }
  if (idx < 12) {
    int wtask;
    if (idx < 8) {
      const int task = xcd * 8 + idx;
      ln1_task(A, task, lds0, tid, lane, wave);
      wtask = task < 16 ? 32 + task : 48 + task - 16;
    } else {
      wtask = xcd * 4 + (idx - 8);
    }
    weight_task(A, wtask, lds0, tid, lane, wave);
    if (blockIdx.x == 95 && wave == 7) {
      const u64 t0 = __builtin_amdgcn_s_memrealtime();
      for (;;) {
        bool ok = true;
#pragma unroll
        for (int k = 0; k < 4; ++k) ok &= __hip_atomic_load((gu32*)(flags + FL_STARTED + k * 64 + lane), RLX_AGENT) == tag7;
        if (__all(ok)) { if (lane == 0) set_flag(flags, tag7); break; }
        if (__builtin_amdgcn_s_memrealtime() - t0 > SPIN_TICKS) break;
        __builtin_amdgcn_s_sleep(8);
      }
    }
  } else {
    conv_role(A, xcd * 20 + idx - 12, lds0, tid, lane, wave, tag7);
  }
  WAIT_LGKM0();
  BARRIER();
  const u32 tag = UNI(*LDS(u32, lds0 + OFF_TAG));
  if (wave >= 4) {
    gemm1_loader(A, lds0, lane, wave, tag);
    gemm2_loader(A, lds0, lane, wave, tag);
  } else {
    __builtin_amdgcn_s_setprio(2);
    gemm1_compute(A, lds0, lane, wave);
    gemm2_compute(A, lds0, tid, lane, wave, tag);
  }
}

extern "C" void kernel_launch(void* const* d_in, const int* in_sizes, int n_in, void* d_out, int out_size, void* d_ws,
                              size_t ws_size, hipStream_t stream) {
  Args A;
  A.x = (const u16*)d_in[0];
  A.Are = (const u16*)d_in[1]; A.Aim = (const u16*)d_in[2]; A.Bre = (const u16*)d_in[3]; A.Bim = (const u16*)d_in[4];
  A.Cre = (const u16*)d_in[5]; A.Cim = (const u16*)d_in[6]; A.logdt = (const u16*)d_in[7]; A.Dskip = (const u16*)d_in[8];
  A.Wg = (const u16*)d_in[10]; A.Wu = (const u16*)d_in[11]; A.Wd = (const u16*)d_in[12];
  A.out = (u16*)d_out;
  A.ws = (char*)d_ws;
  (void)hipFuncSetAttribute((const void*)s4_reglu_block, hipFuncAttributeMaxDynamicSharedMemorySize, LDS_BYTES);
  s4_reglu_block<<<256, 512, LDS_BYTES, stream>>>(A);
}
```

```cpp
#include <hip/hip_runtime.h>
#include <hip/hip_bf16.h>
#include <stdint.h>

typedef unsigned short u16;
typedef unsigned u32;
typedef unsigned long long u64;
typedef short s16x4 __attribute__((ext_vector_type(4)));
typedef __bf16 bf16x8 __attribute__((ext_vector_type(8)));
typedef __bf16 bf16x2 __attribute__((ext_vector_type(2)));
typedef _Float16 f16x2 __attribute__((ext_vector_type(2)));
typedef _Float16 f16x8 __attribute__((ext_vector_type(8)));
typedef float f32x16 __attribute__((ext_vector_type(16)));
typedef float f32x4 __attribute__((ext_vector_type(4)));
typedef float f32x2 __attribute__((ext_vector_type(2)));
typedef u32 u32x4 __attribute__((ext_vector_type(4)));
typedef u32 u32x2 __attribute__((ext_vector_type(2)));
typedef __attribute__((address_space(1))) u32 gu32;
typedef __attribute__((address_space(1))) u64 gu64;
#define LAS __attribute__((address_space(3)))
#define LDS(T, a) ((LAS T*)(size_t)(a))

constexpr int L = 2048, D = 640, NS = 192, F = 1536;

constexpr size_t WS_UT = 0;
constexpr size_t WS_LN = 4u << 20;
constexpr size_t WS_HT = 8u << 20;
constexpr size_t WS_HM = 12u << 20;
constexpr size_t WS_W1 = 32u << 20;
constexpr size_t WS_WD = 36u << 20;
constexpr size_t WS_PT = 40u << 20;
constexpr size_t WS_FL = 52u << 20;
constexpr int FL_STARTED = 1024;
constexpr int FL_U = 2048;
constexpr int FL_W = FL_U + 64 * 32;
constexpr int FL_HE = FL_W + 96 * 32;
constexpr int FL_HL = FL_HE + 160 * 32;
constexpr int FL_P = FL_HL + 160 * 32;
constexpr int UT_ROW = 4160;

__host__ __device__ constexpr int ht_row(int d) { return ((d & 2) ? 320 : 0) + (d >> 2) * 2 + (d & 1); }
__host__ __device__ constexpr int ht_chan(int r) { return r < 320 ? (r >> 1) * 4 + (r & 1) : ((r - 320) >> 1) * 4 + 2 + (r & 1); }
static_assert(ht_chan(ht_row(639)) == 639 && ht_chan(ht_row(5)) == 5 && ht_row(2) == 320, "permutation");

struct Args {
  const u16 *x, *Are, *Aim, *Bre, *Bim, *Cre, *Cim, *logdt, *Dskip, *Wg, *Wu, *Wd;
  u16* out;
  char* ws;
};

__device__ __forceinline__ float bf2f(u16 v) { return __uint_as_float(((u32)v) << 16); }
__device__ __forceinline__ u32 pack_bf16(float lo, float hi) { return __builtin_bit_cast(u32, __builtin_convertvector((f32x2){lo, hi}, bf16x2)); }
__device__ __forceinline__ u32 pack_f16(float lo, float hi) { return __builtin_bit_cast(u32, __builtin_convertvector((f32x2){lo, hi}, f16x2)); }
__device__ __forceinline__ float rbf(float f) { return __uint_as_float(pack_bf16(f, f) << 16); }
__device__ __forceinline__ float lo_bf(u32 v) { return __uint_as_float(v << 16); }
__device__ __forceinline__ float hi_bf(u32 v) { return __uint_as_float(v & 0xffff0000u); }

#define WAIT_VM(n) asm volatile("s_waitcnt vmcnt(%0)" ::"n"(n) : "memory")
#define WAIT_LGKM0() do { asm volatile("" ::: "memory"); __builtin_amdgcn_s_waitcnt(0xC07F); asm volatile("" ::: "memory"); } while (0)
#define BARRIER() do { asm volatile("" ::: "memory"); __builtin_amdgcn_s_barrier(); asm volatile("" ::: "memory"); } while (0)
#define SB() __builtin_amdgcn_sched_barrier(0)
#define SG(m, n) __builtin_amdgcn_sched_group_barrier(m, n, 0)
#define RLX_AGENT __ATOMIC_RELAXED, __HIP_MEMORY_SCOPE_AGENT

__device__ __forceinline__ void glds16(const void* gsrc, u32 lds_dst) {
  u32 keep;
  asm volatile("s_mov_b32 %0, m0\n\ts_mov_b32 m0, %2\n\ts_nop 0\n\tglobal_load_lds_dwordx4 %1, off\n\ts_mov_b32 m0, %0"
               : "=&s"(keep) : "v"(gsrc), "s"(__builtin_amdgcn_readfirstlane(lds_dst)) : "memory");
}
__device__ __forceinline__ void glds16d(const void* gsrc, u32 lds_dst) {
  u32 keep;
  asm volatile("s_mov_b32 %0, m0\n\ts_mov_b32 m0, %2\n\ts_nop 0\n\tglobal_load_lds_dwordx4 %1, off sc1\n\ts_mov_b32 m0, %0"
               : "=&s"(keep) : "v"(gsrc), "s"(__builtin_amdgcn_readfirstlane(lds_dst)) : "memory");
}
#define UNI(v) ((u32)__builtin_amdgcn_readfirstlane((int)(v)))
__device__ __forceinline__ void st16d(void* p, u32x4 v) {
  asm volatile("global_store_dwordx4 %0, %1, off sc1\n\ts_nop 1" :: "v"(p), "v"(v) : "memory");
}
constexpr u64 SPIN_TICKS = 300000;
constexpr int POLL_SLEEP = 2;
template <int NL>
__device__ __forceinline__ void wait_flags(const u32* flags, int n, u32 tag, int lane) {
  const u64 t0 = __builtin_amdgcn_s_memrealtime();
  for (;;) {
    bool ok = true;
#pragma unroll
    for (int k = 0; k < NL; ++k) {
      const int i = k * 64 + lane;
      ok &= __hip_atomic_load((gu32*)(flags + (i < n ? i : n - 1) * 32), RLX_AGENT) == tag;
    }
    if (__all(ok)) break;
    if (__builtin_amdgcn_s_memrealtime() - t0 > SPIN_TICKS) break;
    __builtin_amdgcn_s_sleep(POLL_SLEEP);
  }
  asm volatile("" ::: "memory");
}
template <int NL>
__device__ __forceinline__ bool poll_once(const u32* flags, int n, u32 tag, int lane) {
  bool ok = true;
#pragma unroll
  for (int k = 0; k < NL; ++k) {
    const int i = k * 64 + lane;
    ok &= __hip_atomic_load((gu32*)(flags + (i < n ? i : n - 1) * 32), RLX_AGENT) == tag;
  }
  return __all(ok);
}
__device__ __forceinline__ void set_flag(u32* flag, u32 tag) { __hip_atomic_store((gu32*)flag, tag, RLX_AGENT); }

__device__ __forceinline__ s16x4 tr_read(u32 addr) { return __builtin_amdgcn_ds_read_tr16_b64_v4i16(LDS(s16x4, addr)); }
__device__ __forceinline__ u32x4 tr_read2(u32 a0, u32 a1) {
  const s16x4 a = tr_read(a0), b = tr_read(a1);
  return __builtin_bit_cast(u32x4, __builtin_shufflevector(a, b, 0, 1, 2, 3, 4, 5, 6, 7));
}
__device__ __forceinline__ bf16x8 tr_frag(u32 a0, u32 a1) { return __builtin_bit_cast(bf16x8, tr_read2(a0, a1)); }
__device__ __forceinline__ f32x4 mfma16(bf16x8 a, bf16x8 b, f32x4 c) { return __builtin_amdgcn_mfma_f32_16x16x32_bf16(a, b, c, 0, 0, 0); }
#define ONES_BF16 __builtin_bit_cast(bf16x8, (u32x4){0x3f803f80u, 0x3f803f80u, 0x3f803f80u, 0x3f803f80u})
__device__ __forceinline__ float diag16(f32x4 a, int lane) { const int r = lane & 3; return r == 0 ? a[0] : r == 1 ? a[1] : r == 2 ? a[2] : a[3]; }

template <int CTRL> __device__ __forceinline__ float dpp_f(float v) {
  return __builtin_bit_cast(float, __builtin_amdgcn_update_dpp(0, __builtin_bit_cast(int, v), CTRL, 0xf, 0xf, true));
}
__device__ __forceinline__ float wave_max(float v) {
  v = fmaxf(v, dpp_f<0xB1>(v));
  v = fmaxf(v, dpp_f<0x4E>(v));
  v = fmaxf(v, dpp_f<0x141>(v));
  v = fmaxf(v, dpp_f<0x140>(v));
  { auto r = __builtin_amdgcn_permlane16_swap(__float_as_uint(v), __float_as_uint(v), false, false); v = fmaxf(__uint_as_float(r[0]), __uint_as_float(r[1])); }
  { auto r = __builtin_amdgcn_permlane32_swap(__float_as_uint(v), __float_as_uint(v), false, false); v = fmaxf(__uint_as_float(r[0]), __uint_as_float(r[1])); }
  return v;
}
__device__ __forceinline__ u32x4 widen16(u32x2 a, u32x2 b) {
  const auto s0 = __builtin_amdgcn_permlane16_swap(a[0], b[0], false, false);
  const auto s1 = __builtin_amdgcn_permlane16_swap(a[1], b[1], false, false);
  return (u32x4){s0[0], s1[0], s0[1], s1[1]};
}

constexpr int LDS_BYTES = 163840;
constexpr int OFF_TAG = LDS_BYTES - 16;
constexpr int C_ROW = 272;
constexpr int C_SCR = 60 * C_ROW;
constexpr int C_KCH = 4608;
constexpr int C_UCP = 4160, C_UCH = 2 * C_UCP;
constexpr int F_OFF_P = 4 * C_SCR;
constexpr int F_OFF_S = F_OFF_P + 9216;
constexpr int F_OFF_X = F_OFF_S + 16;
constexpr int F_OFF_K = 74752;
constexpr int F_OFF_U = F_OFF_K + 4 * C_KCH;
constexpr int F_OFF_L = F_OFF_U + 4 * C_UCH;
constexpr int LN_RS = 1344;
constexpr int G1_STG = 20480, G1_NS = 7, G1_NK = 20;
constexpr int G2_STG = 36864, G2_NK = 12;
constexpr int G2_OFF_H = G2_STG * 4;
static_assert(F_OFF_L + 16384 <= OFF_TAG && G1_STG * G1_NS <= OFF_TAG && G2_OFF_H + 10240 <= OFF_TAG, "LDS");

__device__ __forceinline__ void ln1_task(const Args& A, int task, u32 lds0, int tid, int lane, int wave) {
  char* uT = A.ws + WS_UT;
  const int t0 = task * 32;
  const u32 tX = lds0, tU = lds0 + 40960;
#pragma unroll
  for (int k = 0; k < 5; ++k) {
    const int P = (wave * 5 + k) * 64 + lane, row = P / 80, chk = (P % 80) ^ (row & 15);
    glds16((const char*)A.x + (size_t)(t0 + row) * 1280 + chk * 16, UNI(tX + (wave * 5 + k) * 1024));
  }
  WAIT_VM(0);
  BARRIER();
  {
    const int row = 4 * wave + (lane >> 4);
    f32x2 xv[20];
#pragma unroll
    for (int k = 0; k < 5; ++k) {
      const u32x4 w = *LDS(u32x4, tX + row * 1280 + (((lane & 15) + 16 * k) ^ (row & 15)) * 16);
#pragma unroll
      for (int e = 0; e < 4; ++e) xv[4 * k + e] = (f32x2){lo_bf(w[e]), hi_bf(w[e])};
    }
    f32x2 a1 = xv[0] + xv[1], a2 = xv[0] * xv[0], b1 = xv[2] + xv[3], b2 = xv[1] * xv[1];
    a2 = __builtin_elementwise_fma(xv[2], xv[2], a2); b2 = __builtin_elementwise_fma(xv[3], xv[3], b2);
#pragma unroll
    for (int k = 4; k < 20; k += 2) {
      a1 += xv[k]; b1 += xv[k + 1];
      a2 = __builtin_elementwise_fma(xv[k], xv[k], a2); b2 = __builtin_elementwise_fma(xv[k + 1], xv[k + 1], b2);
    }
    float s1 = (a1.x + a1.y) + (b1.x + b1.y), s2 = (a2.x + a2.y) + (b2.x + b2.y);
    s1 += dpp_f<0xB1>(s1); s2 += dpp_f<0xB1>(s2);
    s1 += dpp_f<0x4E>(s1); s2 += dpp_f<0x4E>(s2);
    s1 += dpp_f<0x141>(s1); s2 += dpp_f<0x141>(s2);
    s1 += dpp_f<0x140>(s1); s2 += dpp_f<0x140>(s2);
    const float mean = s1 * (1.f / D);
    const float var = fmaxf(s2 * (1.f / D) - mean * mean, 0.f);
    const float mb = rbf(mean), sd = rbf(__builtin_amdgcn_sqrtf(rbf(rbf(var) + 1e-5f)));
    const float ri = __builtin_amdgcn_rcpf(sd), mi = -mb * ri;
    if ((lane & 15) == 0)
      __hip_atomic_store((gu64*)((u64*)(A.ws + WS_LN) + t0 + row), ((u64)__float_as_uint(mb) << 32) | __float_as_uint(sd), RLX_AGENT);
#pragma unroll
    for (int k = 0; k < 5; ++k) {
      u32x4 o;
#pragma unroll
      for (int e = 0; e < 4; ++e) {
        const f32x2 u = __builtin_elementwise_fma(xv[4 * k + e], (f32x2){ri, ri}, (f32x2){mi, mi});
        o[e] = pack_f16(u.x, u.y);
      }
      *LDS(u32x4, tU + row * LN_RS + ((lane & 15) + 16 * k) * 16) = o;
    }
  }
  WAIT_LGKM0();
  BARRIER();
  {
    const int g = lane >> 4, q = (lane & 15) >> 2, p = lane & 3;
    u32x4 ov[5];
#pragma unroll
    for (int k = 0; k < 5; ++k) {
      const u32 a = tU + (8 * g + q) * LN_RS + ((wave * 5 + k) * 16 + 4 * p) * 2;
      ov[k] = tr_read2(a, a + 4 * LN_RS);
    }
#pragma unroll
    for (int k = 0; k < 5; ++k) st16d(uT + (size_t)((wave * 5 + k) * 16 + (lane & 15)) * UT_ROW + 32 + (t0 + 8 * g) * 2, ov[k]);
  }
  WAIT_VM(0);
  BARRIER();
  if (tid == 0) set_flag((u32*)(A.ws + WS_FL) + FL_U + task * 32, *LDS(u32, lds0 + OFF_TAG));
}

__device__ __forceinline__ void weight_task(const Args& A, int wtask, u32 lds0, int tid, int lane, int wave) {
  const int g = lane >> 4, q = (lane & 15) >> 2, p = lane & 3;
  if (wtask < 48) {
    const u16* W = wtask < 24 ? A.Wg : A.Wu;
    const int c0 = (wtask % 24) * 64;
    {
      const int row = 8 * wave + (lane >> 3), chk = (lane & 7) ^ ((row & 3) << 1);
      const char* src = (const char*)W + c0 * 2 + chk * 16;
#pragma unroll
      for (int jj = 0; jj < 10; ++jj)
        glds16(src + (size_t)ht_chan(row + 64 * jj) * 3072, UNI(lds0 + (wave + 8 * jj) * 1024));
    }
    WAIT_VM(0);
    BARRIER();
    const int tl = wave & 3, ks0 = (wave >> 2) * 10;
    const u32 a = lds0 + (32 * ks0 + 8 * g + q) * 128 + (((2 * tl + (p >> 1)) ^ (q << 1)) * 16) + 8 * (p & 1);
    bf16x8 fr[10];
    f32x4 cs = {0.f, 0.f, 0.f, 0.f};
#pragma unroll
    for (int k = 0; k < 10; ++k) { fr[k] = tr_frag(a + k * 4096, a + k * 4096 + 512); cs = mfma16(ONES_BF16, fr[k], cs); }
    WAIT_LGKM0();
    BARRIER();
    if (lane < 16) *LDS(float, lds0 + (wave * 16 + lane) * 4) = cs[0];
    WAIT_LGKM0();
    BARRIER();
    const float cm = (*LDS(float, lds0 + (tl * 16 + (lane & 15)) * 4) + *LDS(float, lds0 + ((tl + 4) * 16 + (lane & 15)) * 4)) * (1.f / D);
    const int f = c0 + 16 * tl, pn = f / 96, fi = (f % 96) / 16;
    const int slot = (fi / 3) * 6 + (wtask < 24 ? 0 : 3) + fi % 3;
    char* dst = A.ws + WS_W1 + (((size_t)pn * 20 + ks0) * 12 + slot) * 1024 + lane * 16;
#pragma unroll
    for (int k = 0; k < 10; ++k) {
      const u32x4 w = __builtin_bit_cast(u32x4, fr[k]);
      u32x4 o;
#pragma unroll
      for (int e = 0; e < 4; ++e) o[e] = pack_bf16(lo_bf(w[e]) - cm, hi_bf(w[e]) - cm);
      st16d(dst + (size_t)k * 12288, o);
    }
  } else {
    const int ks = wtask - 48;
#pragma unroll
    for (int jj = 0; jj < 5; ++jj) {
      const int P = 64 * (wave + 8 * jj) + lane, row = P / 80, chk = P % 80;
      glds16((const char*)A.Wd + (size_t)(32 * ks + row) * 1280 + chk * 16, UNI(lds0 + (wave + 8 * jj) * 1024));
    }
    WAIT_VM(0);
    BARRIER();
#pragma unroll
    for (int jj = 0; jj < 5; ++jj) {
      const int fr = wave + 8 * jj;
      const u32 a = lds0 + (8 * g + q) * 1280 + (16 * fr + 4 * p) * 2;
      st16d(A.ws + WS_WD + (((size_t)(fr / 5) * 48 + ks) * 5 + fr % 5) * 1024 + lane * 16, tr_read2(a, a + 4 * 1280));
    }
  }
  WAIT_VM(0);
  BARRIER();
  if (tid == 0) set_flag((u32*)(A.ws + WS_FL) + FL_W + wtask * 32, *LDS(u32, lds0 + OFF_TAG));
}

struct cplx { f32x2 v, r; };
__device__ __forceinline__ cplx mk(f32x2 v) { return cplx{v, (f32x2){-v.y, v.x}}; }
__device__ __forceinline__ f32x2 cmul(f32x2 a, const cplx& w) {
  const f32x2 t = (f32x2){a.x, a.x} * w.v;
  return __builtin_elementwise_fma((f32x2){a.y, a.y}, w.r, t);
}
__device__ __forceinline__ f32x2 cexp(float zr, float zi, float l, float sgn) {
  const float m = __builtin_amdgcn_exp2f(zr * l * 1.44269504088896341f);
  const float a = __builtin_amdgcn_fractf(zi * l * 0.15915494309189535f);
  return (f32x2){m * __builtin_amdgcn_cosf(a), sgn * m * __builtin_amdgcn_sinf(a)};
}
__device__ __forceinline__ u32 pkh(f32x2 v) { return __builtin_bit_cast(u32, __builtin_convertvector(v, f16x2)); }
__device__ __forceinline__ u32 cmulh(u32 a, u32 b) {
  u32 t, r;
  asm("v_pk_mul_f16 %0, %1, %2 op_sel:[0,0] op_sel_hi:[0,1]" : "=v"(t) : "v"(a), "v"(b));
  asm("v_pk_fma_f16 %0, %1, %2, %3 op_sel:[1,1,0] op_sel_hi:[1,0,1] neg_lo:[0,1,0]" : "=v"(r) : "v"(a), "v"(b), "v"(t));
  return r;
}
#define SET_M0(base) asm volatile("s_mov_b32 m0, %0\n\ts_nop 0" ::"s"(base) : "memory")
#define WR4(row, a, b, c, d)                                                                                    \
  asm volatile("ds_write_addtid_b32 %0 offset:%4\n\tds_write_addtid_b32 %1 offset:%4+272\n\t"                    \
               "ds_write_addtid_b32 %2 offset:%4+544\n\tds_write_addtid_b32 %3 offset:%4+816"                    \
               ::"v"(a), "v"(b), "v"(c), "v"(d), "n"((row) * C_ROW) : "memory")

__device__ __forceinline__ void conv_role(const Args& A, int grp, u32 lds0, int tid, int lane, int wave, u32 tag) {
  const int ch = (wave >> 2) * 2 + ((wave >> 1) & 1), hf = wave & 1, d0 = grp * 4, d = d0 + ch;
  const u32 sb = UNI(lds0 + ch * C_SCR);
  const int r32 = lane & 31, h32 = lane >> 5;
  const int j = lane & 15, g = lane >> 4;
  {
    const u64 q0 = (u64)A.Are, q1 = (u64)A.Aim, q2 = (u64)A.Bre, q3 = (u64)A.Bim, q4 = (u64)A.Cre, q5 = (u64)A.Cim;
#pragma unroll
    for (int rep = 0; rep < 2; ++rep) {
      if (rep == 1 && wave != 0) break;
      const int i = rep ? 8 : wave;
      const int b = 1024 * i + 16 * lane, a = b / 1536, o = b - a * 1536;
      u64 src = +q0;
      src = a == 1 ? +q1 : src; src = a == 2 ? +q2 : src; src = a == 3 ? +q3 : src; src = a == 4 ? +q4 : src; src = a == 5 ? +q5 : src;
      glds16((const char*)(src + (u64)d0 * 384 + o), UNI(lds0 + F_OFF_P + i * 1024));
    }
  }
  const u32 wdt = ((const u32*)A.logdt)[d >> 1], wds = ((const u32*)A.Dskip)[d >> 1];
  const float dt = rbf(__expf((d & 1) ? hi_bf(wdt) : lo_bf(wdt)));
  WAIT_VM(0);
  BARRIER();
  float zr[3], zi[3];
  const u32 pl = lds0 + F_OFF_P + ch * 384 + lane * 2;
#pragma unroll
  for (int r = 0; r < 3; ++r) {
    zr[r] = dt * bf2f(*LDS(u16, pl + r * 128));
    zi[r] = dt * bf2f(*LDS(u16, pl + 1536 + r * 128));
  }
  u32 an[3][11];
  if (hf == 0) {
    f32x2 cb[3];
    float bound = 0.f;
#pragma unroll
    for (int r = 0; r < 3; ++r) {
      const float br = dt * bf2f(*LDS(u16, pl + 2 * 1536 + r * 128)), bi = dt * bf2f(*LDS(u16, pl + 3 * 1536 + r * 128));
      const float cr = bf2f(*LDS(u16, pl + 4 * 1536 + r * 128)), ci = bf2f(*LDS(u16, pl + 5 * 1536 + r * 128));
      cb[r] = (f32x2){cr * br - ci * bi, -(cr * bi + ci * br)};
      const float grow = __builtin_amdgcn_exp2f(fmaxf(zr[r], 0.f) * (2064.f * 1.44269504f));
      bound = fmaxf(bound, (fabsf(cb[r].x) + fabsf(cb[r].y)) * grow);
    }
    bound = wave_max(bound);
    int ex = (int)((__float_as_uint(bound) >> 23) & 255);
    ex = ex < 20 ? 20 : (ex > 240 ? 240 : ex);
    const float scale = __uint_as_float((u32)(267 - ex) << 23);
    if (lane == 0) *LDS(float, lds0 + F_OFF_S + ch * 4) = __uint_as_float((u32)(ex - 5) << 23);
#pragma unroll
    for (int r = 0; r < 3; ++r) {
      const cplx w1 = mk(cexp(zr[r], zi[r], 1.f, -1.f));
      const cplx w512 = mk(cexp(zr[r], zi[r], 512.f, -1.f));
      const cplx w2 = mk(cmul(w1.v, w1));
      const f32x2 w3 = cmul(w2.v, w1);
      const cplx w4 = mk(cmul(w2.v, w2));
      const f32x2 w1024 = cmul(w512.v, w512), w1536 = cmul(w1024, w512);
      const f32x2 P0 = cb[r] * scale, P4 = cmul(P0, w4), P8 = cmul(P4, w4), P12 = cmul(P8, w4), P16 = cmul(P12, w4);
      an[r][0] = pkh(P0); an[r][1] = pkh(P4); an[r][2] = pkh(P8); an[r][3] = pkh(P12); an[r][4] = pkh(P16);
      an[r][5] = pkh(w1.v); an[r][6] = pkh(w2.v); an[r][7] = pkh(w3);
      an[r][8] = pkh(w512.v); an[r][9] = pkh(w1024); an[r][10] = pkh(w1536);
    }
  } else {
#pragma unroll
    for (int r = 0; r < 3; ++r) {
      const cplx w16 = mk(cexp(zr[r], zi[r], 16.f, 1.f));
      const cplx w128 = mk(cexp(zr[r], zi[r], 128.f, 1.f));
      const cplx w32 = mk(cmul(w16.v, w16));
      const f32x2 w48 = cmul(w32.v, w16);
      const f32x2 w64 = cmul(w32.v, w32);
      const f32x2 w192 = cmul(w64, w128), w256 = cmul(w128.v, w128);
      const f32x2 w320 = cmul(w192, w128), w384 = cmul(w256, w128), w448 = cmul(w320, w128);
      an[r][0] = pkh(w64); an[r][1] = pkh(w128.v); an[r][2] = pkh(w192); an[r][3] = pkh(w256);
      an[r][4] = pkh(w320); an[r][5] = pkh(w384); an[r][6] = pkh(w448);
      an[r][7] = pkh(w16.v); an[r][8] = pkh(w32.v); an[r][9] = pkh(w48); an[r][10] = 0x00003c00u;
    }
  }
  u32 arow;
  {
    const int hr = (r32 >> 2) & 1, rho = (r32 & 3) + 4 * (r32 >> 3);
    const int row = rho <= 8 ? rho + 8 * hr : rho <= 11 ? rho + 8 + 2 * hr : rho <= 13 ? rho + 10 + hr : rho + 11 + hr;
    arow = sb + row * C_ROW + h32 * 16 + hf * 128;
  }
  const u32 brow = sb + (28 + r32) * C_ROW + h32 * 16 + hf * 128;
  f32x16 kacc;
#pragma unroll
  for (int i = 0; i < 16; ++i) kacc[i] = 0.f;
  SET_M0(sb);
#pragma unroll
  for (int r = 0; r < 3; ++r) {
    u32 gv[2][4];
    auto grp4 = [&](int gi, u32 (&o)[4]) {
      const u32* a = an[r];
      if (hf == 0) {
        if (gi < 4) { o[0] = a[gi]; o[1] = cmulh(a[gi], a[5]); o[2] = cmulh(a[gi], a[6]); o[3] = cmulh(a[gi], a[7]); }
        else if (gi == 4) { o[0] = a[4]; o[1] = cmulh(a[0], a[8]); o[2] = cmulh(a[1], a[8]); o[3] = cmulh(a[2], a[8]); }
        else if (gi == 5) { o[0] = cmulh(a[3], a[8]); o[1] = cmulh(a[4], a[8]); o[2] = cmulh(a[0], a[9]); o[3] = cmulh(a[2], a[9]); }
        else if (gi == 6) { o[0] = cmulh(a[4], a[9]); o[1] = cmulh(a[0], a[10]); o[2] = cmulh(a[2], a[10]); o[3] = cmulh(a[4], a[10]); }
      } else {
        if (gi == 0) { o[0] = a[10]; o[1] = a[7]; o[2] = a[8]; o[3] = a[9]; }
        else { o[0] = a[gi - 1]; o[1] = cmulh(a[gi - 1], a[7]); o[2] = cmulh(a[gi - 1], a[8]); o[3] = cmulh(a[gi - 1], a[9]); }
      }
    };
    if (hf == 0) {
      grp4(0, gv[0]);
#pragma unroll
      for (int gi = 0; gi < 7; ++gi) {
        if (gi < 6) grp4(gi + 1, gv[(gi + 1) & 1]);
        SB();
        WR4(4 * gi, gv[gi & 1][0], gv[gi & 1][1], gv[gi & 1][2], gv[gi & 1][3]);
        SB();
      }
    } else {
      grp4(0, gv[0]);
#pragma unroll
      for (int gi = 0; gi < 8; ++gi) {
        if (gi < 7) grp4(gi + 1, gv[(gi + 1) & 1]);
        SB();
        WR4(28 + 4 * gi, gv[gi & 1][0], gv[gi & 1][1], gv[gi & 1][2], gv[gi & 1][3]);
        SB();
      }
    }
    WAIT_LGKM0();
    BARRIER();
    f16x8 fa[4], fb[4];
#pragma unroll
    for (int k = 0; k < 4; ++k) { fa[k] = *LDS(f16x8, arow + k * 32); fb[k] = *LDS(f16x8, brow + k * 32); }
    WAIT_LGKM0();
    BARRIER();
#pragma unroll
    for (int k = 0; k < 4; ++k) kacc = __builtin_amdgcn_mfma_f32_32x32x16_f16(fa[k], fb[k], kacc, 0, 0, 0);
  }
  {
    const u32 ex = sb + hf * 4096 + lane * 16, exr = sb + (hf ^ 1) * 4096 + lane * 16;
    if (hf == 0) {
      *LDS(f32x4, ex) = (f32x4){kacc[12], kacc[13], kacc[14], kacc[15]};
    } else {
      *LDS(f32x4, ex) = (f32x4){kacc[0], kacc[1], kacc[2], kacc[3]};
      *LDS(f32x4, ex + 1024) = (f32x4){kacc[4], kacc[5], kacc[6], kacc[7]};
      *LDS(f32x4, ex + 2048) = (f32x4){kacc[8], kacc[9], kacc[10], kacc[11]};
    }
    WAIT_LGKM0();
    BARRIER();
    const u32 kb = lds0 + F_OFF_K + ch * C_KCH + 4080 - 32 * r32 - 16 * h32;
    auto put = [&](int base, float e0, float e1, float e2, float e3, float e4, float e5, float e6, float e7) {
      *LDS(u32x4, kb - 2 * base) = (u32x4){pack_f16(e7, e6), pack_f16(e5, e4), pack_f16(e3, e2), pack_f16(e1, e0)};
    };
    if (hf == 0) {
      const f32x4 o0 = *LDS(f32x4, exr), o1 = *LDS(f32x4, exr + 1024), o2 = *LDS(f32x4, exr + 2048);
      float v[12];
#pragma unroll
      for (int i = 0; i < 4; ++i) {
        v[i] = (kacc[i] + o0[i]) * (1.f / 256.f); v[4 + i] = (kacc[4 + i] + o1[i]) * (1.f / 256.f); v[8 + i] = (kacc[8 + i] + o2[i]) * (1.f / 256.f);
      }
      const bool far = r32 >= 16;
      put(0, v[0], far ? v[0] : v[1], v[2], far ? v[4] : v[3], v[4], far ? v[4] : v[5], v[6], far ? v[8] : v[7]);
      put(512, v[9], v[9], v[9], v[10], v[10], v[10], v[11], v[11]);
    } else {
      const f32x4 o = *LDS(f32x4, exr);
      const float v12 = (kacc[12] + o[0]) * (1.f / 256.f), v13 = (kacc[13] + o[1]) * (1.f / 256.f),
                  v14 = (kacc[14] + o[2]) * (1.f / 256.f), v15 = (kacc[15] + o[3]) * (1.f / 256.f);
      put(1024, v12, v12, v12, v12, h32 ? v13 : v12, v13, v13, v13);
      put(1536, v14, v14, v14, v14, h32 ? v15 : v14, v15, v15, v15);
    }
  }
  if (hf == 1 && lane < 32) *LDS(u32x4, lds0 + F_OFF_K + ch * C_KCH + 4096 + lane * 16) = (u32x4){0u, 0u, 0u, 0u};
  if (wave == 0 && lane < 10) *LDS(u32, lds0 + F_OFF_X + lane * 4) = 0u;
  if (wave == 7) wait_flags<1>((const u32*)(A.ws + WS_FL) + FL_U, 64, tag, lane);
  WAIT_LGKM0();
  BARRIER();
  {
    const char* up = A.ws + WS_UT + (size_t)d * UT_ROW + lane * 16 + hf * 2;
    const u32 dst = UNI(lds0 + F_OFF_U + ch * C_UCH + hf * C_UCP);
#pragma unroll
    for (int k = 0; k < 4; ++k) glds16d(up + k * 1024, dst + k * 1024);
    if (lane < 2) glds16d(up + 4096, dst + 4096);
    glds16d(A.ws + WS_LN + wave * 2048 + lane * 16, UNI(lds0 + F_OFF_L + wave * 2048));
    glds16d(A.ws + WS_LN + wave * 2048 + 1024 + lane * 16, UNI(lds0 + F_OFF_L + wave * 2048 + 1024));
  }
  WAIT_VM(0);
  if (lane < 8) {
    const u32 zb = lds0 + F_OFF_U + ch * C_UCH + hf * C_UCP;
    if (lane < 7 || hf == 0) *LDS(u32, zb + lane * 4) = 0u; else *LDS(u16, zb + 28) = (u16)0;
  }
  WAIT_LGKM0();
  BARRIER();
  const int cc = (1 + j) & 1;
  const u32 aa = lds0 + F_OFF_U + ch * C_UCH + cc * C_UCP + 2 * (1 + j + 8 * g - cc) + hf * 256;
  const u32 ba = lds0 + F_OFF_K + ch * C_KCH + 32 * j + 16 * g + hf * 256;
  f32x4 acc[8];
#pragma unroll
  for (int i = 0; i < 8; ++i) acc[i] = (f32x4){0.f, 0.f, 0.f, 0.f};
  if (wave < 4) __builtin_amdgcn_s_setprio(2);
  {
    f16x8 Af[2][8], Bf[2][8];
    u32 am[4];
#pragma unroll
    for (int k = 0; k < 4; ++k) { am[k] = aa + 1024 * k; asm volatile("" : "+v"(am[k])); }
    auto rd = [&](int set, int r) {
#pragma unroll
      for (int m = 0; m < 8; ++m) {
        const LAS u32* ap = LDS(const u32, am[m >> 1] + 64 * r + 512 * (m & 1));
        Af[set][m] = __builtin_bit_cast(f16x8, (u32x4){ap[0], ap[1], ap[2], ap[3]});
        Bf[set][m] = *LDS(f16x8, ba + 64 * r + 512 * (7 - m));
      }
    };
    auto mm = [&](int set) {
#pragma unroll
      for (int sdx = 0; sdx < 8; ++sdx)
#pragma unroll
        for (int m = 0; m <= sdx; ++m) acc[sdx] = __builtin_amdgcn_mfma_f32_16x16x32_f16(Af[set][m], Bf[set][sdx - m], acc[sdx], 0, 0, 0);
    };
#define C_SCHED() do { _Pragma("unroll") for (int k_ = 0; k_ < 24; ++k_) { SG(0x008, 1); SG(0x100, 1); } SG(0x008, 12); } while (0)
    rd(0, 0);
    SB(); rd(1, 1); mm(0); C_SCHED(); SB();
    rd(0, 2); mm(1); C_SCHED(); SB();
    rd(1, 3); mm(0); C_SCHED(); SB();
    mm(1);
  }
  {
    const u32 ex = lds0 + wave * 4096 + lane * 16, exr = lds0 + (wave ^ 1) * 4096 + lane * 16;
    if (hf == 0) {
#pragma unroll
      for (int i = 0; i < 4; ++i) *LDS(f32x4, ex + i * 1024) = acc[4 + i];
    } else {
#pragma unroll
      for (int i = 0; i < 4; ++i) *LDS(f32x4, ex + i * 1024) = acc[i];
    }
    WAIT_LGKM0();
    if (lane == 0) *LDS(volatile u32, lds0 + F_OFF_X + wave * 4) = 1u;
    const float it = *LDS(float, lds0 + F_OFF_S + ch * 4);
    const float dsk = (d & 1) ? hi_bf(wds) : lo_bf(wds);
    u32x2 dv[4], xv[4];
#pragma unroll
    for (int i = 0; i < 4; ++i) {
      const int t = 16 * (16 * (4 * hf + i) + 15 - j) + 4 * g;
      const u32x2 uw = *LDS(u32x2, lds0 + F_OFF_U + ch * C_UCH + 2 * (16 + t));
      const u32 w0 = uw[0], w1 = uw[1];
      const f16x2 u01 = __builtin_bit_cast(f16x2, w0), u23 = __builtin_bit_cast(f16x2, w1);
      const float uf[4] = {(float)u01[0], (float)u01[1], (float)u23[0], (float)u23[1]};
      dv[i] = (u32x2){pack_bf16(uf[0] * dsk, uf[1] * dsk), pack_bf16(uf[2] * dsk, uf[3] * dsk)};
      const f32x4 l01 = *LDS(f32x4, lds0 + F_OFF_L + t * 8), l23 = *LDS(f32x4, lds0 + F_OFF_L + t * 8 + 16);
      xv[i] = (u32x2){pack_bf16(fmaf(uf[0], l01[0], l01[1]), fmaf(uf[1], l01[2], l01[3])), pack_bf16(fmaf(uf[2], l23[0], l23[1]), fmaf(uf[3], l23[2], l23[3]))};
    }
#pragma unroll
    for (int i = 0; i < 4; ++i) asm volatile("" : "+v"(dv[i]), "+v"(xv[i]));
    while (*LDS(volatile u32, lds0 + F_OFF_X + (wave ^ 1) * 4) == 0u) __builtin_amdgcn_s_sleep(1);
    asm volatile("" ::: "memory");
    f32x4 y[4];
#pragma unroll
    for (int i = 0; i < 4; ++i) y[i] = *LDS(f32x4, exr + i * 1024);
    SB();
    if (hf) {
#pragma unroll
      for (int i = 0; i < 4; ++i) y[i] += acc[4 + i];
      asm volatile("" ::: "memory");
    } else {
#pragma unroll
      for (int i = 0; i < 4; ++i) y[i] += acc[i];
    }
#pragma unroll
    for (int i = 0; i < 4; ++i) y[i] *= it;
    u32x2 w[4];
#pragma unroll
    for (int i = 0; i < 4; ++i) w[i] = (u32x2){pack_bf16(y[i][0], y[i][1]), pack_bf16(y[i][2], y[i][3])};
#pragma unroll
    for (int i = 0; i < 4; ++i)
      w[i] = (u32x2){pack_bf16(lo_bf(w[i][0]) + lo_bf(dv[i][0]), hi_bf(w[i][0]) + hi_bf(dv[i][0])), pack_bf16(lo_bf(w[i][1]) + lo_bf(dv[i][1]), hi_bf(w[i][1]) + hi_bf(dv[i][1]))};
    u32x2 hv[4];
#pragma unroll
    for (int i = 0; i < 4; ++i)
      hv[i] = (u32x2){pack_bf16(lo_bf(xv[i][0]) + lo_bf(w[i][0]), hi_bf(xv[i][0]) + hi_bf(w[i][0])), pack_bf16(lo_bf(xv[i][1]) + lo_bf(w[i][1]), hi_bf(xv[i][1]) + hi_bf(w[i][1]))};
    char* hrow = A.ws + WS_HT + (size_t)ht_row(d) * 4096;
#pragma unroll
    for (int i = 0; i < 4; i += 2) st16d(hrow + (16 * (16 * (4 * hf + i + (g & 1)) + 15 - j) + 8 * (g >> 1)) * 2, widen16(hv[i], hv[i + 1]));
  }
  __builtin_amdgcn_s_setprio(0);
  if (wave < 4) {
    WAIT_VM(0);
    if (lane == 0 && __hip_atomic_fetch_add(LDS(u32, lds0 + F_OFF_X + 32), 1u, __ATOMIC_RELAXED, __HIP_MEMORY_SCOPE_WORKGROUP) == 3u)
      set_flag((u32*)(A.ws + WS_FL) + FL_HE + grp * 32, *LDS(u32, lds0 + OFF_TAG));
  }
}

__device__ __forceinline__ void gemm1_loader(const Args& A, u32 lds0, int lane, int wave, u32 tag) {
  const bool conv_wg = (blockIdx.x >> 3) >= 12;
  const int xcd = blockIdx.x & 7, idx = blockIdx.x >> 3;
  const int pm = (xcd & 1) * 8 + (idx & 7), pn = (xcd >> 1) * 4 + (idx >> 3);
  const int t0 = pm * 128;
  const u32* flags = (const u32*)(A.ws + WS_FL);
    const int lw = wave - 4;
    const int k0 = 5 * (pn & 1);
    int kH = 0, kW = 0;
    const int row = 8 * lw + (lane >> 4);
    const int chk = (lane & 15) ^ (((row & 3) << 1) | (((row >> 3) & 1) << 3));
    const char* srcH = A.ws + WS_HT + (size_t)(row + 32 * k0) * 4096 + t0 * 2 + chk * 16;
    const char* srcW = A.ws + WS_W1 + (size_t)pn * 245760 + (size_t)k0 * 12288 + lw * 3072 + lane * 16;
    auto kstep = [&](int p) { return (p / 10) * 10 + (p % 10 + k0) % 10; };
    const u32 dH = lds0 + lw * 2048, dW = lds0 + 8192 + lw * 3072;
    auto issueH = [&](u32 so) {
      glds16d(srcH, dH + so);
      glds16d(srcH + 4 * 4096, dH + so + 1024);
      srcH += (ptrdiff_t)(kstep(kH + 1) - kstep(kH)) * (32 * 4096); ++kH;
    };
    auto issueW = [&](u32 so) {
      glds16d(srcW, dW + so);
      glds16d(srcW + 1024, dW + so + 1024);
      glds16d(srcW + 2048, dW + so + 2048);
      srcW += (ptrdiff_t)(kstep(kW + 1) - kstep(kW)) * 12288; ++kW;
    };
    u32 so = 0;
    auto issue = [&]() { issueH(so); issueW(so); so = so == (G1_NS - 1) * G1_STG ? 0 : so + G1_STG; };
    if (conv_wg) {
      bool seen = false;
      if (wave == 4) seen = poll_once<4>(flags + FL_W, 256, tag, lane);
      WAIT_VM(0);
      if (lane == 0 && __hip_atomic_fetch_add(LDS(u32, lds0 + OFF_TAG + 4), 1u, __ATOMIC_RELAXED, __HIP_MEMORY_SCOPE_WORKGROUP) == 3u)
        set_flag((u32*)(A.ws + WS_FL) + FL_HL + (xcd * 20 + idx - 12) * 32, tag);
      if (wave == 4 && !seen) wait_flags<4>(flags + FL_W, 256, tag, lane);
      BARRIER();
      BARRIER();
      issue(); issue();
      WAIT_VM(5); BARRIER();
      issue(); issue();
      WAIT_VM(10); BARRIER();
      issue(); issue();
      WAIT_VM(15); BARRIER();
      issue(); issue();
      WAIT_VM(20); BARRIER();
      issue();
      WAIT_VM(20); BARRIER();
      issue();
      if (wave == 4) wait_flags<3>(flags + FL_HL, 160, tag, lane);
      WAIT_VM(20); BARRIER();
    } else {
      if (wave == 4) wait_flags<2>(flags + FL_W, 96, tag, lane);
      BARRIER();
#pragma unroll
      for (int s = 0; s < G1_NS; ++s) issueW(s * G1_STG);
      if (wave == 4) wait_flags<3>(flags + FL_HE, 160, tag, lane);
      BARRIER();
      issueH(0); issueH(G1_STG); issueH(2 * G1_STG);
      WAIT_VM(4); BARRIER();
      issueH(3 * G1_STG); issueH(4 * G1_STG);
      WAIT_VM(6); BARRIER();
      issueH(5 * G1_STG); issueH(6 * G1_STG);
      WAIT_VM(8); BARRIER();
      issue(); WAIT_VM(11); BARRIER();
      issue(); WAIT_VM(14); BARRIER();
      issue();
      if (wave == 4) wait_flags<3>(flags + FL_HL, 160, tag, lane);
      WAIT_VM(17); BARRIER();
    }
    for (int i = 5; i < G1_NK - 5; ++i) {
      issue();
      WAIT_VM(20); BARRIER();
    }
    WAIT_VM(15); BARRIER(); WAIT_VM(10); BARRIER(); WAIT_VM(5); BARRIER(); WAIT_VM(0); BARRIER();
}

__device__ __forceinline__ void gemm1_compute(const Args& A, u32 lds0, int lane, int wave) {
  const int xcd = blockIdx.x & 7, idx = blockIdx.x >> 3;
  const int pm = (xcd & 1) * 8 + (idx & 7), pn = (xcd >> 1) * 4 + (idx >> 3);
  const int t0 = pm * 128;
  const int wt = wave & 1, wf = wave >> 1;
  const int g = lane >> 4, q = (lane & 15) >> 2, p = lane & 3;
  const int rot = (pn >> 1) & 3;
  const u32 hrow = (8 * g + q) * 256 + 8 * (p & 1);
  const u32 hx = q | ((g & 1) << 2);
  u32 hc[4];
#pragma unroll
  for (int j = 0; j < 4; ++j) hc[j] = lds0 + hrow + ((((u32)(4 * wt + ((j + rot) & 3)) ^ hx) << 1) | (p >> 1)) * 16;
  u32 wc = lds0 + 8192 + wf * 6144 + lane * 16, so = 0;
  f32x4 acc[6][4];
#pragma unroll
  for (int i = 0; i < 6; ++i)
#pragma unroll
    for (int j = 0; j < 4; ++j) acc[i][j] = (f32x4){0.f, 0.f, 0.f, 0.f};
  f32x4 st1 = {0.f, 0.f, 0.f, 0.f}, st2 = st1;
  bf16x8 hf[2][4], wfr[2][6];
  auto rd = [&](int set) {
    wfr[set][0] = *LDS(bf16x8, wc);
#pragma unroll
    for (int j = 0; j < 4; ++j) hf[set][j] = tr_frag(hc[j], hc[j] + 1024);
#pragma unroll
    for (int i = 1; i < 6; ++i) wfr[set][i] = *LDS(bf16x8, wc + i * 1024);
  };
  auto adv = [&]() {
    u32 dl = so == (G1_NS - 1) * G1_STG ? (u32)(-(G1_NS - 1) * G1_STG) : (u32)G1_STG;
    so += dl;
    asm volatile("" : "+s"(dl));
#pragma unroll
    for (int j = 0; j < 4; ++j) hc[j] += dl;
    wc += dl;
  };
  auto mma = [&](int set) {
#pragma unroll
    for (int i = 0; i < 6; ++i)
#pragma unroll
      for (int j = 0; j < 4; ++j) acc[i][j] = mfma16(wfr[set][i], hf[set][j], acc[i][j]);
  };
  auto stat = [&](int set) {
    st1 = mfma16(ONES_BF16, hf[set][0], st1);
    st2 = mfma16(hf[set][0], hf[set][0], st2);
  };
#define G1_SCHED(extra) do { \
    SG(0x008, 1); SG(0x100, 1); SG(0x008, 1); SG(0x100, 1); SG(0x008, 1); SG(0x100, 1); SG(0x008, 1); SG(0x100, 1); \
    SG(0x008, 1); SG(0x100, 1); SG(0x008, 1); SG(0x100, 1); SG(0x008, 1); SG(0x100, 1); SG(0x008, 1); SG(0x100, 1); \
    SG(0x008, 1); SG(0x100, 1); SG(0x008, 2); SG(0x100, 1); SG(0x008, 2); SG(0x100, 1); SG(0x008, 2); SG(0x100, 1); \
    SG(0x008, 2); SG(0x100, 1); SG(0x008, 1); SG(0x100, 1); SG(0x008, 1); SG(0x002, 1); SG(0x008, 1); SG(0x002, 1); \
    SG(0x008, 1); SG(0x002, 1); SG(0x008, 1); SG(0x002, 1); SG(0x008, 1); SG(0x002, 1); SG(0x008, 1 + (extra)); } while (0)
  BARRIER();
  BARRIER();
  BARRIER();
  rd(0); adv();
#pragma unroll
  for (int i = 1; i < G1_NK - 1; i += 2) {
    BARRIER();
    SB(); rd(1); mma(0); adv();
    if ((i - 1) % 10 < 5) { stat(0); G1_SCHED(2); } else { G1_SCHED(0); }
    SB();
    BARRIER();
    SB(); rd(0); mma(1); adv();
    if (i % 10 < 5) { stat(1); G1_SCHED(2); } else { G1_SCHED(0); }
    SB();
  }
  BARRIER();
  SB(); rd(1); mma(0); G1_SCHED(0); SB();
  WAIT_LGKM0();
  BARRIER();
#pragma unroll
  for (int i = 0; i < 3; ++i)
#pragma unroll
    for (int j = 0; j < 4; ++j) {
      acc[i][j] = mfma16(wfr[1][i], hf[1][j], acc[i][j]);
      acc[3 + i][j] = mfma16(wfr[1][3 + i], hf[1][j], acc[3 + i][j]);
    }
  {
    char* ob = A.ws + WS_HM + ((size_t)(pm * 8 + 4 * wt) * 48 + 3 * pn) * 1024 + (lane & 15) * 16 + (g >> 1) * 256;
#pragma unroll
    for (int j = 0; j < 4; j += 2) {
      char* oj = ob + (size_t)((j + (g & 1) + rot) & 3) * 49152;
#pragma unroll
      for (int i = 0; i < 3; ++i) {
        u32x2 o[2];
#pragma unroll
        for (int jj = 0; jj < 2; ++jj) {
          const f32x4 gv = acc[i][j + jj], uv = acc[3 + i][j + jj];
          o[jj] = (u32x2){pack_bf16(fmaxf(gv[0], 0.f) * uv[0], fmaxf(gv[1], 0.f) * uv[1]), pack_bf16(fmaxf(gv[2], 0.f) * uv[2], fmaxf(gv[3], 0.f) * uv[3])};
        }
        const int E = 6 * wf + 2 * i;
        st16d(oj + (E >> 2) * 1024 + (E & 3) * 256, widen16(o[0], o[1]));
      }
    }
  }
  if (wf == 0 && pn < 8 && g == ((lane & 15) >> 2))
    __hip_atomic_store((gu64*)((u64*)(A.ws + WS_PT) + (size_t)(pn & 1) * L + t0 + (4 * wt + rot) * 16 + (lane & 15)),
                       ((u64)__float_as_uint(diag16(st2, lane)) << 32) | __float_as_uint(st1[0]), RLX_AGENT);
  WAIT_VM(0);
}

__device__ __forceinline__ void gemm2_loader(const Args& A, u32 lds0, int lane, int wave, u32 tag) {
  const int xcd = blockIdx.x & 7, idx = blockIdx.x >> 3;
  const int pm = (xcd & 3) * 8 + (idx & 7), pn = (xcd >> 2) * 4 + (idx >> 3);
  const int t0 = pm * 64, d0 = pn * 80;
  u32* flags = (u32*)(A.ws + WS_FL);
    const int lw = wave - 4;
    const char* srcP = A.ws + WS_HM + (size_t)(pm * 4 + lw) * 49152 + lane * 16;
    const char* srcW = A.ws + WS_WD + (size_t)pn * 245760 + lw * 5120 + lane * 16;
    const u32 dP = lds0 + lw * 4096, dW = lds0 + 16384 + lw * 5120;
    auto issueP = [&](u32 so) {
      glds16d(srcP, dP + so); glds16d(srcP + 1024, dP + so + 1024); glds16d(srcP + 2048, dP + so + 2048); glds16d(srcP + 3072, dP + so + 3072);
      srcP += 4096;
    };
    auto issueW = [&](u32 so) {
      glds16d(srcW, dW + so); glds16d(srcW + 1024, dW + so + 1024); glds16d(srcW + 2048, dW + so + 2048);
      glds16d(srcW + 3072, dW + so + 3072); glds16d(srcW + 4096, dW + so + 4096);
      srcW += 20480;
    };
    BARRIER();
    issueW(0); issueW(G2_STG); issueW(2 * G2_STG); issueW(3 * G2_STG);
#pragma unroll
    for (int k = 0; k < 3; ++k) {
      const int jj = lw + 4 * k < 10 ? lw + 4 * k : lw + 4;
      glds16d(A.ws + WS_HT + (size_t)ht_row(d0 + 8 * jj + (lane >> 3)) * 4096 + t0 * 2 + (lane & 7) * 16, UNI(lds0 + G2_OFF_H + jj * 1024));
    }
    BARRIER();
    if (wave == 4) wait_flags<1>(flags + FL_P + (pm >> 1) * 16 * 32, 16, tag, lane);
    BARRIER();
    issueP(0); issueP(G2_STG);
    WAIT_VM(4); BARRIER();
    issueP(2 * G2_STG); issueP(3 * G2_STG);
    WAIT_VM(8); BARRIER();
    u32 so = 0;
    auto issue = [&]() { issueP(so); issueW(so); so = so == 3 * G2_STG ? 0 : so + G2_STG; };
    issue(); WAIT_VM(13); BARRIER();
    for (int i = 2; i < G2_NK - 3; ++i) {
      issue();
      WAIT_VM(18); BARRIER();
    }
    WAIT_VM(9); BARRIER();
    WAIT_VM(0); BARRIER();
    BARRIER();
    BARRIER();
}

__device__ __forceinline__ void gemm2_compute(const Args& A, u32 lds0, int tid, int lane, int wave, u32 tag) {
  const int xcd = blockIdx.x & 7, idx = blockIdx.x >> 3;
  const int pm = (xcd & 3) * 8 + (idx & 7), pn = (xcd >> 2) * 4 + (idx >> 3);
  const int t0 = pm * 64, d0 = pn * 80;
  u32* flags = (u32*)(A.ws + WS_FL);
  BARRIER();
  if (tid == 0) {
    const int pm1 = (xcd & 1) * 8 + (idx & 7), pn1 = (xcd >> 1) * 4 + (idx >> 3);
    set_flag(flags + FL_P + (pm1 * 16 + pn1) * 32, tag);
  }
  const int wt = wave & 1, wk = wave >> 1;
  u32 pc = lds0 + (wt * 2) * 4096 + wk * 2048 + lane * 16, wc = lds0 + 16384 + wk * 2 * 5120 + lane * 16, so = 0;
  f32x4 acc[5][2];
#pragma unroll
  for (int i = 0; i < 5; ++i) { acc[i][0] = (f32x4){0.f, 0.f, 0.f, 0.f}; acc[i][1] = acc[i][0]; }
  bf16x8 pf[2][2][2], wf[2][2][5];
  auto rd = [&](int set) {
#pragma unroll
    for (int ks = 0; ks < 2; ++ks) {
      wf[set][ks][0] = *LDS(bf16x8, wc + ks * 5120);
#pragma unroll
      for (int j = 0; j < 2; ++j) pf[set][ks][j] = *LDS(bf16x8, pc + j * 4096 + ks * 1024);
#pragma unroll
      for (int i = 1; i < 5; ++i) wf[set][ks][i] = *LDS(bf16x8, wc + ks * 5120 + i * 1024);
    }
  };
  auto adv = [&]() {
    u32 dl = so == 3 * G2_STG ? (u32)(-3 * G2_STG) : (u32)G2_STG;
    so += dl;
    asm volatile("" : "+s"(dl));
    pc += dl; wc += dl;
  };
  auto mma = [&](int set) {
#pragma unroll
    for (int ks = 0; ks < 2; ++ks)
#pragma unroll
      for (int i = 0; i < 5; ++i)
#pragma unroll
        for (int j = 0; j < 2; ++j) acc[i][j] = mfma16(wf[set][ks][i], pf[set][ks][j], acc[i][j]);
  };
#define G2_SCHED() do { _Pragma("unroll") for (int k_ = 0; k_ < 4; ++k_) { SG(0x008, 1); SG(0x100, 1); SG(0x008, 1); SG(0x100, 1); SG(0x008, 1); SG(0x100, 1); SG(0x008, 1); } \
    SG(0x008, 1); SG(0x100, 1); SG(0x008, 1); SG(0x100, 1); SG(0x008, 2); } while (0)
  BARRIER();
  const int tl = wt * 32 + wk * 16;
  u64 pw[2];
#pragma unroll
  for (int k = 0; k < 2; ++k) pw[k] = __hip_atomic_load((gu64*)((u64*)(A.ws + WS_PT) + (size_t)k * L + t0 + tl + (lane & 15)), RLX_AGENT);
  BARRIER();
  rd(0); adv();
  WAIT_LGKM0();
#pragma unroll
  for (int i = 1; i < G2_NK - 1; i += 2) {
    BARRIER();
    SB(); rd(1); mma(0); adv(); G2_SCHED(); SB();
    WAIT_LGKM0();
    BARRIER();
    SB(); rd(0); mma(1); adv(); G2_SCHED(); SB();
    WAIT_LGKM0();
  }
  BARRIER();
  SB(); rd(1); mma(0); G2_SCHED(); SB();
  WAIT_LGKM0();
  mma(1);
  float rr;
  {
    float s1 = 0.f, s2 = 0.f;
#pragma unroll
    for (int k = 0; k < 2; ++k) { s1 += __uint_as_float((u32)pw[k]); s2 += __uint_as_float((u32)(pw[k] >> 32)); }
    const float mean = s1 * (1.f / D);
    const float var = fmaxf(s2 * (1.f / D) - mean * mean, 0.f);
    const float r = __builtin_amdgcn_rcpf(rbf(__builtin_amdgcn_sqrtf(rbf(rbf(var) + 1e-5f))));
    rr = r * r;
  }
  const int g = lane >> 4, q = (lane & 15) >> 2, p = lane & 3;
  s16x4 hv[5];
#pragma unroll
  for (int i = 0; i < 5; ++i) hv[i] = tr_read(lds0 + G2_OFF_H + (16 * i + 4 * g + q) * 128 + (tl + 4 * p) * 2);
  BARRIER();
  const u32 ex = lds0 + (wave ^ 2) * 5120 + lane * 16, exr = lds0 + wave * 5120 + lane * 16;
#pragma unroll
  for (int i = 0; i < 5; ++i) { if (wk) *LDS(f32x4, ex + i * 1024) = acc[i][0]; else *LDS(f32x4, ex + i * 1024) = acc[i][1]; }
  WAIT_LGKM0();
  BARRIER();
  f32x4 m[5];
#pragma unroll
  for (int i = 0; i < 5; ++i) m[i] = *LDS(f32x4, exr + i * 1024);
  SB();
  if (wk) {
#pragma unroll
    for (int i = 0; i < 5; ++i) m[i] += acc[i][1];
    asm volatile("" ::: "memory");
  } else {
#pragma unroll
    for (int i = 0; i < 5; ++i) m[i] += acc[i][0];
  }
#pragma unroll
  for (int i = 0; i < 5; ++i) m[i] *= rr;
  u32x2 o2[5];
#pragma unroll
  for (int i = 0; i < 5; ++i) o2[i] = (u32x2){pack_bf16(m[i][0], m[i][1]), pack_bf16(m[i][2], m[i][3])};
#pragma unroll
  for (int i = 0; i < 5; ++i)
    o2[i] = (u32x2){pack_bf16(bf2f((u16)hv[i][0]) + lo_bf(o2[i][0]), bf2f((u16)hv[i][1]) + hi_bf(o2[i][0])),
                    pack_bf16(bf2f((u16)hv[i][2]) + lo_bf(o2[i][1]), bf2f((u16)hv[i][3]) + hi_bf(o2[i][1]))};
  char* orow = (char*)A.out + (size_t)(t0 + tl + (lane & 15)) * 1280 + d0 * 2;
  *(u32x4*)(orow + (16 * (0 + (g & 1)) + 8 * (g >> 1)) * 2) = widen16(o2[0], o2[1]);
  *(u32x4*)(orow + (16 * (2 + (g & 1)) + 8 * (g >> 1)) * 2) = widen16(o2[2], o2[3]);
  *(u32x2*)(orow + (64 + 4 * g) * 2) = o2[4];
}

__global__ __launch_bounds__(512, 2) void s4_reglu_block(const Args A) {
  extern __shared__ __attribute__((aligned(16))) char smem[];
  const u32 lds0 = (u32)(size_t)(LAS char*)smem;
  const int tid = threadIdx.x, lane = tid & 63;
  const int wave = __builtin_amdgcn_readfirstlane(tid >> 6);
  const int xcd = blockIdx.x & 7, idx = blockIdx.x >> 3;
  asm volatile("" :: "s"(A.x), "s"(A.Are), "s"(A.Aim), "s"(A.Bre), "s"(A.Bim), "s"(A.Cre), "s"(A.Cim), "s"(A.logdt), "s"(A.Dskip),
               "s"(A.Wg), "s"(A.Wu), "s"(A.Wd), "s"(A.out), "s"(A.ws));
  u32* flags = (u32*)(A.ws + WS_FL);
  u32 tag7 = 0;
  if (wave == 7) {
    tag7 = __hip_atomic_load((gu32*)flags, RLX_AGENT) + 1u;
    if (lane == 0) { *LDS(u32, lds0 + OFF_TAG) = tag7; *LDS(u32, lds0 + OFF_TAG + 4) = 0u; set_flag(flags + FL_STARTED + blockIdx.x, tag7); }
  }
  u32 cpf = 0;
  if (idx >= 8 && idx < 12 && wave == 6) {
    const char* pc = (const char*)(__builtin_amdgcn_s_getpc() & ~(u64)127);
    const int l0 = (idx - 8) * 116 + lane;
    asm volatile("global_load_dword %0, %1, off" : "+v"(cpf) : "v"(pc + (size_t)l0 * 128) : "memory");
    asm volatile("global_load_dword %0, %1, off" : "+v"(cpf) : "v"(pc + (size_t)(lane < 52 ? l0 + 64 : l0) * 128) : "memory");
  }
  if (idx < 12) {
    int wtask;
    if (idx < 8) {
      const int task = xcd * 8 + idx;
      ln1_task(A, task, lds0, tid, lane, wave);
      wtask = task < 16 ? 32 + task : 48 + task - 16;
    } else {
      wtask = xcd * 4 + (idx - 8);
    }
    weight_task(A, wtask, lds0, tid, lane, wave);
    asm volatile("" :: "v"(cpf));
    if (blockIdx.x == 95 && wave == 7) {
      const u64 t0 = __builtin_amdgcn_s_memrealtime();
      for (;;) {
        bool ok = true;
#pragma unroll
        for (int k = 0; k < 4; ++k) ok &= __hip_atomic_load((gu32*)(flags + FL_STARTED + k * 64 + lane), RLX_AGENT) == tag7;
        if (__all(ok)) { if (lane == 0) set_flag(flags, tag7); break; }
        if (__builtin_amdgcn_s_memrealtime() - t0 > SPIN_TICKS) break;
        __builtin_amdgcn_s_sleep(8);
      }
    }
  } else {
    conv_role(A, xcd * 20 + idx - 12, lds0, tid, lane, wave, tag7);
  }
  WAIT_LGKM0();
  BARRIER();
  const u32 tag = UNI(*LDS(u32, lds0 + OFF_TAG));
  if (wave >= 4) {
    gemm1_loader(A, lds0, lane, wave, tag);
    gemm2_loader(A, lds0, lane, wave, tag);
  } else {
    __builtin_amdgcn_s_setprio(2);
    gemm1_compute(A, lds0, lane, wave);
    gemm2_compute(A, lds0, tid, lane, wave, tag);
  }
}

extern "C" void kernel_launch(void* const* d_in, const int* in_sizes, int n_in, void* d_out, int out_size, void* d_ws,
                              size_t ws_size, hipStream_t stream) {
  Args A;
  A.x = (const u16*)d_in[0];
  A.Are = (const u16*)d_in[1]; A.Aim = (const u16*)d_in[2]; A.Bre = (const u16*)d_in[3]; A.Bim = (const u16*)d_in[4];
  A.Cre = (const u16*)d_in[5]; A.Cim = (const u16*)d_in[6]; A.logdt = (const u16*)d_in[7]; A.Dskip = (const u16*)d_in[8];
  A.Wg = (const u16*)d_in[10]; A.Wu = (const u16*)d_in[11]; A.Wd = (const u16*)d_in[12];
  A.out = (u16*)d_out;
  A.ws = (char*)d_ws;
  (void)hipFuncSetAttribute((const void*)s4_reglu_block, hipFuncAttributeMaxDynamicSharedMemorySize, LDS_BYTES);
  s4_reglu_block<<<256, 512, LDS_BYTES, stream>>>(A);
}
```

```cpp
#include <hip/hip_runtime.h>
#include <hip/hip_bf16.h>
#include <stdint.h>

typedef unsigned short u16;
typedef unsigned u32;
typedef unsigned long long u64;
typedef short s16x4 __attribute__((ext_vector_type(4)));
typedef __bf16 bf16x8 __attribute__((ext_vector_type(8)));
typedef __bf16 bf16x2 __attribute__((ext_vector_type(2)));
typedef _Float16 f16x2 __attribute__((ext_vector_type(2)));
typedef _Float16 f16x8 __attribute__((ext_vector_type(8)));
typedef float f32x16 __attribute__((ext_vector_type(16)));
typedef float f32x4 __attribute__((ext_vector_type(4)));
typedef float f32x2 __attribute__((ext_vector_type(2)));
typedef u32 u32x4 __attribute__((ext_vector_type(4)));
typedef u32 u32x2 __attribute__((ext_vector_type(2)));
typedef __attribute__((address_space(1))) u32 gu32;
typedef __attribute__((address_space(1))) u64 gu64;
#define LAS __attribute__((address_space(3)))
#define LDS(T, a) ((LAS T*)(size_t)(a))

constexpr int L = 2048, D = 640, NS = 192, F = 1536;

constexpr size_t WS_UT = 0;
constexpr size_t WS_LN = 4u << 20;
constexpr size_t WS_HT = 8u << 20;
constexpr size_t WS_HM = 12u << 20;
constexpr size_t WS_W1 = 32u << 20;
constexpr size_t WS_WD = 36u << 20;
constexpr size_t WS_PT = 40u << 20;
constexpr size_t WS_FL = 52u << 20;
constexpr int FL_STARTED = 1024;
constexpr int FL_U = 2048;
constexpr int FL_W = FL_U + 64 * 32;
constexpr int FL_HE = FL_W + 96 * 32;
constexpr int FL_HL = FL_HE + 160 * 32;
constexpr int FL_P = FL_HL + 160 * 32;
constexpr int UT_ROW = 4160;

__host__ __device__ constexpr int ht_row(int d) { return ((d & 2) ? 320 : 0) + (d >> 2) * 2 + (d & 1); }
__host__ __device__ constexpr int ht_chan(int r) { return r < 320 ? (r >> 1) * 4 + (r & 1) : ((r - 320) >> 1) * 4 + 2 + (r & 1); }
static_assert(ht_chan(ht_row(639)) == 639 && ht_chan(ht_row(5)) == 5 && ht_row(2) == 320, "permutation");

struct Args {
  const u16 *x, *Are, *Aim, *Bre, *Bim, *Cre, *Cim, *logdt, *Dskip, *Wg, *Wu, *Wd;
  u16* out;
  char* ws;
};

__device__ __forceinline__ float bf2f(u16 v) { return __uint_as_float(((u32)v) << 16); }
__device__ __forceinline__ u32 pack_bf16(float lo, float hi) { return __builtin_bit_cast(u32, __builtin_convertvector((f32x2){lo, hi}, bf16x2)); }
__device__ __forceinline__ u32 pack_f16(float lo, float hi) { return __builtin_bit_cast(u32, __builtin_convertvector((f32x2){lo, hi}, f16x2)); }
__device__ __forceinline__ float rbf(float f) { return __uint_as_float(pack_bf16(f, f) << 16); }
__device__ __forceinline__ float lo_bf(u32 v) { return __uint_as_float(v << 16); }
__device__ __forceinline__ float hi_bf(u32 v) { return __uint_as_float(v & 0xffff0000u); }

#define WAIT_VM(n) asm volatile("s_waitcnt vmcnt(%0)" ::"n"(n) : "memory")
#define WAIT_LGKM0() do { asm volatile("" ::: "memory"); __builtin_amdgcn_s_waitcnt(0xC07F); asm volatile("" ::: "memory"); } while (0)
#define BARRIER() do { asm volatile("" ::: "memory"); __builtin_amdgcn_s_barrier(); asm volatile("" ::: "memory"); } while (0)
#define SB() __builtin_amdgcn_sched_barrier(0)
#define SG(m, n) __builtin_amdgcn_sched_group_barrier(m, n, 0)
#define RLX_AGENT __ATOMIC_RELAXED, __HIP_MEMORY_SCOPE_AGENT

__device__ __forceinline__ void glds16(const void* gsrc, u32 lds_dst) {
  u32 keep;
  asm volatile("s_mov_b32 %0, m0\n\ts_mov_b32 m0, %2\n\ts_nop 0\n\tglobal_load_lds_dwordx4 %1, off\n\ts_mov_b32 m0, %0"
               : "=&s"(keep) : "v"(gsrc), "s"(__builtin_amdgcn_readfirstlane(lds_dst)) : "memory");
}
__device__ __forceinline__ void glds16d(const void* gsrc, u32 lds_dst) {
  u32 keep;
  asm volatile("s_mov_b32 %0, m0\n\ts_mov_b32 m0, %2\n\ts_nop 0\n\tglobal_load_lds_dwordx4 %1, off sc1\n\ts_mov_b32 m0, %0"
               : "=&s"(keep) : "v"(gsrc), "s"(__builtin_amdgcn_readfirstlane(lds_dst)) : "memory");
}
#define UNI(v) ((u32)__builtin_amdgcn_readfirstlane((int)(v)))
__device__ __forceinline__ void st16d(void* p, u32x4 v) {
  asm volatile("global_store_dwordx4 %0, %1, off sc1\n\ts_nop 1" :: "v"(p), "v"(v) : "memory");
}
constexpr u64 SPIN_TICKS = 300000;
constexpr int POLL_SLEEP = 2;
template <int NL>
__device__ __forceinline__ void wait_flags(const u32* flags, int n, u32 tag, int lane) {
  const u64 t0 = __builtin_amdgcn_s_memrealtime();
  for (;;) {
    bool ok = true;
#pragma unroll
    for (int k = 0; k < NL; ++k) {
      const int i = k * 64 + lane;
      ok &= __hip_atomic_load((gu32*)(flags + (i < n ? i : n - 1) * 32), RLX_AGENT) == tag;
    }
    if (__all(ok)) break;
    if (__builtin_amdgcn_s_memrealtime() - t0 > SPIN_TICKS) break;
    __builtin_amdgcn_s_sleep(POLL_SLEEP);
  }
  asm volatile("" ::: "memory");
}
template <int NL>
__device__ __forceinline__ bool poll_once(const u32* flags, int n, u32 tag, int lane) {
  bool ok = true;
#pragma unroll
  for (int k = 0; k < NL; ++k) {
    const int i = k * 64 + lane;
    ok &= __hip_atomic_load((gu32*)(flags + (i < n ? i : n - 1) * 32), RLX_AGENT) == tag;
  }
  return __all(ok);
}
__device__ __forceinline__ void set_flag(u32* flag, u32 tag) { __hip_atomic_store((gu32*)flag, tag, RLX_AGENT); }

__device__ __forceinline__ s16x4 tr_read(u32 addr) { return __builtin_amdgcn_ds_read_tr16_b64_v4i16(LDS(s16x4, addr)); }
__device__ __forceinline__ u32x4 tr_read2(u32 a0, u32 a1) {
  const s16x4 a = tr_read(a0), b = tr_read(a1);
  return __builtin_bit_cast(u32x4, __builtin_shufflevector(a, b, 0, 1, 2, 3, 4, 5, 6, 7));
}
__device__ __forceinline__ bf16x8 tr_frag(u32 a0, u32 a1) { return __builtin_bit_cast(bf16x8, tr_read2(a0, a1)); }
__device__ __forceinline__ f32x4 mfma16(bf16x8 a, bf16x8 b, f32x4 c) { return __builtin_amdgcn_mfma_f32_16x16x32_bf16(a, b, c, 0, 0, 0); }
#define ONES_BF16 __builtin_bit_cast(bf16x8, (u32x4){0x3f803f80u, 0x3f803f80u, 0x3f803f80u, 0x3f803f80u})
__device__ __forceinline__ float diag16(f32x4 a, int lane) { const int r = lane & 3; return r == 0 ? a[0] : r == 1 ? a[1] : r == 2 ? a[2] : a[3]; }

template <int CTRL> __device__ __forceinline__ float dpp_f(float v) {
  return __builtin_bit_cast(float, __builtin_amdgcn_update_dpp(0, __builtin_bit_cast(int, v), CTRL, 0xf, 0xf, true));
}
__device__ __forceinline__ float wave_max(float v) {
  v = fmaxf(v, dpp_f<0xB1>(v));
  v = fmaxf(v, dpp_f<0x4E>(v));
  v = fmaxf(v, dpp_f<0x141>(v));
  v = fmaxf(v, dpp_f<0x140>(v));
  { auto r = __builtin_amdgcn_permlane16_swap(__float_as_uint(v), __float_as_uint(v), false, false); v = fmaxf(__uint_as_float(r[0]), __uint_as_float(r[1])); }
  { auto r = __builtin_amdgcn_permlane32_swap(__float_as_uint(v), __float_as_uint(v), false, false); v = fmaxf(__uint_as_float(r[0]), __uint_as_float(r[1])); }
  return v;
}
__device__ __forceinline__ u32x4 widen16(u32x2 a, u32x2 b) {
  const auto s0 = __builtin_amdgcn_permlane16_swap(a[0], b[0], false, false);
  const auto s1 = __builtin_amdgcn_permlane16_swap(a[1], b[1], false, false);
  return (u32x4){s0[0], s1[0], s0[1], s1[1]};
}

constexpr int LDS_BYTES = 163840;
constexpr int OFF_TAG = LDS_BYTES - 16;
constexpr int C_ROW = 272;
constexpr int C_SCR = 60 * C_ROW;
constexpr int C_KCH = 4608;
constexpr int C_UCP = 4160, C_UCH = 2 * C_UCP;
constexpr int F_OFF_P = 4 * C_SCR;
constexpr int F_OFF_S = F_OFF_P + 9216;
constexpr int F_OFF_X = F_OFF_S + 16;
constexpr int F_OFF_K = 74752;
constexpr int F_OFF_U = F_OFF_K + 4 * C_KCH;
constexpr int F_OFF_L = F_OFF_U + 4 * C_UCH;
constexpr int LN_RS = 1344;
constexpr int G1_STG = 20480, G1_NS = 7, G1_NK = 20;
constexpr int G2_STG = 36864, G2_NK = 12;
constexpr int G2_OFF_H = G2_STG * 4;
static_assert(F_OFF_L + 16384 <= OFF_TAG && G1_STG * G1_NS <= OFF_TAG && G2_OFF_H + 10240 <= OFF_TAG, "LDS");

__device__ __forceinline__ void ln1_task(const Args& A, int task, u32 lds0, int tid, int lane, int wave) {
  char* uT = A.ws + WS_UT;
  const int t0 = task * 32;
  const u32 tX = lds0, tU = lds0 + 40960;
#pragma unroll
  for (int k = 0; k < 5; ++k) {
    const int P = (wave * 5 + k) * 64 + lane, row = P / 80, chk = (P % 80) ^ (row & 15);
    glds16((const char*)A.x + (size_t)(t0 + row) * 1280 + chk * 16, UNI(tX + (wave * 5 + k) * 1024));
  }
  WAIT_VM(0);
  BARRIER();
  {
    const int row = 4 * wave + (lane >> 4);
    f32x2 xv[20];
#pragma unroll
    for (int k = 0; k < 5; ++k) {
      const u32x4 w = *LDS(u32x4, tX + row * 1280 + (((lane & 15) + 16 * k) ^ (row & 15)) * 16);
#pragma unroll
      for (int e = 0; e < 4; ++e) xv[4 * k + e] = (f32x2){lo_bf(w[e]), hi_bf(w[e])};
    }
    f32x2 a1 = xv[0] + xv[1], a2 = xv[0] * xv[0], b1 = xv[2] + xv[3], b2 = xv[1] * xv[1];
    a2 = __builtin_elementwise_fma(xv[2], xv[2], a2); b2 = __builtin_elementwise_fma(xv[3], xv[3], b2);
#pragma unroll
    for (int k = 4; k < 20; k += 2) {
      a1 += xv[k]; b1 += xv[k + 1];
      a2 = __builtin_elementwise_fma(xv[k], xv[k], a2); b2 = __builtin_elementwise_fma(xv[k + 1], xv[k + 1], b2);
    }
    float s1 = (a1.x + a1.y) + (b1.x + b1.y), s2 = (a2.x + a2.y) + (b2.x + b2.y);
    s1 += dpp_f<0xB1>(s1); s2 += dpp_f<0xB1>(s2);
    s1 += dpp_f<0x4E>(s1); s2 += dpp_f<0x4E>(s2);
    s1 += dpp_f<0x141>(s1); s2 += dpp_f<0x141>(s2);
    s1 += dpp_f<0x140>(s1); s2 += dpp_f<0x140>(s2);
    const float mean = s1 * (1.f / D);
    const float var = fmaxf(s2 * (1.f / D) - mean * mean, 0.f);
    const float mb = rbf(mean), sd = rbf(__builtin_amdgcn_sqrtf(rbf(rbf(var) + 1e-5f)));
    const float ri = __builtin_amdgcn_rcpf(sd), mi = -mb * ri;
    if ((lane & 15) == 0)
      __hip_atomic_store((gu64*)((u64*)(A.ws + WS_LN) + t0 + row), ((u64)__float_as_uint(mb) << 32) | __float_as_uint(sd), RLX_AGENT);
#pragma unroll
    for (int k = 0; k < 5; ++k) {
      u32x4 o;
#pragma unroll
      for (int e = 0; e < 4; ++e) {
        const f32x2 u = __builtin_elementwise_fma(xv[4 * k + e], (f32x2){ri, ri}, (f32x2){mi, mi});
        o[e] = pack_f16(u.x, u.y);
      }
      *LDS(u32x4, tU + row * LN_RS + ((lane & 15) + 16 * k) * 16) = o;
    }
  }
  WAIT_LGKM0();
  BARRIER();
  {
    const int g = lane >> 4, q = (lane & 15) >> 2, p = lane & 3;
    u32x4 ov[5];
#pragma unroll
    for (int k = 0; k < 5; ++k) {
      const u32 a = tU + (8 * g + q) * LN_RS + ((wave * 5 + k) * 16 + 4 * p) * 2;
      ov[k] = tr_read2(a, a + 4 * LN_RS);
    }
#pragma unroll
    for (int k = 0; k < 5; ++k) st16d(uT + (size_t)((wave * 5 + k) * 16 + (lane & 15)) * UT_ROW + 32 + (t0 + 8 * g) * 2, ov[k]);
  }
  WAIT_VM(0);
  BARRIER();
  if (tid == 0) set_flag((u32*)(A.ws + WS_FL) + FL_U + task * 32, *LDS(u32, lds0 + OFF_TAG));
}

__device__ __forceinline__ void weight_task(const Args& A, int wtask, u32 lds0, int tid, int lane, int wave) {
  const int g = lane >> 4, q = (lane & 15) >> 2, p = lane & 3;
  if (wtask < 48) {
    const u16* W = wtask < 24 ? A.Wg : A.Wu;
    const int c0 = (wtask % 24) * 64;
    {
      const int row = 8 * wave + (lane >> 3), chk = (lane & 7) ^ ((row & 3) << 1);
      const char* src = (const char*)W + c0 * 2 + chk * 16;
#pragma unroll
      for (int jj = 0; jj < 10; ++jj)
        glds16(src + (size_t)ht_chan(row + 64 * jj) * 3072, UNI(lds0 + (wave + 8 * jj) * 1024));
    }
    WAIT_VM(0);
    BARRIER();
    const int tl = wave & 3, ks0 = (wave >> 2) * 10;
    const u32 a = lds0 + (32 * ks0 + 8 * g + q) * 128 + (((2 * tl + (p >> 1)) ^ (q << 1)) * 16) + 8 * (p & 1);
    bf16x8 fr[10];
    f32x4 cs = {0.f, 0.f, 0.f, 0.f};
#pragma unroll
    for (int k = 0; k < 10; ++k) { fr[k] = tr_frag(a + k * 4096, a + k * 4096 + 512); cs = mfma16(ONES_BF16, fr[k], cs); }
    WAIT_LGKM0();
    BARRIER();
    if (lane < 16) *LDS(float, lds0 + (wave * 16 + lane) * 4) = cs[0];
    WAIT_LGKM0();
    BARRIER();
    const float cm = (*LDS(float, lds0 + (tl * 16 + (lane & 15)) * 4) + *LDS(float, lds0 + ((tl + 4) * 16 + (lane & 15)) * 4)) * (1.f / D);
    const int f = c0 + 16 * tl, pn = f / 96, fi = (f % 96) / 16;
    const int slot = (fi / 3) * 6 + (wtask < 24 ? 0 : 3) + fi % 3;
    char* dst = A.ws + WS_W1 + (((size_t)pn * 20 + ks0) * 12 + slot) * 1024 + lane * 16;
#pragma unroll
    for (int k = 0; k < 10; ++k) {
      const u32x4 w = __builtin_bit_cast(u32x4, fr[k]);
      u32x4 o;
#pragma unroll
      for (int e = 0; e < 4; ++e) o[e] = pack_bf16(lo_bf(w[e]) - cm, hi_bf(w[e]) - cm);
      st16d(dst + (size_t)k * 12288, o);
    }
  } else {
    const int ks = wtask - 48;
#pragma unroll
    for (int jj = 0; jj < 5; ++jj) {
      const int P = 64 * (wave + 8 * jj) + lane, row = P / 80, chk = P % 80;
      glds16((const char*)A.Wd + (size_t)(32 * ks + row) * 1280 + chk * 16, UNI(lds0 + (wave + 8 * jj) * 1024));
    }
    WAIT_VM(0);
    BARRIER();
#pragma unroll
    for (int jj = 0; jj < 5; ++jj) {
      const int fr = wave + 8 * jj;
      const u32 a = lds0 + (8 * g + q) * 1280 + (16 * fr + 4 * p) * 2;
      st16d(A.ws + WS_WD + (((size_t)(fr / 5) * 48 + ks) * 5 + fr % 5) * 1024 + lane * 16, tr_read2(a, a + 4 * 1280));
    }
  }
  WAIT_VM(0);
  BARRIER();
  if (tid == 0) set_flag((u32*)(A.ws + WS_FL) + FL_W + wtask * 32, *LDS(u32, lds0 + OFF_TAG));
}

struct cplx { f32x2 v, r; };
__device__ __forceinline__ cplx mk(f32x2 v) { return cplx{v, (f32x2){-v.y, v.x}}; }
__device__ __forceinline__ f32x2 cmul(f32x2 a, const cplx& w) {
  const f32x2 t = (f32x2){a.x, a.x} * w.v;
  return __builtin_elementwise_fma((f32x2){a.y, a.y}, w.r, t);
}
__device__ __forceinline__ f32x2 cexp(float zr, float zi, float l, float sgn) {
  const float m = __builtin_amdgcn_exp2f(zr * l * 1.44269504088896341f);
  const float a = __builtin_amdgcn_fractf(zi * l * 0.15915494309189535f);
  return (f32x2){m * __builtin_amdgcn_cosf(a), sgn * m * __builtin_amdgcn_sinf(a)};
}
__device__ __forceinline__ u32 pkh(f32x2 v) { return __builtin_bit_cast(u32, __builtin_convertvector(v, f16x2)); }
__device__ __forceinline__ u32 cmulh(u32 a, u32 b) {
  u32 t, r;
  asm("v_pk_mul_f16 %0, %1, %2 op_sel:[0,0] op_sel_hi:[0,1]" : "=v"(t) : "v"(a), "v"(b));
  asm("v_pk_fma_f16 %0, %1, %2, %3 op_sel:[1,1,0] op_sel_hi:[1,0,1] neg_lo:[0,1,0]" : "=v"(r) : "v"(a), "v"(b), "v"(t));
  return r;
}
#define SET_M0(base) asm volatile("s_mov_b32 m0, %0\n\ts_nop 0" ::"s"(base) : "memory")
#define WR4(row, a, b, c, d)                                                                                    \
  asm volatile("ds_write_addtid_b32 %0 offset:%4\n\tds_write_addtid_b32 %1 offset:%4+272\n\t"                    \
               "ds_write_addtid_b32 %2 offset:%4+544\n\tds_write_addtid_b32 %3 offset:%4+816"                    \
               ::"v"(a), "v"(b), "v"(c), "v"(d), "n"((row) * C_ROW) : "memory")

__device__ __forceinline__ void conv_role(const Args& A, int grp, u32 lds0, int tid, int lane, int wave, u32 tag) {
  const int ch = (wave >> 2) * 2 + ((wave >> 1) & 1), hf = wave & 1, d0 = grp * 4, d = d0 + ch;
  const u32 sb = UNI(lds0 + ch * C_SCR);
  const int r32 = lane & 31, h32 = lane >> 5;
  const int j = lane & 15, g = lane >> 4;
  {
    const u64 q0 = (u64)A.Are, q1 = (u64)A.Aim, q2 = (u64)A.Bre, q3 = (u64)A.Bim, q4 = (u64)A.Cre, q5 = (u64)A.Cim;
#pragma unroll
    for (int rep = 0; rep < 2; ++rep) {
      if (rep == 1 && wave != 0) break;
      const int i = rep ? 8 : wave;
      const int b = 1024 * i + 16 * lane, a = b / 1536, o = b - a * 1536;
      u64 src = +q0;
      src = a == 1 ? +q1 : src; src = a == 2 ? +q2 : src; src = a == 3 ? +q3 : src; src = a == 4 ? +q4 : src; src = a == 5 ? +q5 : src;
      glds16((const char*)(src + (u64)d0 * 384 + o), UNI(lds0 + F_OFF_P + i * 1024));
    }
  }
  const u32 wdt = ((const u32*)A.logdt)[d >> 1], wds = ((const u32*)A.Dskip)[d >> 1];
  const float dt = rbf(__expf((d & 1) ? hi_bf(wdt) : lo_bf(wdt)));
  WAIT_VM(0);
  BARRIER();
  float zr[3], zi[3];
  const u32 pl = lds0 + F_OFF_P + ch * 384 + lane * 2;
#pragma unroll
  for (int r = 0; r < 3; ++r) {
    zr[r] = dt * bf2f(*LDS(u16, pl + r * 128));
    zi[r] = dt * bf2f(*LDS(u16, pl + 1536 + r * 128));
  }
  u32 an[3][11];
  if (hf == 0) {
    f32x2 cb[3];
    float bound = 0.f;
#pragma unroll
    for (int r = 0; r < 3; ++r) {
      const float br = dt * bf2f(*LDS(u16, pl + 2 * 1536 + r * 128)), bi = dt * bf2f(*LDS(u16, pl + 3 * 1536 + r * 128));
      const float cr = bf2f(*LDS(u16, pl + 4 * 1536 + r * 128)), ci = bf2f(*LDS(u16, pl + 5 * 1536 + r * 128));
      cb[r] = (f32x2){cr * br - ci * bi, -(cr * bi + ci * br)};
      const float grow = __builtin_amdgcn_exp2f(fmaxf(zr[r], 0.f) * (2064.f * 1.44269504f));
      bound = fmaxf(bound, (fabsf(cb[r].x) + fabsf(cb[r].y)) * grow);
    }
    bound = wave_max(bound);
    int ex = (int)((__float_as_uint(bound) >> 23) & 255);
    ex = ex < 20 ? 20 : (ex > 240 ? 240 : ex);
    const float scale = __uint_as_float((u32)(267 - ex) << 23);
    if (lane == 0) *LDS(float, lds0 + F_OFF_S + ch * 4) = __uint_as_float((u32)(ex - 5) << 23);
#pragma unroll
    for (int r = 0; r < 3; ++r) {
      const cplx w1 = mk(cexp(zr[r], zi[r], 1.f, -1.f));
      const cplx w512 = mk(cexp(zr[r], zi[r], 512.f, -1.f));
      const cplx w2 = mk(cmul(w1.v, w1));
      const f32x2 w3 = cmul(w2.v, w1);
      const cplx w4 = mk(cmul(w2.v, w2));
      const f32x2 w1024 = cmul(w512.v, w512), w1536 = cmul(w1024, w512);
      const f32x2 P0 = cb[r] * scale, P4 = cmul(P0, w4), P8 = cmul(P4, w4), P12 = cmul(P8, w4), P16 = cmul(P12, w4);
      an[r][0] = pkh(P0); an[r][1] = pkh(P4); an[r][2] = pkh(P8); an[r][3] = pkh(P12); an[r][4] = pkh(P16);
      an[r][5] = pkh(w1.v); an[r][6] = pkh(w2.v); an[r][7] = pkh(w3);
      an[r][8] = pkh(w512.v); an[r][9] = pkh(w1024); an[r][10] = pkh(w1536);
    }
  } else {
#pragma unroll
    for (int r = 0; r < 3; ++r) {
      const cplx w16 = mk(cexp(zr[r], zi[r], 16.f, 1.f));
      const cplx w128 = mk(cexp(zr[r], zi[r], 128.f, 1.f));
      const cplx w32 = mk(cmul(w16.v, w16));
      const f32x2 w48 = cmul(w32.v, w16);
      const f32x2 w64 = cmul(w32.v, w32);
      const f32x2 w192 = cmul(w64, w128), w256 = cmul(w128.v, w128);
      const f32x2 w320 = cmul(w192, w128), w384 = cmul(w256, w128), w448 = cmul(w320, w128);
      an[r][0] = pkh(w64); an[r][1] = pkh(w128.v); an[r][2] = pkh(w192); an[r][3] = pkh(w256);
      an[r][4] = pkh(w320); an[r][5] = pkh(w384); an[r][6] = pkh(w448);
      an[r][7] = pkh(w16.v); an[r][8] = pkh(w32.v); an[r][9] = pkh(w48); an[r][10] = 0x00003c00u;
    }
  }
  u32 arow;
  {
    const int hr = (r32 >> 2) & 1, rho = (r32 & 3) + 4 * (r32 >> 3);
    const int row = rho <= 8 ? rho + 8 * hr : rho <= 11 ? rho + 8 + 2 * hr : rho <= 13 ? rho + 10 + hr : rho + 11 + hr;
    arow = sb + row * C_ROW + h32 * 16 + hf * 128;
  }
  const u32 brow = sb + (28 + r32) * C_ROW + h32 * 16 + hf * 128;
  f32x16 kacc;
#pragma unroll
  for (int i = 0; i < 16; ++i) kacc[i] = 0.f;
  SET_M0(sb);
#pragma unroll
  for (int r = 0; r < 3; ++r) {
    u32 gv[2][4];
    auto grp4 = [&](int gi, u32 (&o)[4]) {
      const u32* a = an[r];
      if (hf == 0) {
        if (gi < 4) { o[0] = a[gi]; o[1] = cmulh(a[gi], a[5]); o[2] = cmulh(a[gi], a[6]); o[3] = cmulh(a[gi], a[7]); }
        else if (gi == 4) { o[0] = a[4]; o[1] = cmulh(a[0], a[8]); o[2] = cmulh(a[1], a[8]); o[3] = cmulh(a[2], a[8]); }
        else if (gi == 5) { o[0] = cmulh(a[3], a[8]); o[1] = cmulh(a[4], a[8]); o[2] = cmulh(a[0], a[9]); o[3] = cmulh(a[2], a[9]); }
        else if (gi == 6) { o[0] = cmulh(a[4], a[9]); o[1] = cmulh(a[0], a[10]); o[2] = cmulh(a[2], a[10]); o[3] = cmulh(a[4], a[10]); }
      } else {
        if (gi == 0) { o[0] = a[10]; o[1] = a[7]; o[2] = a[8]; o[3] = a[9]; }
        else { o[0] = a[gi - 1]; o[1] = cmulh(a[gi - 1], a[7]); o[2] = cmulh(a[gi - 1], a[8]); o[3] = cmulh(a[gi - 1], a[9]); }
      }
    };
    if (hf == 0) {
      grp4(0, gv[0]);
#pragma unroll
      for (int gi = 0; gi < 7; ++gi) {
        if (gi < 6) grp4(gi + 1, gv[(gi + 1) & 1]);
        SB();
        WR4(4 * gi, gv[gi & 1][0], gv[gi & 1][1], gv[gi & 1][2], gv[gi & 1][3]);
        SB();
      }
    } else {
      grp4(0, gv[0]);
#pragma unroll
      for (int gi = 0; gi < 8; ++gi) {
        if (gi < 7) grp4(gi + 1, gv[(gi + 1) & 1]);
        SB();
        WR4(28 + 4 * gi, gv[gi & 1][0], gv[gi & 1][1], gv[gi & 1][2], gv[gi & 1][3]);
        SB();
      }
    }
    WAIT_LGKM0();
    BARRIER();
    f16x8 fa[4], fb[4];
#pragma unroll
    for (int k = 0; k < 4; ++k) { fa[k] = *LDS(f16x8, arow + k * 32); fb[k] = *LDS(f16x8, brow + k * 32); }
    WAIT_LGKM0();
    BARRIER();
#pragma unroll
    for (int k = 0; k < 4; ++k) kacc = __builtin_amdgcn_mfma_f32_32x32x16_f16(fa[k], fb[k], kacc, 0, 0, 0);
  }
  {
    const u32 ex = sb + hf * 4096 + lane * 16, exr = sb + (hf ^ 1) * 4096 + lane * 16;
    if (hf == 0) {
      *LDS(f32x4, ex) = (f32x4){kacc[12], kacc[13], kacc[14], kacc[15]};
    } else {
      *LDS(f32x4, ex) = (f32x4){kacc[0], kacc[1], kacc[2], kacc[3]};
      *LDS(f32x4, ex + 1024) = (f32x4){kacc[4], kacc[5], kacc[6], kacc[7]};
      *LDS(f32x4, ex + 2048) = (f32x4){kacc[8], kacc[9], kacc[10], kacc[11]};
    }
    WAIT_LGKM0();
    BARRIER();
    const u32 kb = lds0 + F_OFF_K + ch * C_KCH + 4080 - 32 * r32 - 16 * h32;
    auto put = [&](int base, float e0, float e1, float e2, float e3, float e4, float e5, float e6, float e7) {
      *LDS(u32x4, kb - 2 * base) = (u32x4){pack_f16(e7, e6), pack_f16(e5, e4), pack_f16(e3, e2), pack_f16(e1, e0)};
    };
    if (hf == 0) {
      const f32x4 o0 = *LDS(f32x4, exr), o1 = *LDS(f32x4, exr + 1024), o2 = *LDS(f32x4, exr + 2048);
      float v[12];
#pragma unroll
      for (int i = 0; i < 4; ++i) {
        v[i] = (kacc[i] + o0[i]) * (1.f / 256.f); v[4 + i] = (kacc[4 + i] + o1[i]) * (1.f / 256.f); v[8 + i] = (kacc[8 + i] + o2[i]) * (1.f / 256.f);
      }
      const bool far = r32 >= 16;
      put(0, v[0], far ? v[0] : v[1], v[2], far ? v[4] : v[3], v[4], far ? v[4] : v[5], v[6], far ? v[8] : v[7]);
      put(512, v[9], v[9], v[9], v[10], v[10], v[10], v[11], v[11]);
    } else {
      const f32x4 o = *LDS(f32x4, exr);
      const float v12 = (kacc[12] + o[0]) * (1.f / 256.f), v13 = (kacc[13] + o[1]) * (1.f / 256.f),
                  v14 = (kacc[14] + o[2]) * (1.f / 256.f), v15 = (kacc[15] + o[3]) * (1.f / 256.f);
      put(1024, v12, v12, v12, v12, h32 ? v13 : v12, v13, v13, v13);
      put(1536, v14, v14, v14, v14, h32 ? v15 : v14, v15, v15, v15);
    }
  }
  if (hf == 1 && lane < 32) *LDS(u32x4, lds0 + F_OFF_K + ch * C_KCH + 4096 + lane * 16) = (u32x4){0u, 0u, 0u, 0u};
  if (wave == 0 && lane < 10) *LDS(u32, lds0 + F_OFF_X + lane * 4) = 0u;
  if (wave == 7) wait_flags<1>((const u32*)(A.ws + WS_FL) + FL_U, 64, tag, lane);
  WAIT_LGKM0();
  BARRIER();
  {
    const char* up = A.ws + WS_UT + (size_t)d * UT_ROW + lane * 16 + hf * 2;
    const u32 dst = UNI(lds0 + F_OFF_U + ch * C_UCH + hf * C_UCP);
#pragma unroll
    for (int k = 0; k < 4; ++k) glds16d(up + k * 1024, dst + k * 1024);
    if (lane < 2) glds16d(up + 4096, dst + 4096);
    glds16d(A.ws + WS_LN + wave * 2048 + lane * 16, UNI(lds0 + F_OFF_L + wave * 2048));
    glds16d(A.ws + WS_LN + wave * 2048 + 1024 + lane * 16, UNI(lds0 + F_OFF_L + wave * 2048 + 1024));
  }
  WAIT_VM(0);
  if (lane < 8) {
    const u32 zb = lds0 + F_OFF_U + ch * C_UCH + hf * C_UCP;
    if (lane < 7 || hf == 0) *LDS(u32, zb + lane * 4) = 0u; else *LDS(u16, zb + 28) = (u16)0;
  }
  WAIT_LGKM0();
  BARRIER();
  const int cc = (1 + j) & 1;
  const u32 aa = lds0 + F_OFF_U + ch * C_UCH + cc * C_UCP + 2 * (1 + j + 8 * g - cc) + hf * 256;
  const u32 ba = lds0 + F_OFF_K + ch * C_KCH + 32 * j + 16 * g + hf * 256;
  f32x4 acc[8];
#pragma unroll
  for (int i = 0; i < 8; ++i) acc[i] = (f32x4){0.f, 0.f, 0.f, 0.f};
  if (wave < 4) __builtin_amdgcn_s_setprio(2);
  {
    f16x8 Af[2][8], Bf[2][8];
    u32 am[4];
#pragma unroll
    for (int k = 0; k < 4; ++k) { am[k] = aa + 1024 * k; asm volatile("" : "+v"(am[k])); }
    auto rd = [&](int set, int r) {
#pragma unroll
      for (int m = 0; m < 8; ++m) {
        const LAS u32* ap = LDS(const u32, am[m >> 1] + 64 * r + 512 * (m & 1));
        Af[set][m] = __builtin_bit_cast(f16x8, (u32x4){ap[0], ap[1], ap[2], ap[3]});
        Bf[set][m] = *LDS(f16x8, ba + 64 * r + 512 * (7 - m));
      }
    };
    auto mm = [&](int set) {
#pragma unroll
      for (int sdx = 0; sdx < 8; ++sdx)
#pragma unroll
        for (int m = 0; m <= sdx; ++m) acc[sdx] = __builtin_amdgcn_mfma_f32_16x16x32_f16(Af[set][m], Bf[set][sdx - m], acc[sdx], 0, 0, 0);
    };
#define C_SCHED() do { _Pragma("unroll") for (int k_ = 0; k_ < 24; ++k_) { SG(0x008, 1); SG(0x100, 1); } SG(0x008, 12); } while (0)
    rd(0, 0);
    SB(); rd(1, 1); mm(0); C_SCHED(); SB();
    rd(0, 2); mm(1); C_SCHED(); SB();
    rd(1, 3); mm(0); C_SCHED(); SB();
    mm(1);
  }
  {
    const u32 ex = lds0 + wave * 4096 + lane * 16, exr = lds0 + (wave ^ 1) * 4096 + lane * 16;
    if (hf == 0) {
#pragma unroll
      for (int i = 0; i < 4; ++i) *LDS(f32x4, ex + i * 1024) = acc[4 + i];
    } else {
#pragma unroll
      for (int i = 0; i < 4; ++i) *LDS(f32x4, ex + i * 1024) = acc[i];
    }
    WAIT_LGKM0();
    if (lane == 0) *LDS(volatile u32, lds0 + F_OFF_X + wave * 4) = 1u;
    const float it = *LDS(float, lds0 + F_OFF_S + ch * 4);
    const float dsk = (d & 1) ? hi_bf(wds) : lo_bf(wds);
    u32x2 dv[4], xv[4];
#pragma unroll
    for (int i = 0; i < 4; ++i) {
      const int t = 16 * (16 * (4 * hf + i) + 15 - j) + 4 * g;
      const u32x2 uw = *LDS(u32x2, lds0 + F_OFF_U + ch * C_UCH + 2 * (16 + t));
      const u32 w0 = uw[0], w1 = uw[1];
      const f16x2 u01 = __builtin_bit_cast(f16x2, w0), u23 = __builtin_bit_cast(f16x2, w1);
      const float uf[4] = {(float)u01[0], (float)u01[1], (float)u23[0], (float)u23[1]};
      dv[i] = (u32x2){pack_bf16(uf[0] * dsk, uf[1] * dsk), pack_bf16(uf[2] * dsk, uf[3] * dsk)};
      const f32x4 l01 = *LDS(f32x4, lds0 + F_OFF_L + t * 8), l23 = *LDS(f32x4, lds0 + F_OFF_L + t * 8 + 16);
      xv[i] = (u32x2){pack_bf16(fmaf(uf[0], l01[0], l01[1]), fmaf(uf[1], l01[2], l01[3])), pack_bf16(fmaf(uf[2], l23[0], l23[1]), fmaf(uf[3], l23[2], l23[3]))};
    }
#pragma unroll
    for (int i = 0; i < 4; ++i) asm volatile("" : "+v"(dv[i]), "+v"(xv[i]));
    while (*LDS(volatile u32, lds0 + F_OFF_X + (wave ^ 1) * 4) == 0u) __builtin_amdgcn_s_sleep(1);
    asm volatile("" ::: "memory");
    f32x4 y[4];
#pragma unroll
    for (int i = 0; i < 4; ++i) y[i] = *LDS(f32x4, exr + i * 1024);
    SB();
    if (hf) {
#pragma unroll
      for (int i = 0; i < 4; ++i) y[i] += acc[4 + i];
      asm volatile("" ::: "memory");
    } else {
#pragma unroll
      for (int i = 0; i < 4; ++i) y[i] += acc[i];
    }
#pragma unroll
    for (int i = 0; i < 4; ++i) y[i] *= it;
    u32x2 w[4];
#pragma unroll
    for (int i = 0; i < 4; ++i) w[i] = (u32x2){pack_bf16(y[i][0], y[i][1]), pack_bf16(y[i][2], y[i][3])};
#pragma unroll
    for (int i = 0; i < 4; ++i)
      w[i] = (u32x2){pack_bf16(lo_bf(w[i][0]) + lo_bf(dv[i][0]), hi_bf(w[i][0]) + hi_bf(dv[i][0])), pack_bf16(lo_bf(w[i][1]) + lo_bf(dv[i][1]), hi_bf(w[i][1]) + hi_bf(dv[i][1]))};
    u32x2 hv[4];
#pragma unroll
    for (int i = 0; i < 4; ++i)
      hv[i] = (u32x2){pack_bf16(lo_bf(xv[i][0]) + lo_bf(w[i][0]), hi_bf(xv[i][0]) + hi_bf(w[i][0])), pack_bf16(lo_bf(xv[i][1]) + lo_bf(w[i][1]), hi_bf(xv[i][1]) + hi_bf(w[i][1]))};
    char* hrow = A.ws + WS_HT + (size_t)ht_row(d) * 4096;
#pragma unroll
    for (int i = 0; i < 4; i += 2) st16d(hrow + (16 * (16 * (4 * hf + i + (g & 1)) + 15 - j) + 8 * (g >> 1)) * 2, widen16(hv[i], hv[i + 1]));
  }
  __builtin_amdgcn_s_setprio(0);
  if (wave < 4) {
    WAIT_VM(0);
    if (lane == 0 && __hip_atomic_fetch_add(LDS(u32, lds0 + F_OFF_X + 32), 1u, __ATOMIC_RELAXED, __HIP_MEMORY_SCOPE_WORKGROUP) == 3u)
      set_flag((u32*)(A.ws + WS_FL) + FL_HE + grp * 32, *LDS(u32, lds0 + OFF_TAG));
  }
}

__device__ __forceinline__ void gemm1_loader(const Args& A, u32 lds0, int lane, int wave, u32 tag) {
  const bool conv_wg = (blockIdx.x >> 3) >= 12;
  const int xcd = blockIdx.x & 7, idx = blockIdx.x >> 3;
  const int pm = (xcd & 1) * 8 + (idx & 7), pn = (xcd >> 1) * 4 + (idx >> 3);
  const int t0 = pm * 128;
  const u32* flags = (const u32*)(A.ws + WS_FL);
    const int lw = wave - 4;
    const int k0 = 5 * (pn & 1);
    int kH = 0, kW = 0;
    const int row = 8 * lw + (lane >> 4);
    const int chk = (lane & 15) ^ (((row & 3) << 1) | (((row >> 3) & 1) << 3));
    const char* srcH = A.ws + WS_HT + (size_t)(row + 32 * k0) * 4096 + t0 * 2 + chk * 16;
    const char* srcW = A.ws + WS_W1 + (size_t)pn * 245760 + (size_t)k0 * 12288 + lw * 3072 + lane * 16;
    auto kstep = [&](int p) { return (p / 10) * 10 + (p % 10 + k0) % 10; };
    const u32 dH = lds0 + lw * 2048, dW = lds0 + 8192 + lw * 3072;
    auto issueH = [&](u32 so) {
      glds16d(srcH, dH + so);
      glds16d(srcH + 4 * 4096, dH + so + 1024);
      srcH += (ptrdiff_t)(kstep(kH + 1) - kstep(kH)) * (32 * 4096); ++kH;
    };
    auto issueW = [&](u32 so) {
      glds16d(srcW, dW + so);
      glds16d(srcW + 1024, dW + so + 1024);
      glds16d(srcW + 2048, dW + so + 2048);
      srcW += (ptrdiff_t)(kstep(kW + 1) - kstep(kW)) * 12288; ++kW;
    };
    u32 so = 0;
    auto issue = [&]() { issueH(so); issueW(so); so = so == (G1_NS - 1) * G1_STG ? 0 : so + G1_STG; };
    if (conv_wg) {
      bool seen = false;
      if (wave == 4) seen = poll_once<4>(flags + FL_W, 256, tag, lane);
      WAIT_VM(0);
      if (lane == 0 && __hip_atomic_fetch_add(LDS(u32, lds0 + OFF_TAG + 4), 1u, __ATOMIC_RELAXED, __HIP_MEMORY_SCOPE_WORKGROUP) == 3u)
        set_flag((u32*)(A.ws + WS_FL) + FL_HL + (xcd * 20 + idx - 12) * 32, tag);
      if (wave == 4 && !seen) wait_flags<4>(flags + FL_W, 256, tag, lane);
      BARRIER();
      BARRIER();
      issue(); issue();
      WAIT_VM(5); BARRIER();
      issue(); issue();
      WAIT_VM(10); BARRIER();
      issue(); issue();
      WAIT_VM(15); BARRIER();
      issue(); issue();
      WAIT_VM(20); BARRIER();
      issue();
      WAIT_VM(20); BARRIER();
      issue();
      if (wave == 4) wait_flags<3>(flags + FL_HL, 160, tag, lane);
      WAIT_VM(20); BARRIER();
    } else {
      if (wave == 4) wait_flags<2>(flags + FL_W, 96, tag, lane);
      BARRIER();
#pragma unroll
      for (int s = 0; s < G1_NS; ++s) issueW(s * G1_STG);
      if (wave == 4) wait_flags<3>(flags + FL_HE, 160, tag, lane);
      BARRIER();
      issueH(0); issueH(G1_STG); issueH(2 * G1_STG);
      WAIT_VM(4); BARRIER();
      issueH(3 * G1_STG); issueH(4 * G1_STG);
      WAIT_VM(6); BARRIER();
      issueH(5 * G1_STG); issueH(6 * G1_STG);
      WAIT_VM(8); BARRIER();
      issue(); WAIT_VM(11); BARRIER();
      issue(); WAIT_VM(14); BARRIER();
      issue();
      if (wave == 4) wait_flags<3>(flags + FL_HL, 160, tag, lane);
      WAIT_VM(17); BARRIER();
    }
    for (int i = 5; i < G1_NK - 5; ++i) {
      issue();
      WAIT_VM(20); BARRIER();
    }
    WAIT_VM(15); BARRIER(); WAIT_VM(10); BARRIER(); WAIT_VM(5); BARRIER(); WAIT_VM(0); BARRIER();
}

__device__ __forceinline__ void gemm1_compute(const Args& A, u32 lds0, int lane, int wave) {
  const int xcd = blockIdx.x & 7, idx = blockIdx.x >> 3;
  const int pm = (xcd & 1) * 8 + (idx & 7), pn = (xcd >> 1) * 4 + (idx >> 3);
  const int t0 = pm * 128;
  const int wt = wave & 1, wf = wave >> 1;
  const int g = lane >> 4, q = (lane & 15) >> 2, p = lane & 3;
  const int rot = (pn >> 1) & 3;
  const u32 hrow = (8 * g + q) * 256 + 8 * (p & 1);
  const u32 hx = q | ((g & 1) << 2);
  u32 hc[4];
#pragma unroll
  for (int j = 0; j < 4; ++j) hc[j] = lds0 + hrow + ((((u32)(4 * wt + ((j + rot) & 3)) ^ hx) << 1) | (p >> 1)) * 16;
  u32 wc = lds0 + 8192 + wf * 6144 + lane * 16, so = 0;
  f32x4 acc[6][4];
#pragma unroll
  for (int i = 0; i < 6; ++i)
#pragma unroll
    for (int j = 0; j < 4; ++j) acc[i][j] = (f32x4){0.f, 0.f, 0.f, 0.f};
  f32x4 st1 = {0.f, 0.f, 0.f, 0.f}, st2 = st1;
  bf16x8 hf[2][4], wfr[2][6];
  auto rd = [&](int set) {
    wfr[set][0] = *LDS(bf16x8, wc);
#pragma unroll
    for (int j = 0; j < 4; ++j) hf[set][j] = tr_frag(hc[j], hc[j] + 1024);
#pragma unroll
    for (int i = 1; i < 6; ++i) wfr[set][i] = *LDS(bf16x8, wc + i * 1024);
  };
  auto adv = [&]() {
    u32 dl = so == (G1_NS - 1) * G1_STG ? (u32)(-(G1_NS - 1) * G1_STG) : (u32)G1_STG;
    so += dl;
    asm volatile("" : "+s"(dl));
#pragma unroll
    for (int j = 0; j < 4; ++j) hc[j] += dl;
    wc += dl;
  };
  auto mma = [&](int set) {
#pragma unroll
    for (int i = 0; i < 6; ++i)
#pragma unroll
      for (int j = 0; j < 4; ++j) acc[i][j] = mfma16(wfr[set][i], hf[set][j], acc[i][j]);
  };
  auto stat = [&](int set) {
    st1 = mfma16(ONES_BF16, hf[set][0], st1);
    st2 = mfma16(hf[set][0], hf[set][0], st2);
  };
#define G1_SCHED(extra) do { \
    SG(0x008, 1); SG(0x100, 1); SG(0x008, 1); SG(0x100, 1); SG(0x008, 1); SG(0x100, 1); SG(0x008, 1); SG(0x100, 1); \
    SG(0x008, 1); SG(0x100, 1); SG(0x008, 1); SG(0x100, 1); SG(0x008, 1); SG(0x100, 1); SG(0x008, 1); SG(0x100, 1); \
    SG(0x008, 1); SG(0x100, 1); SG(0x008, 2); SG(0x100, 1); SG(0x008, 2); SG(0x100, 1); SG(0x008, 2); SG(0x100, 1); \
    SG(0x008, 2); SG(0x100, 1); SG(0x008, 1); SG(0x100, 1); SG(0x008, 1); SG(0x002, 1); SG(0x008, 1); SG(0x002, 1); \
    SG(0x008, 1); SG(0x002, 1); SG(0x008, 1); SG(0x002, 1); SG(0x008, 1); SG(0x002, 1); SG(0x008, 1 + (extra)); } while (0)
  BARRIER();
  BARRIER();
  BARRIER();
  rd(0); adv();
#pragma unroll
  for (int i = 1; i < G1_NK - 1; i += 2) {
    BARRIER();
    SB(); rd(1); mma(0); adv();
    if ((i - 1) % 10 < 5) { stat(0); G1_SCHED(2); } else { G1_SCHED(0); }
    SB();
    BARRIER();
    SB(); rd(0); mma(1); adv();
    if (i % 10 < 5) { stat(1); G1_SCHED(2); } else { G1_SCHED(0); }
    SB();
  }
  BARRIER();
  SB(); rd(1); mma(0); G1_SCHED(0); SB();
  WAIT_LGKM0();
  BARRIER();
#pragma unroll
  for (int i = 0; i < 3; ++i)
#pragma unroll
    for (int j = 0; j < 4; ++j) {
      acc[i][j] = mfma16(wfr[1][i], hf[1][j], acc[i][j]);
      acc[3 + i][j] = mfma16(wfr[1][3 + i], hf[1][j], acc[3 + i][j]);
    }
  {
    char* ob = A.ws + WS_HM + ((size_t)(pm * 8 + 4 * wt) * 48 + 3 * pn) * 1024 + (lane & 15) * 16 + (g >> 1) * 256;
#pragma unroll
    for (int j = 0; j < 4; j += 2) {
      char* oj = ob + (size_t)((j + (g & 1) + rot) & 3) * 49152;
#pragma unroll
      for (int i = 0; i < 3; ++i) {
        u32x2 o[2];
#pragma unroll
        for (int jj = 0; jj < 2; ++jj) {
          const f32x4 gv = acc[i][j + jj], uv = acc[3 + i][j + jj];
          o[jj] = (u32x2){pack_bf16(fmaxf(gv[0], 0.f) * uv[0], fmaxf(gv[1], 0.f) * uv[1]), pack_bf16(fmaxf(gv[2], 0.f) * uv[2], fmaxf(gv[3], 0.f) * uv[3])};
        }
        const int E = 6 * wf + 2 * i;
        st16d(oj + (E >> 2) * 1024 + (E & 3) * 256, widen16(o[0], o[1]));
      }
    }
  }
  if (wf == 0 && pn < 8 && g == ((lane & 15) >> 2))
    __hip_atomic_store((gu64*)((u64*)(A.ws + WS_PT) + (size_t)(pn & 1) * L + t0 + (4 * wt + rot) * 16 + (lane & 15)),
                       ((u64)__float_as_uint(diag16(st2, lane)) << 32) | __float_as_uint(st1[0]), RLX_AGENT);
  WAIT_VM(0);
}

__device__ __forceinline__ void gemm2_loader(const Args& A, u32 lds0, int lane, int wave, u32 tag) {
  const int xcd = blockIdx.x & 7, idx = blockIdx.x >> 3;
  const int pm = (xcd & 3) * 8 + (idx & 7), pn = (xcd >> 2) * 4 + (idx >> 3);
  const int t0 = pm * 64, d0 = pn * 80;
  u32* flags = (u32*)(A.ws + WS_FL);
    const int lw = wave - 4;
    const char* srcP = A.ws + WS_HM + (size_t)(pm * 4 + lw) * 49152 + lane * 16;
    const char* srcW = A.ws + WS_WD + (size_t)pn * 245760 + lw * 5120 + lane * 16;
    const u32 dP = lds0 + lw * 4096, dW = lds0 + 16384 + lw * 5120;
    auto issueP = [&](u32 so) {
      glds16d(srcP, dP + so); glds16d(srcP + 1024, dP + so + 1024); glds16d(srcP + 2048, dP + so + 2048); glds16d(srcP + 3072, dP + so + 3072);
      srcP += 4096;
    };
    auto issueW = [&](u32 so) {
      glds16d(srcW, dW + so); glds16d(srcW + 1024, dW + so + 1024); glds16d(srcW + 2048, dW + so + 2048);
      glds16d(srcW + 3072, dW + so + 3072); glds16d(srcW + 4096, dW + so + 4096);
      srcW += 20480;
    };
    BARRIER();
    issueW(0); issueW(G2_STG); issueW(2 * G2_STG); issueW(3 * G2_STG);
#pragma unroll
    for (int k = 0; k < 3; ++k) {
      const int jj = lw + 4 * k < 10 ? lw + 4 * k : lw + 4;
      glds16d(A.ws + WS_HT + (size_t)ht_row(d0 + 8 * jj + (lane >> 3)) * 4096 + t0 * 2 + (lane & 7) * 16, UNI(lds0 + G2_OFF_H + jj * 1024));
    }
    BARRIER();
    if (wave == 4) wait_flags<1>(flags + FL_P + (pm >> 1) * 16 * 32, 16, tag, lane);
    BARRIER();
    issueP(0); issueP(G2_STG);
    WAIT_VM(4); BARRIER();
    issueP(2 * G2_STG); issueP(3 * G2_STG);
    WAIT_VM(8); BARRIER();
    u32 so = 0;
    auto issue = [&]() { issueP(so); issueW(so); so = so == 3 * G2_STG ? 0 : so + G2_STG; };
    issue(); WAIT_VM(13); BARRIER();
    for (int i = 2; i < G2_NK - 3; ++i) {
      issue();
      WAIT_VM(18); BARRIER();
    }
    WAIT_VM(9); BARRIER();
    WAIT_VM(0); BARRIER();
    BARRIER();
    BARRIER();
}

__device__ __forceinline__ void gemm2_compute(const Args& A, u32 lds0, int tid, int lane, int wave, u32 tag) {
  const int xcd = blockIdx.x & 7, idx = blockIdx.x >> 3;
  const int pm = (xcd & 3) * 8 + (idx & 7), pn = (xcd >> 2) * 4 + (idx >> 3);
  const int t0 = pm * 64, d0 = pn * 80;
  u32* flags = (u32*)(A.ws + WS_FL);
  BARRIER();
  if (tid == 0) {
    const int pm1 = (xcd & 1) * 8 + (idx & 7), pn1 = (xcd >> 1) * 4 + (idx >> 3);
    set_flag(flags + FL_P + (pm1 * 16 + pn1) * 32, tag);
  }
  const int wt = wave & 1, wk = wave >> 1;
  u32 pc = lds0 + (wt * 2) * 4096 + wk * 2048 + lane * 16, wc = lds0 + 16384 + wk * 2 * 5120 + lane * 16, so = 0;
  f32x4 acc[5][2];
#pragma unroll
  for (int i = 0; i < 5; ++i) { acc[i][0] = (f32x4){0.f, 0.f, 0.f, 0.f}; acc[i][1] = acc[i][0]; }
  bf16x8 pf[2][2][2], wf[2][2][5];
  auto rd = [&](int set) {
#pragma unroll
    for (int ks = 0; ks < 2; ++ks) {
      wf[set][ks][0] = *LDS(bf16x8, wc + ks * 5120);
#pragma unroll
      for (int j = 0; j < 2; ++j) pf[set][ks][j] = *LDS(bf16x8, pc + j * 4096 + ks * 1024);
#pragma unroll
      for (int i = 1; i < 5; ++i) wf[set][ks][i] = *LDS(bf16x8, wc + ks * 5120 + i * 1024);
    }
  };
  auto adv = [&]() {
    u32 dl = so == 3 * G2_STG ? (u32)(-3 * G2_STG) : (u32)G2_STG;
    so += dl;
    asm volatile("" : "+s"(dl));
    pc += dl; wc += dl;
  };
  auto mma = [&](int set) {
#pragma unroll
    for (int ks = 0; ks < 2; ++ks)
#pragma unroll
      for (int i = 0; i < 5; ++i)
#pragma unroll
        for (int j = 0; j < 2; ++j) acc[i][j] = mfma16(wf[set][ks][i], pf[set][ks][j], acc[i][j]);
  };
#define G2_SCHED() do { _Pragma("unroll") for (int k_ = 0; k_ < 4; ++k_) { SG(0x008, 1); SG(0x100, 1); SG(0x008, 1); SG(0x100, 1); SG(0x008, 1); SG(0x100, 1); SG(0x008, 1); } \
    SG(0x008, 1); SG(0x100, 1); SG(0x008, 1); SG(0x100, 1); SG(0x008, 2); } while (0)
  BARRIER();
  const int tl = wt * 32 + wk * 16;
  u64 pw[2];
#pragma unroll
  for (int k = 0; k < 2; ++k) pw[k] = __hip_atomic_load((gu64*)((u64*)(A.ws + WS_PT) + (size_t)k * L + t0 + tl + (lane & 15)), RLX_AGENT);
  BARRIER();
  rd(0); adv();
  WAIT_LGKM0();
#pragma unroll
  for (int i = 1; i < G2_NK - 1; i += 2) {
    BARRIER();
    SB(); rd(1); mma(0); adv(); G2_SCHED(); SB();
    WAIT_LGKM0();
    BARRIER();
    SB(); rd(0); mma(1); adv(); G2_SCHED(); SB();
    WAIT_LGKM0();
  }
  BARRIER();
  SB(); rd(1); mma(0); G2_SCHED(); SB();
  WAIT_LGKM0();
  mma(1);
  float rr;
  {
    float s1 = 0.f, s2 = 0.f;
#pragma unroll
    for (int k = 0; k < 2; ++k) { s1 += __uint_as_float((u32)pw[k]); s2 += __uint_as_float((u32)(pw[k] >> 32)); }
    const float mean = s1 * (1.f / D);
    const float var = fmaxf(s2 * (1.f / D) - mean * mean, 0.f);
    const float r = __builtin_amdgcn_rcpf(rbf(__builtin_amdgcn_sqrtf(rbf(rbf(var) + 1e-5f))));
    rr = r * r;
  }
  const int g = lane >> 4, q = (lane & 15) >> 2, p = lane & 3;
  s16x4 hv[5];
#pragma unroll
  for (int i = 0; i < 5; ++i) hv[i] = tr_read(lds0 + G2_OFF_H + (16 * i + 4 * g + q) * 128 + (tl + 4 * p) * 2);
  BARRIER();
  const u32 ex = lds0 + (wave ^ 2) * 5120 + lane * 16, exr = lds0 + wave * 5120 + lane * 16;
#pragma unroll
  for (int i = 0; i < 5; ++i) { if (wk) *LDS(f32x4, ex + i * 1024) = acc[i][0]; else *LDS(f32x4, ex + i * 1024) = acc[i][1]; }
  WAIT_LGKM0();
  BARRIER();
  f32x4 m[5];
#pragma unroll
  for (int i = 0; i < 5; ++i) m[i] = *LDS(f32x4, exr + i * 1024);
  SB();
  if (wk) {
#pragma unroll
    for (int i = 0; i < 5; ++i) m[i] += acc[i][1];
    asm volatile("" ::: "memory");
  } else {
#pragma unroll
    for (int i = 0; i < 5; ++i) m[i] += acc[i][0];
  }
#pragma unroll
  for (int i = 0; i < 5; ++i) m[i] *= rr;
  u32x2 o2[5];
#pragma unroll
  for (int i = 0; i < 5; ++i) o2[i] = (u32x2){pack_bf16(m[i][0], m[i][1]), pack_bf16(m[i][2], m[i][3])};
#pragma unroll
  for (int i = 0; i < 5; ++i)
    o2[i] = (u32x2){pack_bf16(bf2f((u16)hv[i][0]) + lo_bf(o2[i][0]), bf2f((u16)hv[i][1]) + hi_bf(o2[i][0])),
                    pack_bf16(bf2f((u16)hv[i][2]) + lo_bf(o2[i][1]), bf2f((u16)hv[i][3]) + hi_bf(o2[i][1]))};
  char* orow = (char*)A.out + (size_t)(t0 + tl + (lane & 15)) * 1280 + d0 * 2;
  *(u32x4*)(orow + (16 * (0 + (g & 1)) + 8 * (g >> 1)) * 2) = widen16(o2[0], o2[1]);
  *(u32x4*)(orow + (16 * (2 + (g & 1)) + 8 * (g >> 1)) * 2) = widen16(o2[2], o2[3]);
  *(u32x2*)(orow + (64 + 4 * g) * 2) = o2[4];
}

__global__ __launch_bounds__(512, 2) void s4_reglu_block(const Args A) {
  extern __shared__ __attribute__((aligned(16))) char smem[];
  const u32 lds0 = (u32)(size_t)(LAS char*)smem;
  const int tid = threadIdx.x, lane = tid & 63;
  const int wave = __builtin_amdgcn_readfirstlane(tid >> 6);
  const int xcd = blockIdx.x & 7, idx = blockIdx.x >> 3;
  asm volatile("" :: "s"(A.x), "s"(A.Are), "s"(A.Aim), "s"(A.Bre), "s"(A.Bim), "s"(A.Cre), "s"(A.Cim), "s"(A.logdt), "s"(A.Dskip),
               "s"(A.Wg), "s"(A.Wu), "s"(A.Wd), "s"(A.out), "s"(A.ws));
  u32* flags = (u32*)(A.ws + WS_FL);
  u32 tag7 = 0;
  if (wave == 7) {
    tag7 = __hip_atomic_load((gu32*)flags, RLX_AGENT) + 1u;
    if (lane == 0) { *LDS(u32, lds0 + OFF_TAG) = tag7; *LDS(u32, lds0 + OFF_TAG + 4) = 0u; set_flag(flags + FL_STARTED + blockIdx.x, tag7); }
  }
  if (idx < 12) {
    int wtask;
    if (idx < 8) {
      const int task = xcd * 8 + idx;
      ln1_task(A, task, lds0, tid, lane, wave);
      wtask = task < 16 ? 32 + task : 48 + task - 16;
    } else {
      wtask = xcd * 4 + (idx - 8);
    }
    weight_task(A, wtask, lds0, tid, lane, wave);
    if (blockIdx.x == 95 && wave == 7) {
      const u64 t0 = __builtin_amdgcn_s_memrealtime();
      for (;;) {
        bool ok = true;
#pragma unroll
        for (int k = 0; k < 4; ++k) ok &= __hip_atomic_load((gu32*)(flags + FL_STARTED + k * 64 + lane), RLX_AGENT) == tag7;
        if (__all(ok)) { if (lane == 0) set_flag(flags, tag7); break; }
        if (__builtin_amdgcn_s_memrealtime() - t0 > SPIN_TICKS) break;
        __builtin_amdgcn_s_sleep(8);
      }
    }
  } else {
    conv_role(A, xcd * 20 + idx - 12, lds0, tid, lane, wave, tag7);
  }
  WAIT_LGKM0();
  BARRIER();
  const u32 tag = UNI(*LDS(u32, lds0 + OFF_TAG));
  if (wave >= 4) {
    gemm1_loader(A, lds0, lane, wave, tag);
    gemm2_loader(A, lds0, lane, wave, tag);
  } else {
    __builtin_amdgcn_s_setprio(2);
    gemm1_compute(A, lds0, lane, wave);
    gemm2_compute(A, lds0, tid, lane, wave, tag);
  }
}

extern "C" void kernel_launch(void* const* d_in, const int* in_sizes, int n_in, void* d_out, int out_size, void* d_ws,
                              size_t ws_size, hipStream_t stream) {
  Args A;
  A.x = (const u16*)d_in[0];
  A.Are = (const u16*)d_in[1]; A.Aim = (const u16*)d_in[2]; A.Bre = (const u16*)d_in[3]; A.Bim = (const u16*)d_in[4];
  A.Cre = (const u16*)d_in[5]; A.Cim = (const u16*)d_in[6]; A.logdt = (const u16*)d_in[7]; A.Dskip = (const u16*)d_in[8];
  A.Wg = (const u16*)d_in[10]; A.Wu = (const u16*)d_in[11]; A.Wd = (const u16*)d_in[12];
  A.out = (u16*)d_out;
  A.ws = (char*)d_ws;
  (void)hipFuncSetAttribute((const void*)s4_reglu_block, hipFuncAttributeMaxDynamicSharedMemorySize, LDS_BYTES);
  s4_reglu_block<<<256, 512, LDS_BYTES, stream>>>(A);
}
```
